```python
import torch

def _softmax(x, axis=-1):
    return torch.softmax(x, dim=axis)
def _gelu(x):
    return torch.nn.functional.gelu(x, approximate="tanh")
def _silu(x):
    return torch.nn.functional.silu(x)
def _sigmoid(x):
    return torch.sigmoid(x)
def _layernorm(x, axis=-1, eps=1e-5):
    m = x.mean(dim=axis, keepdim=True)
    v = x.var(dim=axis, keepdim=True, unbiased=False)
    return (x - m) / torch.sqrt(v + eps)
def _scatter_add(tgt, idx, src, axis=0):
    return tgt.clone().index_add_(axis, idx, src)
def _segment_sum(src, seg, n):
    out = torch.zeros((n,) + src.shape[1:], dtype=src.dtype)
    return out.index_add_(0, seg, src)
def _pad(x, pw):
    flat = tuple(v for pair in reversed(pw) for v in pair)
    return torch.nn.functional.pad(x, flat)

kernel_name = 'parallel_sum__linear_kernel_hedgehog__moe_arch_mod__prompt'

def setup_inputs():

    _w = (lambda a: (lambda t: t.to(torch.bfloat16) if t.is_floating_point() else t)(torch.as_tensor(a)))
    def _A_setup():
        B, S, D, H = 1, 2048, 640, 32
        Dh = D // H
        return {
            "x": _w((lambda *s: torch.randn(*s, dtype=torch.bfloat16) * 0.05)(B, S, D)),
            "wqkv": _w((lambda *s: torch.randn(*s, dtype=torch.bfloat16) * 0.4)(D, 3 * D)),
            "w_phi": _w((lambda *s: torch.randn(*s, dtype=torch.bfloat16) * 0.4)(H, Dh, Dh) * (Dh ** -0.5)),
            "wo": _w((lambda *s: torch.randn(*s, dtype=torch.bfloat16) * 0.1)(D, D)),
        }
    _A_in = _A_setup()

    def _B_setup():
        B, S, D, F, C = 1, 2048, 640, 1536, 1
        return {
            "x": _w((lambda *s: torch.randn(*s, dtype=torch.bfloat16) * 0.05)(B, S, D)),
            "w_r": _w((lambda *s: torch.randn(*s, dtype=torch.bfloat16) * 0.05)(D)),
            "w_gate": _w((lambda *s: torch.randn(*s, dtype=torch.bfloat16) * 0.05)(D, F)),
            "w_up": _w((lambda *s: torch.randn(*s, dtype=torch.bfloat16) * 0.05)(D, F)),
            "w_down": _w((lambda *s: torch.randn(*s, dtype=torch.bfloat16) * 0.05)(F, D)),
        }
    _B_in = _B_setup()

    return {
        "A_x": _A_in["x"],
        "A_wqkv": _A_in["wqkv"],
        "A_w_phi": _A_in["w_phi"],
        "A_wo": _A_in["wo"],
        "B_x": _B_in["x"],
        "B_w_r": _B_in["w_r"],
        "B_w_gate": _B_in["w_gate"],
        "B_w_up": _B_in["w_up"],
        "B_w_down": _B_in["w_down"],
    }

def reference(A_x, A_wqkv, A_w_phi, A_wo, B_x, B_w_r, B_w_gate, B_w_up, B_w_down):
    def _A_ref(x, wqkv, w_phi, wo):
        B, S, D = x.shape
        H = 32; Dh = D // H
        qkv = (lambda x, *d: x.permute(*d))((x @ wqkv).reshape(B, S, 3, H, Dh), 2, 0, 3, 1, 4)
        q, k, v = qkv[0], qkv[1], qkv[2]
        pq = torch.einsum("bhsd,hdf->bhsf", q, w_phi)
        pk = torch.einsum("bhsd,hdf->bhsf", k, w_phi)
        phi_q = torch.concatenate([_softmax(pq, axis=-1), _softmax(-pq, axis=-1)], axis=-1)
        phi_k = torch.concatenate([_softmax(pk, axis=-1), _softmax(-pk, axis=-1)], axis=-1)
        kv = torch.einsum("bhsf,bhsd->bhfd", phi_k, v)
        z  = torch.einsum("bhsf->bhf", phi_k)
        num = torch.einsum("bhqf,bhfd->bhqd", phi_q, kv)
        den = torch.einsum("bhqf,bhf->bhq", phi_q, z)[..., None] + 1e-6
        o = num / den
        return (lambda x, *d: x.permute(*d))(o, 0, 2, 1, 3).reshape(B, S, D) @ wo

    def _B_ref(x, w_r, w_gate, w_up, w_down):
        B, S, D = x.shape
        C = 1
        r = x @ w_r
        idx = torch.argsort(-r, axis=-1)[:, :C]
        idx_e = (lambda x, n, axis: torch.repeat_interleave(x, n, dim=axis))(idx.reshape(B, C, 1), D, axis=2)
        x_sel = (lambda x, i, axis: torch.take_along_dim(x, i, dim=axis))(x, idx_e, axis=1)
        g = _silu(x_sel @ w_gate)
        u = x_sel @ w_up
        y = (g * u) @ w_down
        r_sel = _sigmoid((lambda x, i, axis: torch.take_along_dim(x, i, dim=axis))(r, idx, axis=1))
        y = y * r_sel.reshape(B, C, 1)
        b_off = (torch.arange(B) * S).reshape(B, 1)
        flat_idx = (idx + b_off).reshape(B * C)
        zero = (x * 0.0).reshape(B * S, D)
        upd = _scatter_add(zero, flat_idx, y.reshape(B * C, D), axis=0)
        return x + upd.reshape(B, S, D)

    x = A_x
    xn = _layernorm(x, axis=-1)
    return x + _A_ref(xn, A_wqkv, A_w_phi, A_wo) + _B_ref(xn, B_w_r, B_w_gate, B_w_up, B_w_down)
```

```cpp
#include <hip/hip_runtime.h>
#include <hip/hip_bf16.h>
#include <stdint.h>
typedef unsigned short u16;
__device__ __forceinline__ float bf2f(u16 v){ return __uint_as_float(((unsigned)v)<<16); }
__device__ __forceinline__ u16 f2bf(float f){ __hip_bfloat16 b = __float2bfloat16(f); return *(u16*)&b; }
__device__ __forceinline__ float rb(float f){ return bf2f(f2bf(f)); }
constexpr int S=2048, D=640, H=32, DH=20, F=1536;

__global__ void k_ln(const u16* x, const u16* wr, u16* xn, float* r){
  int s = blockIdx.x; int t = threadIdx.x;
  float v[10]; float sum=0;
  for(int i=0;i<10;i++){ v[i]=bf2f(x[s*D+t+64*i]); sum+=v[i]; }
  for(int o=32;o;o>>=1) sum+=__shfl_xor(sum,o);
  float mean=sum/D; float sq=0;
  for(int i=0;i<10;i++){ float d=v[i]-mean; sq+=d*d; }
  for(int o=32;o;o>>=1) sq+=__shfl_xor(sq,o);
  float var=rb(sq/D); float m=rb(mean);
  float sd=rb(sqrtf(rb(var+1e-5f)));
  float acc=0;
  for(int i=0;i<10;i++){ float d=rb(v[i]-m); float n=rb(d/sd); xn[s*D+t+64*i]=f2bf(n); acc+=n*bf2f(wr[t+64*i]); }
  for(int o=32;o;o>>=1) acc+=__shfl_xor(acc,o);
  if(t==0) r[s]=rb(acc);
}
__global__ void k_gemm(const u16* A, const u16* B, u16* C, int M, int N, int K){
  int n = blockIdx.x*64+threadIdx.x; int m = blockIdx.y;
  if(n>=N) return;
  float acc=0;
  for(int k=0;k<K;k++) acc+=bf2f(A[m*K+k])*bf2f(B[k*N+n]);
  C[m*N+n]=f2bf(acc);
}
__global__ void k_phi(const u16* qkv, const u16* wphi, u16* phi){
  int id = blockIdx.x*256+threadIdx.x;
  int h=id%H, s=(id/H)%S, w=id/(H*S);
  const u16* q = qkv + s*1920 + w*640 + h*DH;
  float p[20];
  for(int f=0;f<20;f++){ float a=0; for(int d=0;d<20;d++) a+=bf2f(q[d])*bf2f(wphi[h*400+d*20+f]); p[f]=rb(a);}
  float mx=p[0], mn=p[0];
  for(int f=1;f<20;f++){ mx=fmaxf(mx,p[f]); mn=fminf(mn,p[f]); }
  float s1=0,s2=0; float e1[20], e2[20];
  for(int f=0;f<20;f++){ e1[f]=__expf(p[f]-mx); e2[f]=__expf(mn-p[f]); s1+=e1[f]; s2+=e2[f]; }
  u16* o = phi + (size_t)id*40;
  for(int f=0;f<20;f++){ o[f]=f2bf(e1[f]/s1); o[20+f]=f2bf(e2[f]/s2); }
}
__global__ void k_kv(const u16* qkv, const u16* phi, float* kv){
  int h=blockIdx.x; int t=threadIdx.x;
  if(t>=840) return;
  int f=t/21, d=t%21;
  const u16* pk = phi + (size_t)S*H*40;
  float a=0;
  for(int s=0;s<S;s++){ float p=bf2f(pk[((size_t)s*H+h)*40+f]); float v = d<20? bf2f(qkv[s*1920+1280+h*20+d]) : 1.f; a+=p*v; }
  kv[h*840+t]=rb(a);
}
__global__ void k_o(const u16* phi, const float* kv, u16* o){
  int id = blockIdx.x*256+threadIdx.x;
  int h=id%H, s=id/H;
  const u16* pq = phi + (size_t)id*40;
  float den=0; for(int f=0;f<40;f++) den+=bf2f(pq[f])*kv[h*840+f*21+20];
  den=rb(rb(den)+1e-6f);
  for(int d=0;d<20;d++){ float a=0; for(int f=0;f<40;f++) a+=bf2f(pq[f])*kv[h*840+f*21+d]; o[s*D+h*20+d]=f2bf(rb(a)/den); }
}
__global__ void k_argmax(const float* r, int* idx){
  __shared__ float sv[256]; __shared__ int si[256];
  int t=threadIdx.x; float b=-INFINITY; int bi=0;
  for(int i=t;i<S;i+=256){ if(r[i]>b){b=r[i];bi=i;} }
  sv[t]=b; si[t]=bi; __syncthreads();
  if(t==0){ for(int i=1;i<256;i++){ if(sv[i]>b || (sv[i]==b && si[i]<bi)){b=sv[i];bi=si[i];} } *idx=bi; }
}
__global__ void k_gu(const u16* xn, const int* idx, const u16* wg, const u16* wu, float* hbuf){
  int j=blockIdx.x*64+threadIdx.x; const u16* xs=xn+(*idx)*D;
  float g=0,u=0; for(int k=0;k<D;k++){ float xv=bf2f(xs[k]); g+=xv*bf2f(wg[k*F+j]); u+=xv*bf2f(wu[k*F+j]); }
  g=rb(g); u=rb(u); g=rb(g/(1.f+__expf(-g))); hbuf[j]=rb(g*u);
}
__global__ void k_down(const float* hbuf, const u16* wd, const float* r, const int* idx, float* y){
  int n=blockIdx.x*64+threadIdx.x; float a=0; for(int j=0;j<F;j++) a+=hbuf[j]*bf2f(wd[j*D+n]);
  float rs=rb(1.f/(1.f+__expf(-r[*idx]))); y[n]=rb(rb(a)*rs);
}
__global__ void k_final(const u16* x, const u16* xn, const u16* oa, const float* y, const int* idx, u16* out){
  int i=blockIdx.x*256+threadIdx.x; int s=i/D, n=i%D;
  float b = bf2f(xn[i]); if(s==*idx) b=rb(b+y[n]);
  out[i]=f2bf(rb(bf2f(x[i])+bf2f(oa[i]))+b);
}
extern "C" void kernel_launch(void* const* d_in, const int* in_sizes, int n_in, void* d_out, int out_size, void* d_ws, size_t ws_size, hipStream_t stream){
  const u16* x=(const u16*)d_in[0]; const u16* wqkv=(const u16*)d_in[1]; const u16* wphi=(const u16*)d_in[2]; const u16* wo=(const u16*)d_in[3];
  const u16* wr=(const u16*)d_in[5]; const u16* wg=(const u16*)d_in[6]; const u16* wu=(const u16*)d_in[7]; const u16* wd=(const u16*)d_in[8];
  char* ws=(char*)d_ws;
  u16* xn=(u16*)ws; ws+=S*D*2;
  u16* qkv=(u16*)ws; ws+=S*1920*2;
  u16* phi=(u16*)ws; ws+=(size_t)2*S*H*40*2;
  u16* o=(u16*)ws; ws+=S*D*2;
  u16* oa=(u16*)ws; ws+=S*D*2;
  float* r=(float*)ws; ws+=S*4;
  float* kv=(float*)ws; ws+=H*840*4;
  float* hb=(float*)ws; ws+=F*4;
  float* y=(float*)ws; ws+=D*4;
  int* idx=(int*)ws;
  k_ln<<<S,64,0,stream>>>(x,wr,xn,r);
  k_gemm<<<dim3(30,S),64,0,stream>>>(xn,wqkv,qkv,S,1920,640);
  k_phi<<<2*S*H/256,256,0,stream>>>(qkv,wphi,phi);
  k_kv<<<H,896,0,stream>>>(qkv,phi,kv);
  k_o<<<S*H/256,256,0,stream>>>(phi,kv,o);
  k_gemm<<<dim3(10,S),64,0,stream>>>(o,wo,oa,S,640,640);
  k_argmax<<<1,256,0,stream>>>(r,idx);
  k_gu<<<F/64,64,0,stream>>>(xn,idx,wg,wu,hb);
  k_down<<<D/64,64,0,stream>>>(hb,wd,r,idx,y);
  k_final<<<S*D/256,256,0,stream>>>(x,xn,oa,y,idx,(u16*)d_out);
}
```

```cpp
#include <hip/hip_runtime.h>
#include <stdint.h>

typedef unsigned short u16;
typedef unsigned int u32;
typedef unsigned long long u64;
typedef __attribute__((ext_vector_type(8))) __bf16 bf16x8;
typedef __attribute__((ext_vector_type(4))) short s16x4;
typedef __attribute__((ext_vector_type(4))) float f32x4;
typedef __attribute__((ext_vector_type(2))) float f32x2;
typedef __attribute__((ext_vector_type(2))) __bf16 bf16x2;
typedef __attribute__((ext_vector_type(4))) u32 u32x4;
typedef __attribute__((ext_vector_type(2))) u32 u32x2;
#define LAS __attribute__((address_space(3)))
#define DI __device__ __forceinline__

constexpr u32 WS_EPOCH = 0;
constexpr u32 WS_FLG = 4096;
constexpr u32 WS_REC = 8192;
constexpr u32 WS_ST = 16384;
constexpr u32 WS_REG = 49152;
constexpr u32 RS = 22528;
constexpr u32 R_OB = 0;
constexpr u32 R_KVP = 10240;
constexpr u32 R_YP = 16960;
constexpr u32 R_GUP = 19520;
constexpr u32 R_KVR = 21568;
constexpr u32 R_END = 21792;
constexpr u32 WS_END = WS_REG + 256 * RS;
constexpr u32 WS_STAMP = 128u << 20;

constexpr u32 L_W1 = 0;
constexpr u32 L_WQ = 0;
constexpr u32 L_WO = 61440;
constexpr u32 L_EX1 = 61440;
constexpr u32 L_CSP = 102400;
constexpr u32 L_EX2 = 110592;
constexpr u32 L_PHI = 122880;
constexpr u32 L_V = 143360;
constexpr u32 L_SM = 155648;
constexpr u32 L_WPHI = L_SM;
constexpr u32 L_ZERO = L_SM + 1920;
constexpr u32 L_KVT = L_SM + 1984;
constexpr u32 L_CSQ = L_KVT + 3360;
constexpr u32 L_KEY = L_CSQ + 768;
constexpr u32 L_IDX = L_KEY + 64;
constexpr u32 L_Y = L_IDX + 64;
constexpr u32 L_TOTAL = 163840;

constexpr u32 SENT = 0xFFFFFFFFu;
constexpr u32 SPIN_CAP = 1u << 15;
#define HOP1_AT 6

DI u32 pk2(float a, float b) { f32x2 v = {a, b}; return __builtin_bit_cast(u32, __builtin_convertvector(v, bf16x2)); }
DI float bflo(u32 u) { return __uint_as_float(u << 16); }
DI float bfhi(u32 u) { return __uint_as_float(u & 0xFFFF0000u); }
DI float rbf(float a) { return bflo(pk2(a, 0.f)); }
DI float rcp(float a) { return __builtin_amdgcn_rcpf(a); }
DI float dot2(u32 a, u32 b, float c) { return __builtin_amdgcn_fdot2_f32_bf16(__builtin_bit_cast(bf16x2, a), __builtin_bit_cast(bf16x2, b), c, false); }
DI f32x4 mfma32(u32x4 a, u32x4 b, f32x4 c) {
  return __builtin_amdgcn_mfma_f32_16x16x32_bf16(__builtin_bit_cast(bf16x8, a), __builtin_bit_cast(bf16x8, b), c, 0, 0, 0);
}
DI f32x4 mfma16(u32x2 a, u32x2 b, f32x4 c) {
  return mfma32(u32x4{a[0], a[1], 0u, 0u}, u32x4{b[0], b[1], 0u, 0u}, c);
}
DI u32x2 trd(LAS char* p) { return __builtin_bit_cast(u32x2, __builtin_amdgcn_ds_read_tr16_b64_v4i16((LAS s16x4*)p)); }
DI u32x4 trd2(LAS char* p, u32 d) { u32x2 a = trd(p), b = trd(p + d); return u32x4{a[0], a[1], b[0], b[1]}; }
DI void glds16(const void* sbase, u32 voff, u32 lds_dst) {
  asm volatile("s_mov_b32 m0, %2\n\ts_nop 0\n\tglobal_load_lds_dwordx4 %0, %1" :: "v"(voff), "s"(sbase), "s"(lds_dst) : "memory");
}
DI u32 umin_(u32 a, u32 b) { return a < b ? a : b; }
DI u32 umax_(u32 a, u32 b) { return a > b ? a : b; }
DI u32 san(u32 v) { return umin_(v, 0xFFFFFFFEu); }
DI u32x4 san4(u32x4 v) { return u32x4{san(v[0]), san(v[1]), san(v[2]), san(v[3])}; }
DI u32 max4(u32x4 v) { return umax_(umax_(v[0], v[1]), umax_(v[2], v[3])); }
DI float xchg16_max(float v) { auto r = __builtin_amdgcn_permlane16_swap(__float_as_uint(v), __float_as_uint(v), false, false); return fmaxf(__uint_as_float(r[0]), __uint_as_float(r[1])); }
DI float xchg16_min(float v) { auto r = __builtin_amdgcn_permlane16_swap(__float_as_uint(v), __float_as_uint(v), false, false); return fminf(__uint_as_float(r[0]), __uint_as_float(r[1])); }
DI float xchg16_add(float v) { auto r = __builtin_amdgcn_permlane16_swap(__float_as_uint(v), __float_as_uint(v), false, false); return __uint_as_float(r[0]) + __uint_as_float(r[1]); }
DI float xchg32_add(float v) { auto r = __builtin_amdgcn_permlane32_swap(__float_as_uint(v), __float_as_uint(v), false, false); return __uint_as_float(r[0]) + __uint_as_float(r[1]); }
template <int CTRL> DI float dpp(float v) { return __int_as_float(__builtin_amdgcn_update_dpp(0, __float_as_int(v), CTRL, 0xf, 0xf, true)); }
template <int CTRL> DI u32 dppu(u32 v) { return (u32)__builtin_amdgcn_update_dpp(0, (int)v, CTRL, 0xf, 0xf, true); }
DI float wave_sum(float v) {
  v += dpp<0xB1>(v); v += dpp<0x4E>(v); v += dpp<0x141>(v); v += dpp<0x140>(v);
  return xchg32_add(xchg16_add(v));
}
DI u32 row_max(u32 v) { v = umax_(v, dppu<0xB1>(v)); v = umax_(v, dppu<0x4E>(v)); v = umax_(v, dppu<0x141>(v)); return umax_(v, dppu<0x140>(v)); }
DI u32x4 ld4w(const char* base, u32 off) { u32x4 v; asm volatile("global_load_dwordx4 %0, %1, %2 sc1\n\ts_waitcnt vmcnt(0)" : "=&v"(v) : "v"(off), "s"(base) : "memory"); return v; }
#define MEMFENCE() asm volatile("" ::: "memory")
#define BARRIER() asm volatile("s_waitcnt lgkmcnt(0)\n\ts_barrier" ::: "memory")
#define SB() __builtin_amdgcn_sched_barrier(0)
#define LD4(off) __builtin_amdgcn_raw_buffer_load_b128(wsr, (off), 0, 16)
#define LD2(off) __builtin_amdgcn_raw_buffer_load_b64(wsr, (off), 0, 16)
#define ST4(v, off) __builtin_amdgcn_raw_buffer_store_b128((v), wsr, (off), 0, 16)
#define ST2(v, off) __builtin_amdgcn_raw_buffer_store_b64((v), wsr, (off), 0, 16)

#define STAMP(k) do { } while (0)

DI void softmax_pack(const f32x4& p0, const f32x4& p1, const f32x4& p2, u32 (&o)[10]) {
  float p[10] = {p0[0], p0[1], p0[2], p0[3], p1[0], p1[1], p1[2], p1[3], p2[0], p2[1]};
  float mx = p[0], mn = p[0];
#pragma unroll
  for (int i = 1; i < 10; i++) { mx = fmaxf(mx, p[i]); mn = fminf(mn, p[i]); }
  mx = xchg16_max(mx); mn = xchg16_min(mn);
  const float L2E = 1.4426950408889634f;
  const float a = -mx * L2E, b = mn * L2E;
  float e1[10], e2[10], s1 = 0.f, s2 = 0.f;
#pragma unroll
  for (int i = 0; i < 10; i++) {
    e1[i] = __builtin_amdgcn_exp2f(fmaf(p[i], L2E, a));
    e2[i] = __builtin_amdgcn_exp2f(fmaf(p[i], -L2E, b));
    s1 += e1[i]; s2 += e2[i];
  }
  s1 = xchg16_add(s1); s2 = xchg16_add(s2);
  const float r1 = rcp(s1), r2 = rcp(s2);
#pragma unroll
  for (int i = 0; i < 5; i++) {
    o[i] = pk2(e1[2 * i] * r1, e1[2 * i + 1] * r1);
    o[5 + i] = pk2(e2[2 * i] * r2, e2[2 * i + 1] * r2);
  }
}

__global__ __launch_bounds__(512, 2) void fused_kernel(
    const u16* __restrict__ x, const u16* __restrict__ wqkv, const u16* __restrict__ wphi, const u16* __restrict__ wo,
    const u16* __restrict__ wr, const u16* __restrict__ wg, const u16* __restrict__ wu, const u16* __restrict__ wd,
    u16* __restrict__ out, char* __restrict__ ws) {
  __shared__ __attribute__((aligned(1024))) char smem_[L_TOTAL];
  LAS char* sm = (LAS char*)smem_;
  const u32 ldsb = (u32)(size_t)smem_;
  STAMP(0);
  const int tid = threadIdx.x, l = tid & 63;
  const int w = __builtin_amdgcn_readfirstlane(tid >> 6);
  const int tg = w & 3, kh = w >> 2, tw = 2 * tg + kh;
  const int g = l >> 4, i16 = l & 15, q4 = i16 >> 2, p4 = l & 3;
  const int b = blockIdx.x, xcd = b & 7, slot = b >> 3;
  const int rt = (xcd >> 1) * 4 + (slot >> 3), hg = (xcd & 1) * 8 + (slot & 7);
  const int wgi = rt * 16 + hg;
  const int rot = (hg + 2 * rt) % 5;
  const auto wsr = __builtin_amdgcn_make_buffer_rsrc((void*)ws, 0, WS_END, 0x00020000);
  const u32 reg = WS_REG + wgi * RS;
#define CROT(c) ((c) + rot >= 5 ? (c) + rot - 5 : (c) + rot)

  const int srow = wgi * 8 + w;
  u32x4 sx4, sw4; u32 sx1, sw1, wpv; u32x2 ep;
  {
    const u32 o16 = l * 16, o4 = l * 4, z = 0;
    asm volatile("global_load_dwordx4 %0, %1, %2" : "=v"(sx4) : "v"(o16), "s"(x + srow * 640));
    asm volatile("global_load_dword %0, %1, %2 offset:1024" : "=v"(sx1) : "v"(o4), "s"(x + srow * 640));
    asm volatile("global_load_dwordx2 %0, %1, %2 sc1" : "=v"(ep) : "v"(z), "s"(ws) : "memory");
    asm volatile("global_load_dwordx4 %0, %1, %2" : "=v"(sw4) : "v"(o16), "s"(wr));
    asm volatile("global_load_dword %0, %1, %2 offset:1024" : "=v"(sw1) : "v"(o4), "s"(wr));
    const u32 o = hg * 1600 + (tid < 400 ? tid : 0) * 4;
    asm volatile("global_load_dword %0, %1, %2" : "=v"(wpv) : "v"(o), "s"(wphi));
  }
  u32x4 xf[20];
  const char* xb0 = (const char*)x + (rt * 128 + tw * 16) * 1280 + kh * 640;
  const char* xb1 = (const char*)x + (rt * 128 + (tw ^ 1) * 16) * 1280 + kh * 640;
  const u32 xoff = (u32)((l >> 2) * 1280 + ((p4 ^ ((l >> 5) << 1)) * 16));
#define XL(i, base, o) asm volatile("global_load_dwordx4 %0, %1, %2 offset:%3" : "=v"(xf[i]) : "v"(xoff), "s"(base), "i"(o))
  u32 woff[3];
#pragma unroll
  for (int k = 0; k < 3; k++) {
    int P = 64 * (3 * (w & 1) + k) + l, rho = P / 10, s = P - 10 * rho;
    int c = 8 * ((rho >> 2) & 3) + 4 * ((rho >> 4) & 1) + (rho & 3);
    woff[k] = c * 3840 + 1280 + (s >= 5 ? 1200 : 0) + hg * 80 + s * 16;
  }
  const u32 wdst = ldsb + L_W1 + (w >> 1) * 5120 + (w & 1) * 3072;
  const char* wbase = (const char*)wqkv + ((w >> 1) & 1) * (320 * 3840) + (w >> 2) * (32 * 3840);
#define XL1(c, k) do { if ((k) == 0) XL(2 * (c), xb0 + CROT(c) * 128, 0); if ((k) == 1) XL(2 * (c) + 1, xb0 + CROT(c) * 128, 64); \
    if ((k) == 2) glds16(wbase + CROT(c) * (64 * 3840), woff[0], wdst + (c) * 20480); \
    if ((k) == 3) glds16(wbase + CROT(c) * (64 * 3840), woff[1], wdst + (c) * 20480 + 1024); \
    if ((k) == 4) XL(10 + 2 * (c), xb1 + CROT(c) * 128, 0); if ((k) == 5) XL(11 + 2 * (c), xb1 + CROT(c) * 128, 64); \
    if ((k) == 6) { if (!(w & 1)) glds16(wbase + CROT(c) * (64 * 3840), woff[2], wdst + (c) * 20480 + 2048); } } while (0)
#define GRP(c) do { _Pragma("unroll") for (int k = 0; k < 7; k++) XL1(c, k); } while (0)
  GRP(0);
  __builtin_amdgcn_s_barrier();
  GRP(1);
  if (tid < 4) *(LAS u32x4*)(sm + L_ZERO + tid * 16) = u32x4{0, 0, 0, 0};
  else if (tid < 84) *(LAS u32*)(sm + L_WPHI + ((tid - 4) >> 1) * 48 + ((tid - 4) & 1) * 24 + 20) = 0u;
  STAMP(1);
  asm volatile("s_waitcnt vmcnt(12)" : "+v"(sx4), "+v"(sx1), "+v"(ep), "+v"(sw4), "+v"(sw1), "+v"(wpv) :: "memory");
  if (tid < 400) { int row = tid / 10, e = tid - 10 * row; *(LAS u32*)(sm + L_WPHI + row * 48 + e * 4 + (e >= 5 ? 4 : 0)) = wpv; }
  u32 tag_lo, tag_hi;
  {
    u64 e = (((u64)ep[1] << 32) | ep[0]) + 1;
    tag_lo = __builtin_amdgcn_readfirstlane((u32)e);
    tag_hi = __builtin_amdgcn_readfirstlane((u32)(e >> 32));
  }
  {
    const u32 xd[5] = {sx4[0], sx4[1], sx4[2], sx4[3], sx1}, wdv[5] = {sw4[0], sw4[1], sw4[2], sw4[3], sw1};
    float s = 0.f, q = 0.f;
#pragma unroll
    for (int k = 0; k < 5; k++) { s = dot2(xd[k], 0x3F803F80u, s); q = dot2(xd[k], xd[k], q); }
    const float mean = wave_sum(s) * (1.f / 640.f);
    const float var = fmaxf(wave_sum(q) * (1.f / 640.f) - mean * mean, 0.f);
    const float mb = rbf(mean), sb = rbf(__builtin_amdgcn_sqrtf(rbf(rbf(var) + 1e-5f))), rsb = rcp(sb);
    float r = 0.f;
#pragma unroll
    for (int k = 0; k < 5; k++) {
      u32 d = pk2(bflo(xd[k]) - mb, bfhi(xd[k]) - mb), n = pk2(bflo(d) * rsb, bfhi(d) * rsb);
      r = dot2(n, wdv[k], r);
    }
    r = wave_sum(r);
    const u32 rb = pk2(r, 0.f) & 0xFFFFu;
    const u32 ord = (rb & 0x8000u) ? (~rb & 0xFFFFu) : (rb | 0x8000u);
    const u32 ms = pk2(mb, sb);
    if (l == 0) {
      ST4((u32x4{ms, tag_lo, ms, tag_hi}), WS_ST + srow * 16);
      *(LAS u32x2*)(sm + L_KEY + w * 8) = u32x2{(ord << 16) | (u32)(0x7FFF - srow), ms};
    }
  }
  GRP(2);
  __builtin_amdgcn_s_barrier();
  GRP(3);

  f32x4 acc[2][5], cs[3];
#pragma unroll
  for (int t = 0; t < 5; t++) { acc[0][t] = f32x4{0, 0, 0, 0}; acc[1][t] = f32x4{0, 0, 0, 0}; }
#pragma unroll
  for (int t = 0; t < 3; t++) cs[t] = f32x4{0, 0, 0, 0};
  u32x4 ONES = {0x3F803F80u, 0x3F803F80u, 0x3F803F80u, 0x3F803F80u};
  asm volatile("" : "+v"(ONES));
  LAS char* w1p = sm + L_W1 + kh * 5120 + (4 * g + q4) * 160 + 8 * p4;
  LAS char* xs_w = sm + L_PHI + tw * 2560 + l * 16;
  LAS char* xs_r = sm + L_PHI + tw * 2560 + (4 * i16 + (g ^ ((i16 >> 3) << 1))) * 16;

#define XT(i) do { *(LAS u32x4*)xs_w = xf[i]; __builtin_amdgcn_wave_barrier(); xf[i] = *(LAS u32x4*)xs_r; __builtin_amdgcn_wave_barrier(); } while (0)
#define FR(F, t) (u32x4{F[2 * (t)][0], F[2 * (t)][1], F[2 * (t) + 1][0], F[2 * (t) + 1][1]})
#define RD(F, j, r) F[r] = trd(w1p + (j) * 10240 + 32 * ((r) >> 1) + 2560 * ((r) & 1))
#define MF(F, j, k) acc[(k) & 1][(k) >> 1] = mfma32(FR(F, (k) >> 1), xf[10 * ((k) & 1) + (j)], acc[(k) & 1][(k) >> 1])
#define CSUM(F, j) do { if (tg == 2 * ((j) & 1)) { cs[0] = mfma32(ONES, FR(F, 0), cs[0]); cs[1] = mfma32(ONES, FR(F, 1), cs[1]); cs[2] = mfma32(ONES, FR(F, 2), cs[2]); } \
    if (tg == 2 * ((j) & 1) + 1) { cs[0] = mfma32(ONES, FR(F, 3), cs[0]); cs[1] = mfma32(ONES, FR(F, 4), cs[1]); } SB(); } while (0)
#define QL1(m, k) do { const int s_ = 8 * (m) + w, c_ = s_ >> 2; \
    glds16((const char*)wqkv + ((s_ & 1) * 320 + CROT(c_) * 64 + 32 * ((s_ >> 1) & 1)) * 3840, qoff[k], ldsb + L_WQ + s_ * 3072 + (k) * 1024); } while (0)
#define STEPA(FA, FB, j, V) do { _Pragma("unroll") for (int k = 0; k < 10; k++) { MF(FA, j, k); RD(FB, (j) + 1, k); V(k); SB(); } CSUM(FA, j); } while (0)
#define STEPB(FA, FB, j, N, c, V) do { MF(FB, j, 0); MF(FB, j, 1); SB(); \
    asm volatile("s_waitcnt vmcnt(" #N ")" : "+v"(xf[2 * (c)]), "+v"(xf[2 * (c) + 1]), "+v"(xf[10 + 2 * (c)]), "+v"(xf[11 + 2 * (c)]) :: "memory"); \
    XT(2 * (c)); XT(10 + 2 * (c)); MF(FB, j, 2); SB(); XT(2 * (c) + 1); XT(11 + 2 * (c)); MF(FB, j, 3); SB(); \
    asm volatile("s_waitcnt lgkmcnt(0)\n\ts_barrier" ::: "memory"); \
    _Pragma("unroll") for (int k = 4; k < 9; k++) { RD(FA, (j) + 1, 2 * (k - 4)); RD(FA, (j) + 1, 2 * (k - 4) + 1); MF(FB, j, k); V(10 + k); SB(); } MF(FB, j, 9); CSUM(FB, j); } while (0)

  u32 qoff[3];
  u32x4 srec, gw0, gw1;
  const u32 sra = l < 32 ? WS_REC + (32 * w + l) * 16 : WS_ST + (rt * 128 + tw * 16 + i16) * 16;
  const int cb = wgi < 240 ? wgi / 5 : 0, ksp = wgi < 240 ? wgi - 5 * cb : 0;
  {
    u32x2 fa[10], fb[10];
    asm volatile("s_waitcnt vmcnt(18)" : "+v"(xf[0]), "+v"(xf[1]), "+v"(xf[10]), "+v"(xf[11]) :: "memory");
    XT(0); XT(10); XT(1); XT(11);
    BARRIER();
    STAMP(2);
#pragma unroll
    for (int r = 0; r < 10; r++) RD(fa, 0, r);
    if (w == 0) {
      u32x2 kk = *(LAS u32x2*)(sm + L_KEY + (l & 7) * 8);
      u32 km = row_max(kk[0]);
      if (l < 8 && kk[0] == km) ST4((u32x4{kk[0], tag_lo, kk[1], tag_hi}), WS_REC + b * 16);
    }
    SB();
#define V4(k) do { if ((k) == 0) XL1(4, 0); if ((k) == 2) XL1(4, 1); if ((k) == 5) XL1(4, 2); if ((k) == 8) XL1(4, 3); if ((k) == 14) XL1(4, 4); if ((k) == 16) XL1(4, 5); if ((k) == 18) XL1(4, 6); } while (0)
#define VC(k) do { const u32x4 S4 = {SENT, SENT, SENT, SENT}; if ((k) == 2) ST4(S4, reg + tid * 16); if ((k) == 8) ST4(S4, reg + 8192 + tid * 16); \
      if ((k) == 16) { if (tid < 338) ST4(S4, reg + 16384 + tid * 16); } } while (0)
#define VQ0(k) do { if ((k) == 1) QL1(0, 0); if ((k) == 5) QL1(0, 1); if ((k) == 9) QL1(0, 2); if ((k) == 15) QL1(1, 0); if ((k) == 18) QL1(1, 1); } while (0)
#define VQ1(k) do { if ((k) == 0) asm volatile("global_load_dwordx4 %0, %1, %2 sc1" : "=v"(srec) : "v"(sra), "s"(ws) : "memory"); \
      if ((k) == 3) QL1(1, 2); if (w < 4) { if ((k) == 7) QL1(2, 0); if ((k) == 15) QL1(2, 1); if ((k) == 18) QL1(2, 2); } } while (0)
#define VQ2(k) do { if ((k) == 2) { const u16* gm = cb < 24 ? wg + cb * 64 : wu + (cb - 24) * 64; const u32 go = (128 * ksp + 16 * w + (l >> 3)) * 3072 + (l & 7) * 16; \
        asm volatile("global_load_dwordx4 %0, %1, %2" : "=v"(gw0) : "v"(go), "s"(gm)); \
        asm volatile("global_load_dwordx4 %0, %1, %2" : "=v"(gw1) : "v"(go), "s"(gm + 8 * 1536)); } } while (0)
    STEPA(fa, fb, 0, V4);
    STEPB(fa, fb, 1, 16, 1, V4);
    STAMP(3);
    STEPA(fa, fb, 2, VC);
    STEPB(fa, fb, 3, 14, 2, VC);
    STAMP(4);
#pragma unroll
    for (int k = 0; k < 3; k++) {
      int P = 64 * k + l, rho = P / 6, s = P - 6 * rho; if (s > 4) s = 4;
      qoff[k] = (8 * ((rho >> 2) & 3) + 4 * ((rho >> 4) & 1) + (rho & 3)) * 3840 + hg * 80 + s * 16;
    }
    STEPA(fa, fb, 4, VQ0);
    STEPB(fa, fb, 5, 11, 3, VQ0);
    STAMP(5);
    STEPA(fa, fb, 6, VQ1);
    STEPB(fa, fb, 7, 7, 4, VQ1);
    STAMP(6);
    if (tid == 0) ST2((u32x2{tag_lo, tag_hi}), WS_FLG + b * 8);
    MEMFENCE();
    STEPA(fa, fb, 8, VQ2);
#pragma unroll
    for (int k = 0; k < 10; k++) MF(fb, 9, k);
    CSUM(fb, 9);
  }
  STAMP(7);
  asm volatile("s_waitcnt vmcnt(3)" : "+v"(srec) :: "memory");
#pragma unroll 1
  for (u32 spin = 0; spin < SPIN_CAP; spin++) {
    if (__all(srec[1] == tag_lo && srec[3] == tag_hi && (l < 32 || srec[0] == srec[2]))) break;
    srec = ld4w(ws, sra);
  }
  float m_b, rs, mrs;
  {
    u32 km = row_max(srec[0]);
    { auto r = __builtin_amdgcn_permlane16_swap(km, km, false, false); km = umax_(r[0], r[1]); }
    if (l < 32 && srec[0] == km) *(LAS u32x2*)(sm + L_IDX + w * 8) = u32x2{km, srec[2]};
    auto r = __builtin_amdgcn_permlane32_swap(srec[0], srec[0], false, false);
    m_b = bflo(r[1]); rs = rcp(bfhi(r[1])); mrs = m_b * rs;
  }
  {
    LAS char* cp = sm + L_CSP + (((tg >> 1) + 2 * kh) * 80 + 48 * (tg & 1) + i16) * 4;
    *(LAS float*)(cp) = cs[0][0]; *(LAS float*)(cp + 64) = cs[1][0];
    if (!(tg & 1)) *(LAS float*)(cp + 128) = cs[2][0];
    const int tp = tw ^ 1;
    *(LAS f32x4*)(sm + L_PHI + tp * 2560 + 1024 + l * 16) = acc[1][0];
    *(LAS f32x4*)(sm + L_V + tp * 1536 + l * 16) = acc[1][1];
    *(LAS f32x4*)(sm + L_EX1 + tp * 2048 + l * 16) = acc[1][2];
    *(LAS f32x4*)(sm + L_EX1 + tp * 2048 + 1024 + l * 16) = acc[1][3];
    *(LAS f32x4*)(sm + L_EX2 + tp * 1024 + l * 16) = acc[1][4];
  }
  u32x4 wa32[3]; u32x2 wa16[3];
  {
    const int hp = p4 >> 1;
    const int d0 = 4 * g + q4, d1 = d0 + 16, d2 = d0 + 32;
    LAS char* a0 = sm + (hp == 0 ? L_WPHI + d0 * 48 + 24 * (p4 & 1) : L_ZERO);
    LAS char* a1 = sm + ((d1 >= 20) == (hp == 1) ? L_WPHI + d1 * 48 + 24 * (p4 & 1) : L_ZERO);
    LAS char* a2 = sm + ((hp == 1 && d2 < 40) ? L_WPHI + d2 * 48 + 24 * (p4 & 1) : L_ZERO);
#pragma unroll
    for (int mt = 0; mt < 3; mt++) {
      u32x2 e0 = trd(a0 + 8 * mt), e1 = trd(a1 + 8 * mt);
      wa32[mt] = u32x4{e0[0], e0[1], e1[0], e1[1]};
      wa16[mt] = trd(a2 + 8 * mt);
    }
  }
  BARRIER();
  STAMP(8);
  f32x4 c0, c1, a[5];
  c0 = *(LAS f32x4*)(sm + L_CSP + (p4 * 80 + 16 * q4 + 4 * g) * 4);
  c1 = *(LAS f32x4*)(sm + L_CSP + (p4 * 80 + 64 + 4 * g) * 4);
  a[0] = acc[0][0] + *(LAS f32x4*)(sm + L_PHI + tw * 2560 + 1024 + l * 16);
  a[1] = acc[0][1] + *(LAS f32x4*)(sm + L_V + tw * 1536 + l * 16);
  a[2] = acc[0][2] + *(LAS f32x4*)(sm + L_EX1 + tw * 2048 + l * 16);
  a[3] = acc[0][3] + *(LAS f32x4*)(sm + L_EX1 + tw * 2048 + 1024 + l * 16);
  a[4] = acc[0][4] + *(LAS f32x4*)(sm + L_EX2 + tw * 1024 + l * 16);
#pragma unroll
  for (int r = 0; r < 4; r++) { c0[r] += dpp<0xB1>(c0[r]); c0[r] += dpp<0x4E>(c0[r]); c1[r] += dpp<0xB1>(c1[r]); c1[r] += dpp<0x4E>(c1[r]); }
  {
    u32 kn[5][2];
#define NRM(A, t, C, LN, O) do { \
      float v0 = A[0] * rs - mrs * dpp<0x150 + LN>(C[0]), v1 = A[1] * rs - mrs * dpp<0x150 + LN>(C[1]); \
      float v2 = A[2] * rs - mrs * dpp<0x150 + LN>(C[2]), v3 = A[3] * rs - mrs * dpp<0x150 + LN>(C[3]); \
      O[t][0] = pk2(v0, v1); O[t][1] = pk2(v2, v3); } while (0)
    NRM(a[0], 0, c0, 0, kn); NRM(a[1], 1, c0, 4, kn); NRM(a[2], 2, c0, 8, kn);
    u32x4 b32 = {kn[0][0], kn[0][1], kn[1][0], kn[1][1]};
    u32x2 b16 = {kn[2][0], kn[2][1]};
    f32x4 pp[3];
#pragma unroll
    for (int mt = 0; mt < 3; mt++) {
      pp[mt] = mfma32(wa32[mt], b32, f32x4{0, 0, 0, 0});
      pp[mt] = mfma16(wa16[mt], b16, pp[mt]);
    }
    NRM(a[3], 3, c0, 12, kn);
    kn[4][0] = pk2(a[4][0] * rs - mrs * c1[0], a[4][1] * rs - mrs * c1[1]); kn[4][1] = pk2(a[4][2] * rs - mrs * c1[2], a[4][3] * rs - mrs * c1[3]);
    __builtin_amdgcn_wave_barrier();
    LAS char* vr = sm + L_V + (16 * tw + i16) * 96;
    u32x2 v2 = (g >= 2) ? u32x2{kn[2][0], kn[2][1]} : u32x2{g == 0 ? 0x3F80u : 0u, 0u};
    *(LAS u32x2*)(vr + (g >= 2 ? 8 * (g - 2) : 80 + 8 * g)) = v2;
    *(LAS u32x2*)(vr + 16 + 8 * g) = u32x2{kn[3][0], kn[3][1]};
    *(LAS u32x2*)(vr + 48 + 8 * g) = u32x2{kn[4][0], kn[4][1]};
    u32 ph[10];
    softmax_pack(pp[0], pp[1], pp[2], ph);
    LAS char* pr = sm + L_PHI + (16 * tw + i16) * 160 + 40 * g;
#pragma unroll
    for (int k = 0; k < 5; k++) *(LAS u32x2*)(pr + 8 * k) = u32x2{ph[2 * k], ph[2 * k + 1]};
  }
  STAMP(9);
  asm volatile("s_waitcnt vmcnt(0) lgkmcnt(0)\n\ts_barrier" : "+v"(gw0), "+v"(gw1) :: "memory");
  STAMP(10);
  {
    const int i1 = w < 5 ? w + 8 : w;
    const int mt0 = w / 3, nt0 = w - 3 * mt0, mt1 = i1 < 9 ? i1 / 3 : 3 + ((i1 - 9) >> 1), nt1 = i1 < 9 ? i1 % 3 : 1 + ((i1 - 9) & 1);
    LAS char* ap = sm + L_PHI + (4 * g + q4) * 160 + 8 * p4;
    LAS char* bp = sm + L_V + (4 * g + q4) * 96 + 8 * p4;
    f32x4 d0 = {0, 0, 0, 0}, d1 = {0, 0, 0, 0};
#pragma unroll
    for (int u = 0; u < 4; u++) {
      d0 = mfma32(trd2(ap + 32 * mt0 + u * 5120, 2560), trd2(bp + 32 * nt0 + u * 3072, 1536), d0);
      d1 = mfma32(trd2(ap + 32 * mt1 + u * 5120, 2560), trd2(bp + 32 * nt1 + u * 3072, 1536), d1);
    }
#define KVST(d, mt, nt, en) do { const int fq = 4 * (mt) + g, col = 16 * (nt) + i16; int c = col - (fq >= 10 ? 20 : 0); bool ok = (c >= 0 && c < 20); \
      if (col == 40) { c = 20; ok = true; } ST4(san4(__builtin_bit_cast(u32x4, d)), (ok && (en)) ? reg + R_KVP + (fq * 21 + c) * 16 : WS_END); } while (0)
    KVST(d0, mt0, nt0, true); KVST(d1, mt1, nt1, w < 5);
  }
  MEMFENCE();
  STAMP(11);
  u32x2 rec;
  {
    u32x2 kk = *(LAS u32x2*)(sm + L_IDX + (l & 7) * 8);
    u32 km = row_max(kk[0]);
    u32 ms = row_max(kk[0] == km ? kk[1] : 0u);
    rec = u32x2{(u32)__builtin_amdgcn_readfirstlane(km), (u32)__builtin_amdgcn_readfirstlane(ms)};
  }
  const int idx = (0x7FFF - (rec[0] & 0xFFFFu)) & 2047;
  if (b == 0 && tid == 0) ST2((u32x2{tag_lo, tag_hi}), WS_EPOCH);
  u32 xs0, xs1;
  u32x4 fl;
  u32x2 dw[6];
  {
    const u32 xo = (128 * ksp + 16 * w + (l >> 3)) * 2;
    asm volatile("global_load_ushort %0, %1, %2" : "=v"(xs0) : "v"(xo), "s"(x + idx * 640));
    asm volatile("global_load_ushort %0, %1, %2 offset:16" : "=v"(xs1) : "v"(xo), "s"(x + idx * 640));
    const u32 fo = WS_FLG + w * 256 + i16 * 16;
    asm volatile("global_load_dwordx4 %0, %1, %2 sc1" : "=v"(fl) : "v"(fo), "s"(ws) : "memory");
    const u16* dp = wd + (6 * wgi) * 640;
    const u32 dof = (tid < 160 ? tid : 0) * 8;
    asm volatile("global_load_dwordx2 %0, %1, %2" : "=v"(dw[0]) : "v"(dof), "s"(dp));
    asm volatile("global_load_dwordx2 %0, %1, %2 offset:1280" : "=v"(dw[1]) : "v"(dof), "s"(dp));
    asm volatile("global_load_dwordx2 %0, %1, %2 offset:2560" : "=v"(dw[2]) : "v"(dof), "s"(dp));
    asm volatile("global_load_dwordx2 %0, %1, %2" : "=v"(dw[3]) : "v"(dof), "s"(dp + 1920));
    asm volatile("global_load_dwordx2 %0, %1, %2 offset:1280" : "=v"(dw[4]) : "v"(dof), "s"(dp + 1920));
    asm volatile("global_load_dwordx2 %0, %1, %2 offset:2560" : "=v"(dw[5]) : "v"(dof), "s"(dp + 1920));
  }
  u32 ooff[3];
#pragma unroll
  for (int k = 0; k < 3; k++) {
    int P = 64 * k + l, rho = P / 6, s = P - 6 * rho; if (s > 4) s = 4;
    ooff[k] = (8 * ((rho >> 2) & 3) + 4 * ((rho >> 4) & 1) + (rho & 3)) * 1280 + hg * 80 + s * 16;
  }
#define OL1(n) do { if ((n) < 6 || w < 4) glds16((const char*)wo + (8 * ((n) / 3) + w) * 40960, ooff[(n) % 3], ldsb + L_WO + (8 * ((n) / 3) + w) * 3072 + ((n) % 3) * 1024); } while (0)
  const int he = l >= 28 ? l - 28 : l;
  const bool h1ok = (l < 56) && (rt < 15);
  const u32 h1a = h1ok ? WS_REG + ((l >= 28 ? 128 : 0) + hg) * RS + R_KVP + (28 * rt + he) * 16 : WS_END;
  u32x4 h1[8];
  f32x4 aq[2][3], cq[2];
#pragma unroll
  for (int t = 0; t < 3; t++) { aq[0][t] = f32x4{0, 0, 0, 0}; aq[1][t] = f32x4{0, 0, 0, 0}; }
  cq[0] = f32x4{0, 0, 0, 0}; cq[1] = f32x4{0, 0, 0, 0};
  {
    LAS char* wqp = sm + L_WQ + kh * 3072 + (4 * g + q4) * 96 + 8 * p4;
    u32x2 fa[6], fb[6];
#define RQ(F, j, r) F[r] = trd(wqp + (j) * 6144 + 32 * ((r) >> 1) + 1536 * ((r) & 1))
#define STEPQ(FA, FB, j) do { _Pragma("unroll") for (int k = 0; k < 6; k++) { aq[k & 1][k >> 1] = mfma32(FR(FA, k >> 1), xf[10 * (k & 1) + (j)], aq[k & 1][k >> 1]); \
        if ((j) < 9) RQ(FB, (j) + 1, k); if (k == 3 && (j) < 9) OL1(j); SB(); } \
      if (tg == 2 * ((j) & 1)) { cq[0] = mfma32(ONES, FR(FA, 0), cq[0]); cq[1] = mfma32(ONES, FR(FA, 1), cq[1]); } \
      if (tg == 2 * ((j) & 1) + 1) { cq[0] = mfma32(ONES, FR(FA, 2), cq[0]); } SB(); } while (0)
#pragma unroll
    for (int r = 0; r < 6; r++) RQ(fa, 0, r);
    SB();
#pragma unroll
    for (int j = 0; j < 10; j += 2) {
      STEPQ(fa, fb, j); STEPQ(fb, fa, j + 1);
      if (j == 2) {
        asm volatile("s_waitcnt vmcnt(10)" : "+v"(xs0), "+v"(xs1), "+v"(fl) :: "memory");
        const float sel_m = bflo(rec[1]), sel_r = rcp(bfhi(rec[1]));
        float a0 = rbf(rbf(bflo(xs0) - sel_m) * sel_r), a1 = rbf(rbf(bflo(xs1) - sel_m) * sel_r);
        float v[8];
#pragma unroll
        for (int k = 0; k < 4; k++) {
          v[2 * k] = a0 * bflo(gw0[k]) + a1 * bflo(gw1[k]);
          v[2 * k + 1] = a0 * bfhi(gw0[k]) + a1 * bfhi(gw1[k]);
        }
        float u4[4], u2[2];
#pragma unroll
        for (int k = 0; k < 4; k++) {
          auto r = __builtin_amdgcn_permlane32_swap(__float_as_uint(v[k]), __float_as_uint(v[k + 4]), false, false);
          u4[k] = __uint_as_float(r[0]) + __uint_as_float(r[1]);
        }
#pragma unroll
        for (int k = 0; k < 2; k++) {
          auto r = __builtin_amdgcn_permlane16_swap(__float_as_uint(u4[k]), __float_as_uint(u4[k + 2]), false, false);
          u2[k] = __uint_as_float(r[0]) + __uint_as_float(r[1]);
        }
        u2[0] += dpp<0x128>(u2[0]); u2[1] += dpp<0x128>(u2[1]);
        ST2((u32x2{san(__float_as_uint(u2[0])), san(__float_as_uint(u2[1]))}),
            ((l & 8) == 0 && wgi < 240) ? reg + R_GUP + w * 256 + (l & 7) * 32 + (l >> 5) * 16 + ((l >> 4) & 1) * 8 : WS_END);
#pragma unroll 1
        for (u32 spin = 0; spin < SPIN_CAP; spin++) {
          if (__all(fl[0] == tag_lo && fl[1] == tag_hi && fl[2] == tag_lo && fl[3] == tag_hi)) break;
          fl = ld4w(ws, WS_FLG + w * 256 + i16 * 16);
        }
        asm volatile("s_barrier" ::: "memory");
        SB();
      }
      if (j == HOP1_AT && w == 7) {
#pragma unroll
        for (int k = 0; k < 8; k++) h1[k] = LD4(h1a + k * 16 * RS);
      }
    }
  }
  MEMFENCE();
  {
    LAS char* cp = sm + L_CSQ + (((tg >> 1) + 2 * kh) * 48 + 32 * (tg & 1) + i16) * 4;
    *(LAS float*)(cp) = cq[0][0];
    if (!(tg & 1)) *(LAS float*)(cp + 64) = cq[1][0];
#pragma unroll
    for (int t = 0; t < 3; t++) *(LAS f32x4*)(sm + L_PHI + (tw ^ 1) * 3072 + t * 1024 + l * 16) = aq[1][t];
  }
  STAMP(12);
  if (w == 7) {
#pragma unroll 1
    for (u32 spin = 0; spin < SPIN_CAP; spin++) {
      u32 m = 0;
#pragma unroll
      for (int k = 0; k < 8; k++) m = umax_(m, max4(h1[k]));
      if (__all(m != SENT)) break;
      MEMFENCE();
#pragma unroll
      for (int k = 0; k < 8; k++) h1[k] = LD4(h1a + k * 16 * RS);
    }
    f32x4 s = __builtin_bit_cast(f32x4, h1[0]);
#pragma unroll
    for (int k = 1; k < 8; k++) s += __builtin_bit_cast(f32x4, h1[k]);
#pragma unroll
    for (int r = 0; r < 4; r++) s[r] += __shfl(s[r], (l + 28) & 63);
    if (h1ok && l < 28) ST2((u32x2{san(pk2(s[0], s[1])), san(pk2(s[2], s[3]))}), reg + R_KVR + l * 8);
  }
  u32 ga[4];
  u32x2 gp_[4];
  if (w < 3) {
#pragma unroll
    for (int k = 0; k < 4; k++) {
      int u = l + 64 * k, pc = u / 3, pr = u - 3 * pc;
      int mat = pc >= 40 ? 1 : 0, kw = pc - 40 * mat, col = 6 * wgi + 2 * pr;
      ga[k] = u < 240 ? WS_REG + ((mat * 24 + (col >> 6)) * 5 + (kw >> 3)) * RS + R_GUP + (kw & 7) * 256 + (col & 63) * 4 : WS_END;
      gp_[k] = LD2(ga[k]);
    }
  }
  BARRIER();
  STAMP(13);
  const u32 slo0 = (rt * 128 + tw * 16 + l / 5) * 1280 + hg * 80 + (l % 5) * 16;
  const u32 slo1 = (rt * 128 + tw * 16 + (64 + i16) / 5) * 1280 + hg * 80 + ((64 + i16) % 5) * 16;
  glds16(x, slo0, ldsb + w * 2048);
  if (l < 16) glds16(x, slo1, ldsb + w * 2048 + 1024);
  u32 pq[10];
  {
    f32x4 c2 = *(LAS f32x4*)(sm + L_CSQ + (p4 * 48 + 16 * (q4 < 3 ? q4 : 0) + 4 * g) * 4);
    f32x4 aqo[3];
#pragma unroll
    for (int t = 0; t < 3; t++) aqo[t] = aq[0][t] + *(LAS f32x4*)(sm + L_PHI + tw * 3072 + t * 1024 + l * 16);
#pragma unroll
    for (int r = 0; r < 4; r++) { c2[r] += dpp<0xB1>(c2[r]); c2[r] += dpp<0x4E>(c2[r]); }
    u32 qn[3][2];
    NRM(aqo[0], 0, c2, 0, qn); NRM(aqo[1], 1, c2, 4, qn); NRM(aqo[2], 2, c2, 8, qn);
    u32x4 b32 = {qn[0][0], qn[0][1], qn[1][0], qn[1][1]};
    u32x2 b16 = {qn[2][0], qn[2][1]};
    f32x4 pp[3];
#pragma unroll
    for (int mt = 0; mt < 3; mt++) {
      pp[mt] = mfma32(wa32[mt], b32, f32x4{0, 0, 0, 0});
      pp[mt] = mfma16(wa16[mt], b16, pp[mt]);
    }
    softmax_pack(pp[0], pp[1], pp[2], pq);
#pragma unroll
    for (int k = 0; k < 10; k++) asm volatile("" : "+v"(pq[k]));
  }
  STAMP(14);
  if (w < 3) {
#pragma unroll 1
    for (u32 spin = 0; spin < SPIN_CAP; spin++) {
      u32 m = 0;
#pragma unroll
      for (int k = 0; k < 4; k++) m = umax_(m, umax_(gp_[k][0], gp_[k][1]));
      if (__all(m != SENT)) break;
      MEMFENCE();
#pragma unroll
      for (int k = 0; k < 4; k++) gp_[k] = LD2(ga[k]);
    }
    LAS char* ps = sm + 16384 + w * 2048;
#pragma unroll
    for (int k = 0; k < 4; k++) *(LAS u32x2*)(ps + (l + 64 * k) * 8) = gp_[k];
    __builtin_amdgcn_wave_barrier();
    float sacc = 0.f;
    const int vi = l % 12;
    {
      int qt = l / 12; if (qt > 4) qt = 4;
      LAS char* rp = ps + ((vi >= 6 ? 234 : 0) + qt * 48 + vi) * 4;
#pragma unroll
      for (int k = 0; k < 8; k++) sacc += *(LAS float*)(rp + k * 24);
    }
    float tot = __shfl(sacc, vi);
#pragma unroll
    for (int k = 1; k < 5; k++) tot += __shfl(sacc, vi + 12 * k);
    float gg = rbf(tot);
    float uu = __shfl(gg, (l + 6) & 63);
    float sg = rbf(gg * rcp(1.f + __expf(-gg)));
    float hv = rbf(sg * uu);
    f32x4 y = {0, 0, 0, 0};
    asm volatile("s_waitcnt vmcnt(2)" : "+v"(dw[0]), "+v"(dw[1]), "+v"(dw[2]), "+v"(dw[3]), "+v"(dw[4]), "+v"(dw[5]) :: "memory");
#pragma unroll
    for (int i = 0; i < 6; i++) {
      float h = __int_as_float(__builtin_amdgcn_readlane(__float_as_int(hv), i));
      y[0] += h * bflo(dw[i][0]); y[1] += h * bfhi(dw[i][0]); y[2] += h * bflo(dw[i][1]); y[3] += h * bfhi(dw[i][1]);
    }
    if (tid < 160) ST4(san4(__builtin_bit_cast(u32x4, y)), reg + R_YP + tid * 16);
  } else if (w < 7) {
    const int t2 = tid - 192, hpr = t2 / 14;
    const u32 h2a = t2 < 210 ? WS_REG + (hpr * 16 + hg) * RS + R_KVR + (t2 - 14 * hpr) * 16 : WS_END;
    u32x4 kv;
#pragma unroll 1
    for (u32 spin = 0; spin < SPIN_CAP; spin++) {
      MEMFENCE();
      kv = LD4(h2a);
      if (__all(max4(kv) != SENT)) break;
    }
    if (t2 < 210) {
#pragma unroll
      for (int k = 0; k < 2; k++) {
        int it = 2 * t2 + k, hf = it / 21, c = it - 21 * hf;
        *(LAS u32x2*)(sm + L_KVT + c * 80 + hf * 8 + (hf >= 10 ? 1600 : 0)) = u32x2{kv[2 * k], kv[2 * k + 1]};
      }
    }
  }
  STAMP(15);
  u32 oad[3];
#pragma unroll
  for (int mt = 0; mt < 3; mt++) {
    const int col = mt < 2 ? 8 * q4 + 4 * mt + p4 : 32 + 2 * q4 + p4;
    const int hp = (mt == 2) ? (p4 == 2 ? 0 : 1) : (col >= 20 ? 1 : 0);
    const int row = (mt == 2 && p4 >= 2) ? hp * 21 + 20 : col + hp;
    oad[mt] = ((g >> 1) == hp) ? (u32)(L_KVT + row * 80 + 40 * (g & 1)) : L_ZERO;
    asm volatile("" : "+v"(oad[mt]));
  }
  asm volatile("s_waitcnt vmcnt(0) lgkmcnt(0)\n\ts_barrier" ::: "memory");
  STAMP(16);
  {
    f32x4 oo[3];
#pragma unroll
    for (int mt = 0; mt < 3; mt++) {
      LAS char* ap = sm + oad[mt];
      u32x2 a0 = *(LAS u32x2*)(ap), a1 = *(LAS u32x2*)(ap + 8), a2 = *(LAS u32x2*)(ap + 16), a3 = *(LAS u32x2*)(ap + 24), a4 = *(LAS u32x2*)(ap + 32);
      oo[mt] = mfma32(u32x4{a0[0], a0[1], a1[0], a1[1]}, u32x4{pq[0], pq[1], pq[2], pq[3]}, f32x4{0, 0, 0, 0});
      oo[mt] = mfma32(u32x4{a2[0], a2[1], a3[0], a3[1]}, u32x4{pq[4], pq[5], pq[6], pq[7]}, oo[mt]);
      oo[mt] = mfma16(a4, u32x2{pq[8], pq[9]}, oo[mt]);
    }
    const float r0 = rcp(oo[2][2] + 1e-6f), r1 = rcp(oo[2][3] + 1e-6f);
    const float ra_ = g <= 2 ? r0 : r1, rb_ = g <= 1 ? r0 : r1;
    u32x4 o8 = {pk2(oo[0][0] * ra_, oo[0][1] * ra_), pk2(oo[0][2] * ra_, oo[0][3] * ra_), pk2(oo[1][0] * rb_, oo[1][1] * rb_), pk2(oo[1][2] * rb_, oo[1][3] * rb_)};
    u32 o2 = san(pk2(oo[2][0] * r1, oo[2][1] * r1));
    const u32 ob = reg + R_OB + tw * 1280 + l * 16;
    ST4(san4(o8), ob);
    auto ra = __builtin_amdgcn_permlane32_swap(o2, o2, false, false);
    auto rb = __builtin_amdgcn_permlane16_swap(ra[0], ra[0], false, false);
    auto rc2 = __builtin_amdgcn_permlane16_swap(ra[1], ra[1], false, false);
    if (g == 0) ST4((u32x4{rb[0], rb[1], rc2[0], rc2[1]}), ob + 1024);
  }
  MEMFENCE();
  STAMP(17);
  const bool owner = (idx >> 7) == rt;
  if (owner) {
    if (tid < 320) {
      const int p0 = tid / 10, cg = tid - 10 * p0;
      const u32 ya = WS_REG + p0 * RS + R_YP + hg * 160 + cg * 16;
      f32x4 s;
#pragma unroll 1
      for (u32 spin = 0; spin < SPIN_CAP; spin++) {
        MEMFENCE();
        u32x4 yv[8];
        u32 m = 0;
#pragma unroll
        for (int k = 0; k < 8; k++) yv[k] = __builtin_amdgcn_raw_buffer_load_b128(wsr, ya, k * 32 * RS, 16);
        s = f32x4{0, 0, 0, 0};
#pragma unroll
        for (int k = 0; k < 8; k++) { m = umax_(m, max4(yv[k])); s += __builtin_bit_cast(f32x4, yv[k]); }
        if (__all(m != SENT)) break;
      }
      *(LAS f32x4*)(sm + 24576 + tid * 16) = s;
    }
    BARRIER();
    if (tid < 40) {
      float s = 0.f;
#pragma unroll 4
      for (int k = 0; k < 32; k++) s += *(LAS float*)(sm + 24576 + (k * 40 + tid) * 4);
      u32 rb = rec[0] >> 16;
      rb = (rb & 0x8000u) ? (rb & 0x7FFFu) : (~rb & 0xFFFFu);
      float sig = rbf(rcp(1.f + __expf(-bflo(rb))));
      *(LAS float*)(sm + L_Y + tid * 4) = rbf(rbf(s) * sig);
    }
  }
  STAMP(18);
  f32x4 ao[2][3];
  u32x2 xv[3], xnp[3];
  {
    LAS char* xp = sm + w * 2048 + i16 * 80 + 8 * g;
#pragma unroll
    for (int t = 0; t < 3; t++) {
      xv[t] = *(LAS u32x2*)(xp + 32 * t);
      u32 d0 = pk2(bflo(xv[t][0]) - m_b, bfhi(xv[t][0]) - m_b), d1 = pk2(bflo(xv[t][1]) - m_b, bfhi(xv[t][1]) - m_b);
      xnp[t] = u32x2{pk2(bflo(d0) * rs, bfhi(d0) * rs), pk2(bflo(d1) * rs, bfhi(d1) * rs)};
      asm volatile("" : "+v"(xnp[t]));
    }
    u32x4 wf[10][3];
    LAS char* wop = sm + L_WO + kh * 30720 + (4 * g + q4) * 96 + 8 * p4;
#pragma unroll
    for (int j = 0; j < 10; j++)
#pragma unroll
      for (int t = 0; t < 3; t++) wf[j][t] = trd2(wop + j * 3072 + 32 * t, 1536);
    const u32 obase = WS_REG + (rt * 16 + 8 * kh) * RS + R_OB + tg * 2560;
    u32 oa[5];
#pragma unroll
    for (int k = 0; k < 5; k++) { int C = 64 * k + l, pr = C / 80; oa[k] = obase + pr * (RS - 1280) + C * 16; }
#pragma unroll 1
    for (u32 spin = 0; spin < SPIN_CAP; spin++) {
      MEMFENCE();
      u32 v = __builtin_amdgcn_raw_buffer_load_b32(wsr, obase + (i16 & 7) * RS + (i16 >> 3) * 1280 + 1024 + g * 64, 0, 16);
      if (__all(v != SENT)) break;
    }
    STAMP(19);
#pragma unroll 1
    for (u32 spin = 0; spin < SPIN_CAP; spin++) {
      MEMFENCE();
      u32x4 of[2][10];
#pragma unroll
      for (int j = 0; j < 10; j++) {
        of[0][j] = __builtin_amdgcn_raw_buffer_load_b128(wsr, oa[j % 5], (j / 5) * 4 * RS + kh * 1280, 16);
        of[1][j] = __builtin_amdgcn_raw_buffer_load_b128(wsr, oa[j % 5], (j / 5) * 4 * RS + (1 - kh) * 1280, 16);
      }
#pragma unroll
      for (int t = 0; t < 3; t++) { ao[0][t] = f32x4{0, 0, 0, 0}; ao[1][t] = f32x4{0, 0, 0, 0}; }
      u32 m = 0;
#pragma unroll
      for (int j = 0; j < 10; j++) {
#pragma unroll
        for (int t = 0; t < 3; t++) { ao[0][t] = mfma32(wf[j][t], of[0][j], ao[0][t]); ao[1][t] = mfma32(wf[j][t], of[1][j], ao[1][t]); }
        m = umax_(m, umax_(max4(of[0][j]), max4(of[1][j])));
      }
      if (__all(m != SENT)) break;
    }
  }
#pragma unroll
  for (int t = 0; t < 3; t++) *(LAS f32x4*)(sm + L_PHI + (tw ^ 1) * 3072 + t * 1024 + l * 16) = ao[1][t];
  STAMP(20);
  BARRIER();
  STAMP(21);
  {
    LAS char* xp = sm + w * 2048 + i16 * 80 + 8 * g;
    const bool sel = owner && ((idx & 127) == tw * 16 + i16);
#pragma unroll
    for (int t = 0; t < 3; t++) {
      f32x4 af = ao[0][t] + *(LAS f32x4*)(sm + L_PHI + tw * 3072 + t * 1024 + l * 16);
      if (sel && (t < 2 || g < 2)) {
        f32x4 yy = *(LAS f32x4*)(sm + L_Y + (16 * t + 4 * g) * 4);
        xnp[t] = u32x2{pk2(bflo(xnp[t][0]) + yy[0], bfhi(xnp[t][0]) + yy[1]), pk2(bflo(xnp[t][1]) + yy[2], bfhi(xnp[t][1]) + yy[3])};
      }
      u32 p0 = pk2(af[0], af[1]), p1 = pk2(af[2], af[3]);
      u32 q0 = pk2(bflo(xv[t][0]) + bflo(p0), bfhi(xv[t][0]) + bfhi(p0)), q1 = pk2(bflo(xv[t][1]) + bflo(p1), bfhi(xv[t][1]) + bfhi(p1));
      u32x2 res = {pk2(bflo(q0) + bflo(xnp[t][0]), bfhi(q0) + bfhi(xnp[t][0])), pk2(bflo(q1) + bflo(xnp[t][1]), bfhi(q1) + bfhi(xnp[t][1]))};
      if (t < 2 || g < 2) *(LAS u32x2*)(xp + 32 * t) = res;
    }
    __builtin_amdgcn_wave_barrier();
    LAS char* op = sm + w * 2048 + l * 16;
    *(u32x4*)((char*)out + slo0) = *(LAS u32x4*)(op);
    if (l < 16) *(u32x4*)((char*)out + slo1) = *(LAS u32x4*)(op + 1024);
  }
  STAMP(22);
}

extern "C" void kernel_launch(void* const* d_in, const int* in_sizes, int n_in, void* d_out, int out_size,
                              void* d_ws, size_t ws_size, hipStream_t stream) {
  fused_kernel<<<256, 512, 0, stream>>>((const u16*)d_in[0], (const u16*)d_in[1], (const u16*)d_in[2], (const u16*)d_in[3],
                                        (const u16*)d_in[5], (const u16*)d_in[6], (const u16*)d_in[7], (const u16*)d_in[8],
                                        (u16*)d_out, (char*)d_ws);
}
```

```cpp
#include <hip/hip_runtime.h>
#include <stdint.h>

typedef unsigned short u16;
typedef unsigned int u32;
typedef unsigned long long u64;
typedef __attribute__((ext_vector_type(8))) __bf16 bf16x8;
typedef __attribute__((ext_vector_type(4))) short s16x4;
typedef __attribute__((ext_vector_type(4))) float f32x4;
typedef __attribute__((ext_vector_type(2))) float f32x2;
typedef __attribute__((ext_vector_type(2))) __bf16 bf16x2;
typedef __attribute__((ext_vector_type(4))) u32 u32x4;
typedef __attribute__((ext_vector_type(2))) u32 u32x2;
#define LAS __attribute__((address_space(3)))
#define DI __device__ __forceinline__

constexpr u32 WS_EPOCH = 0;
constexpr u32 WS_REC = 8192;
constexpr u32 WS_ST = 16384;
constexpr u32 WS_REG = 49152;
constexpr u32 RS = 23936;
constexpr u32 R_OB = 0;
constexpr u32 R_KVP = 10240;
constexpr u32 R_YP = 17024;
constexpr u32 R_GUP = 19584;
constexpr u32 R_KVR = 20608;
constexpr u32 R_I1 = 20864;
constexpr u32 R_I3 = 20992;
constexpr u32 R_IG = 21120;
constexpr u32 R_IY = 21760;
constexpr u32 WS_YB = WS_REG + 256 * RS;
constexpr u32 WS_END = WS_YB + 655360;
constexpr u32 WS_STAMP = 128u << 20;

constexpr u32 L_W1 = 0;
constexpr u32 L_WQ = 0;
constexpr u32 L_WO0 = 30720;
constexpr u32 L_WO1 = 92160;
constexpr u32 L_EXQ = 61440;
constexpr u32 L_CSP = 102400;
constexpr u32 L_EXA = 103424;
constexpr u32 L_PF = 162816;
constexpr u32 L_PHI = 122880;
constexpr u32 L_V = 143360;
constexpr u32 L_SM = 155648;
constexpr u32 L_WPHI = L_SM;
constexpr u32 L_ZERO = L_SM + 1920;
constexpr u32 L_KVT = L_SM + 1984;
constexpr u32 L_CSQ = L_KVT + 3360;
constexpr u32 L_KEY = L_CSQ + 768;
constexpr u32 L_IDX = L_KEY + 64;
constexpr u32 L_Y = L_IDX + 64;
constexpr u32 L_MS = L_Y + 160;
constexpr u32 L_SYN = L_MS + 512;
enum { F_W0 = 0, F_W1, F_ST, F_CS, F_P1, F_Q0, F_Q1, F_PHI, F_KV, F_CSQ, F_P2, F_KVT, F_WO, F_OS, F_O, F_Y, F_EX = 16, F_WP = 24, F_KR, F_IDX, F_YG, F_YP, F_O1 };
constexpr u32 L_YS = L_PHI;
constexpr u32 L_TOTAL = 163840;

constexpr u32 SENT = 0xFFFFFFFFu;
constexpr u32 SPIN_CAP = 1u << 15;
#define HOP1_AT 6

DI u32 pk2(float a, float b) { f32x2 v = {a, b}; return __builtin_bit_cast(u32, __builtin_convertvector(v, bf16x2)); }
DI float bflo(u32 u) { return __uint_as_float(u << 16); }
DI float bfhi(u32 u) { return __uint_as_float(u & 0xFFFF0000u); }
DI float rbf(float a) { return bflo(pk2(a, 0.f)); }
DI float rcp(float a) { return __builtin_amdgcn_rcpf(a); }
DI float dot2(u32 a, u32 b, float c) { return __builtin_amdgcn_fdot2_f32_bf16(__builtin_bit_cast(bf16x2, a), __builtin_bit_cast(bf16x2, b), c, false); }
DI f32x4 mfma32(u32x4 a, u32x4 b, f32x4 c) {
  return __builtin_amdgcn_mfma_f32_16x16x32_bf16(__builtin_bit_cast(bf16x8, a), __builtin_bit_cast(bf16x8, b), c, 0, 0, 0);
}
DI f32x4 mfma16(u32x2 a, u32x2 b, f32x4 c) {
  return mfma32(u32x4{a[0], a[1], 0u, 0u}, u32x4{b[0], b[1], 0u, 0u}, c);
}
DI u32x2 trd(LAS char* p) { return __builtin_bit_cast(u32x2, __builtin_amdgcn_ds_read_tr16_b64_v4i16((LAS s16x4*)p)); }
DI u32x4 trd2(LAS char* p, u32 d) { u32x2 a = trd(p), b = trd(p + d); return u32x4{a[0], a[1], b[0], b[1]}; }
DI void glds16(const void* sbase, u32 voff, u32 lds_dst) {
  asm volatile("s_mov_b32 m0, %2\n\ts_nop 0\n\tglobal_load_lds_dwordx4 %0, %1" :: "v"(voff), "s"(sbase), "s"(lds_dst) : "memory");
}
DI u32 umin_(u32 a, u32 b) { return a < b ? a : b; }
DI u32 umax_(u32 a, u32 b) { return a > b ? a : b; }
DI u32 san(u32 v) { return umin_(v, 0xFFFFFFFEu); }
DI u32x4 san4(u32x4 v) { return u32x4{san(v[0]), san(v[1]), san(v[2]), san(v[3])}; }
DI u32 max4(u32x4 v) { return umax_(umax_(v[0], v[1]), umax_(v[2], v[3])); }
DI float xchg16_max(float v) { auto r = __builtin_amdgcn_permlane16_swap(__float_as_uint(v), __float_as_uint(v), false, false); return fmaxf(__uint_as_float(r[0]), __uint_as_float(r[1])); }
DI float xchg16_min(float v) { auto r = __builtin_amdgcn_permlane16_swap(__float_as_uint(v), __float_as_uint(v), false, false); return fminf(__uint_as_float(r[0]), __uint_as_float(r[1])); }
DI float xchg16_add(float v) { auto r = __builtin_amdgcn_permlane16_swap(__float_as_uint(v), __float_as_uint(v), false, false); return __uint_as_float(r[0]) + __uint_as_float(r[1]); }
DI float xchg32_add(float v) { auto r = __builtin_amdgcn_permlane32_swap(__float_as_uint(v), __float_as_uint(v), false, false); return __uint_as_float(r[0]) + __uint_as_float(r[1]); }
template <int CTRL> DI float dpp(float v) { return __int_as_float(__builtin_amdgcn_update_dpp(0, __float_as_int(v), CTRL, 0xf, 0xf, true)); }
template <int CTRL> DI u32 dppu(u32 v) { return (u32)__builtin_amdgcn_update_dpp(0, (int)v, CTRL, 0xf, 0xf, true); }
DI float wave_sum(float v) {
  v += dpp<0xB1>(v); v += dpp<0x4E>(v); v += dpp<0x141>(v); v += dpp<0x140>(v);
  return xchg32_add(xchg16_add(v));
}
DI float row_sum(float v) { v += dpp<0xB1>(v); v += dpp<0x4E>(v); v += dpp<0x141>(v); return v + dpp<0x140>(v); }
DI u32 row_max(u32 v) { v = umax_(v, dppu<0xB1>(v)); v = umax_(v, dppu<0x4E>(v)); v = umax_(v, dppu<0x141>(v)); return umax_(v, dppu<0x140>(v)); }
DI u32x4 ld4w(const char* base, u32 off) { u32x4 v; asm volatile("global_load_dwordx4 %0, %1, %2 sc1\n\ts_waitcnt vmcnt(0)" : "=&v"(v) : "v"(off), "s"(base) : "memory"); return v; }
#define MEMFENCE() asm volatile("" ::: "memory")
#define BARRIER() asm volatile("s_waitcnt lgkmcnt(0)\n\ts_barrier" ::: "memory")
#define SB() __builtin_amdgcn_sched_barrier(0)
#define LD4(off) __builtin_amdgcn_raw_buffer_load_b128(wsr, (off), 0, 16)
#define LD2(off) __builtin_amdgcn_raw_buffer_load_b64(wsr, (off), 0, 16)
#define ST4(v, off) __builtin_amdgcn_raw_buffer_store_b128((v), wsr, (off), 0, 16)
#define ST2(v, off) __builtin_amdgcn_raw_buffer_store_b64((v), wsr, (off), 0, 16)

#define STAMP(k) do { } while (0)

DI void softmax_pack(const f32x4& p0, const f32x4& p1, const f32x4& p2, u32 (&o)[10]) {
  float p[10] = {p0[0], p0[1], p0[2], p0[3], p1[0], p1[1], p1[2], p1[3], p2[0], p2[1]};
  float mx = p[0], mn = p[0];
#pragma unroll
  for (int i = 1; i < 10; i++) { mx = fmaxf(mx, p[i]); mn = fminf(mn, p[i]); }
  mx = xchg16_max(mx); mn = xchg16_min(mn);
  const float L2E = 1.4426950408889634f;
  const float a = -mx * L2E, b = mn * L2E;
  float e1[10], e2[10], s1 = 0.f, s2 = 0.f;
#pragma unroll
  for (int i = 0; i < 10; i++) {
    e1[i] = __builtin_amdgcn_exp2f(fmaf(p[i], L2E, a));
    e2[i] = __builtin_amdgcn_exp2f(fmaf(p[i], -L2E, b));
    s1 += e1[i]; s2 += e2[i];
  }
  s1 = xchg16_add(s1); s2 = xchg16_add(s2);
  const float r1 = rcp(s1), r2 = rcp(s2);
#pragma unroll
  for (int i = 0; i < 5; i++) {
    o[i] = pk2(e1[2 * i] * r1, e1[2 * i + 1] * r1);
    o[5 + i] = pk2(e2[2 * i] * r2, e2[2 * i + 1] * r2);
  }
}

#define THR "3"

#define SIG_SET(f, v) do { MEMFENCE(); *(volatile LAS u32*)(sm + L_SYN + (f) * 4) = (v); MEMFENCE(); } while (0)
DI void sig_add(u32 addr) {
  u64 keep; u32 one = 1;
  asm volatile("s_mov_b64 %0, exec\n\ts_mov_b64 exec, 1\n\tds_add_u32 %1, %2\n\ts_mov_b64 exec, %0" : "=&s"(keep) : "v"(addr), "v"(one) : "memory");
}
#define SIG_ADD(f) sig_add(ldsb + L_SYN + (f) * 4)
DI void sig_wait(u32 addr, u32 val) {
  u32 tv, ts, cnt;
  asm volatile(
      "s_mov_b32 %2, 0x400000\n"
      "LW%=:\n\t"
      "ds_read_b32 %0, %3\n\t"
      "s_waitcnt lgkmcnt(0)\n\t"
      "v_readfirstlane_b32 %1, %0\n\t"
      "s_nop 1\n\t"
      "s_cmp_ge_u32 %1, %4\n\t"
      "s_cbranch_scc1 LD%=\n\t"
      "s_sleep 1\n\t"
      "s_sub_u32 %2, %2, 1\n\t"
      "s_cmp_lg_u32 %2, 0\n\t"
      "s_cbranch_scc1 LW%=\n"
      "LD%=:"
      : "=&v"(tv), "=&s"(ts), "=&s"(cnt) : "v"(addr), "s"(val) : "memory", "scc");
}
#define SIG_WAIT(f, v) sig_wait(ldsb + L_SYN + (f) * 4, (v))
#define WPRIO 0
#define CLR_AT 1
#define PF_AT 0
#define KVD_AT 2
#define WO_FIRST 0
#define GW_AT 9
#define GTHR "40"
#define REC_AT 2
#define ST_AT 2
#define GW_SIG F_P1
#define H1D 8
#define H1S 4
#define RAWBAR() asm volatile("s_barrier" ::: "memory")
__global__ __launch_bounds__(768, 3) void fused_kernel(
    const u16* __restrict__ x, const u16* __restrict__ wqkv, const u16* __restrict__ wphi, const u16* __restrict__ wo,
    const u16* __restrict__ wr, const u16* __restrict__ wg, const u16* __restrict__ wu, const u16* __restrict__ wd,
    u16* __restrict__ out, char* __restrict__ ws) {
  __shared__ __attribute__((aligned(1024))) char smem_[L_TOTAL];
  LAS char* sm = (LAS char*)smem_;
  const u32 ldsb = (u32)(size_t)smem_;
  STAMP(0);
  const int tid = threadIdx.x, l = tid & 63;
  const int w = __builtin_amdgcn_readfirstlane(tid >> 6);
  const int g = l >> 4, i16 = l & 15, q4 = i16 >> 2, p4 = l & 3;
  const int b = blockIdx.x, xcd = b & 7, slot = b >> 3;
  const int rt = (xcd >> 1) * 4 + (slot >> 3), hg = (xcd & 1) * 8 + (slot & 7);
  const int wgi = rt * 16 + hg;
  const int og = hg ^ 1;
  const int rot = (hg + 2 * rt) % 5;
  const auto wsr = __builtin_amdgcn_make_buffer_rsrc((void*)ws, 0, WS_END, 0x00020000);
  const u32 reg = WS_REG + wgi * RS;
#define CROT(c) ((c) + rot >= 5 ? (c) + rot - 5 : (c) + rot)
#define FR(F, t) (u32x4{F[2 * (t)][0], F[2 * (t)][1], F[2 * (t) + 1][0], F[2 * (t) + 1][1]})
#define LOADEP() u32x2 ep; { const u32 z = 0; asm volatile("global_load_dwordx2 %0, %1, %2 sc1" : "=v"(ep) : "v"(z), "s"(ws) : "memory"); }
#define GETTAG() u32 tag_lo, tag_hi; { u64 e = (((u64)ep[1] << 32) | ep[0]) + 1; tag_lo = __builtin_amdgcn_readfirstlane((u32)e); tag_hi = __builtin_amdgcn_readfirstlane((u32)(e >> 32)); }

  if (tid < 32) *(LAS u32*)(sm + L_SYN + tid * 4) = 0u;
  if (w >= 8) {
    const int h = w - 8;
    __builtin_amdgcn_s_setprio(3);
    int l_ = __builtin_amdgcn_mbcnt_hi(~0u, __builtin_amdgcn_mbcnt_lo(~0u, 0u));
    asm volatile("" : "+v"(l_));
    const int l = l_, tid = w * 64 + l, g = l >> 4, i16 = l & 15, q4 = i16 >> 2, p4 = l & 3;
    const u32x4 ONES = {0x3F803F80u, 0x3F803F80u, 0x3F803F80u, 0x3F803F80u};
#define YSUM(part) do { u32x4 yv[15]; _Pragma("unroll") for (int k = 0; k < 15; k++) { const int u = (90 * (part) + 6 * k) * 10 + l; \
        yv[k] = LD4((l < 60 && u < 2560) ? WS_YB + og * 40960 + u * 16 : WS_END); } \
      f32x4 s_ = {0, 0, 0, 0}; _Pragma("unroll") for (int k = 0; k < 15; k++) s_ += __builtin_bit_cast(f32x4, yv[k]); \
      if (l < 60) *(LAS f32x4*)(sm + L_YS + ((part) * 60 + l) * 16) = s_; SIG_ADD(F_YP); } while (0)
    if (h < 2) {
      u32 woff[5];
#pragma unroll
      for (int k = 0; k < 5; k++) {
        int P = 64 * k + l, rho = P / 10, s = P - 10 * rho;
        woff[k] = (32 * ((rho >> 2) & 1) + 16 * ((rho >> 3) & 1) + 4 * ((rho >> 4) & 1) + (rho & 3)) * 3840 + 1280 + (s >= 5 ? 1200 : 0) + hg * 80 + s * 16;
      }
      const char* wbase = (const char*)wqkv + h * (320 * 3840);
#define WL(c) do { const char* b_ = wbase + CROT(c) * (64 * 3840); _Pragma("unroll") for (int k = 0; k < 10; k++) \
        glds16(b_ + (k / 5) * (8 * 3840), woff[k % 5], ldsb + L_W1 + (4 * (c) + 2 * (k / 5) + h) * 5120 + (k % 5) * 1024); } while (0)
#define WDONE(N, c) do { asm volatile("s_waitcnt vmcnt(" #N ")" ::: "memory"); SIG_SET(F_W0 + h, (c) + 1); } while (0)
      WL(0);
      BARRIER();
      WDONE(0, 0);
      __builtin_amdgcn_s_setprio(0);
      WL(1);
      __builtin_amdgcn_s_setprio(3);
      STAMP(1);
      WL(2);
      STAMP(2);
      WDONE(10, 1); WL(3);
      STAMP(3);
      WDONE(10, 2); WL(4);
      STAMP(4);
      {
        const int q = slot * 100 + h * 50 + (l >> 3);
#pragma unroll
        for (int k = 0; k < 7; k++) {
          const int qq = q + (k < 6 ? 8 * k : 42), row = qq / 5, cc_ = qq - 5 * row;
          if (k == 0) WDONE(10, 3);
          glds16(wqkv, row * 3840 + (xcd & 1) * 640 + cc_ * 128 + (l & 7) * 16, ldsb + L_PF);
        }
      }
      WDONE(7, 4);
      STAMP(5);
      u32 qoff[3], ooff[3];
#pragma unroll
      for (int k = 0; k < 3; k++) {
        int P = 64 * k + l, rho = P / 6, s = P - 6 * rho; if (s > 4) s = 4;
        qoff[k] = (32 * ((rho >> 2) & 1) + 16 * ((rho >> 3) & 1) + 4 * ((rho >> 4) & 1) + (rho & 3)) * 3840 + hg * 80 + s * 16;
        ooff[k] = (8 * ((rho >> 2) & 3) + 4 * ((rho >> 4) & 1) + (rho & 3)) * 1280 + og * 80 + s * 16;
      }
      u32x4 wr_[30];
#define OROT(k) (2 * CROT((k) / 6) + ((k) / 3 & 1))
#pragma unroll
      for (int k = 0; k < WO_FIRST; k++)
        asm volatile("global_load_dwordx4 %0, %1, %2" : "=v"(wr_[k]) : "v"(ooff[k % 3]), "s"((const char*)wo + (10 * h + OROT(k)) * 40960));
      SIG_WAIT(F_P1, 9);
      STAMP(6);
#pragma unroll 1
      for (int c = 0; c < 5; c++) {
        const char* b_ = wbase + CROT(c) * (64 * 3840);
#pragma unroll
        for (int k = 0; k < 6; k++) glds16(b_ + (k / 3) * (8 * 3840), qoff[k % 3], ldsb + L_WQ + (4 * c + 2 * (k / 3) + h) * 3072 + (k % 3) * 1024);
      }
      STAMP(7);
      asm volatile("s_waitcnt vmcnt(0)" ::: "memory");
      SIG_SET(F_Q0 + h, 1);
      STAMP(8);
#pragma unroll
      for (int k = WO_FIRST; k < 30; k++)
        asm volatile("global_load_dwordx4 %0, %1, %2" : "=v"(wr_[k]) : "v"(ooff[k % 3]), "s"((const char*)wo + (10 * h + OROT(k)) * 40960));
      STAMP(9);
      {
        f32x4 cq[3];
#pragma unroll
        for (int t = 0; t < 3; t++) cq[t] = f32x4{0, 0, 0, 0};
        LAS char* wqp = sm + L_WQ + h * 3072 + (4 * g + q4) * 96 + 8 * p4;
#pragma unroll 1
        for (int k = 0; k < 10; k++)
#pragma unroll
          for (int t = 0; t < 3; t++) cq[t] = mfma32(ONES, trd2(wqp + k * 6144 + 32 * t, 1536), cq[t]);
#pragma unroll
        for (int t = 0; t < 3; t++) *(LAS float*)(sm + L_CSQ + (48 * h + 16 * t + i16) * 4) = cq[t][0];
        SIG_ADD(F_CSQ);
        SIG_ADD(F_P2);
      }
      SIG_WAIT(h ? F_KR : F_P2, h ? 8 : 10);
      asm volatile("s_waitcnt vmcnt(0)" ::: "memory");
      STAMP(10);
      __builtin_amdgcn_s_setprio(0);
#pragma unroll
      for (int k = 0; k < 30; k++) {
        asm volatile("" : "+v"(wr_[k]));
        *(LAS u32x4*)(sm + (h ? L_WO1 : L_WO0) + OROT(k) * 3072 + (k % 3) * 1024 + l * 16) = wr_[k];
      }
      SIG_ADD(F_WO);
      STAMP(12);
      SIG_WAIT(F_IDX, 1);
      if ((((int)*(volatile LAS u32*)(sm + L_SYN + F_IDX * 4) - 1) >> 7) == rt) { SIG_WAIT(F_YG, 1); YSUM(h); }
    } else {
      const int e = h - 2;
#define STATS_LD() u32x4 sx[5], sw[5]; { const u32 o = (4 * e + g) * 1280 + i16 * 16, o2 = i16 * 16; \
          _Pragma("unroll") for (int k = 0; k < 5; k++) { asm volatile("global_load_dwordx4 %0, %1, %2 offset:%3" : "=v"(sx[k]) : "v"(o), "s"(x + wgi * 8 * 640), "i"(k * 256)); \
            asm volatile("global_load_dwordx4 %0, %1, %2 offset:%3" : "=v"(sw[k]) : "v"(o2), "s"(wr), "i"(k * 256)); } }
#define STATS() do { _Pragma("unroll") for (int k = 0; k < 5; k++) asm volatile("" : "+v"(sx[k]), "+v"(sw[k])); \
          float s = 0.f, q = 0.f; \
          _Pragma("unroll") for (int k = 0; k < 20; k++) { s = dot2(sx[k >> 2][k & 3], 0x3F803F80u, s); q = dot2(sx[k >> 2][k & 3], sx[k >> 2][k & 3], q); } \
          const float mean = row_sum(s) * (1.f / 640.f); \
          const float var = fmaxf(row_sum(q) * (1.f / 640.f) - mean * mean, 0.f); \
          const float mb = rbf(mean), sb = rbf(__builtin_amdgcn_sqrtf(rbf(rbf(var) + 1e-5f))), rsb = rcp(sb); \
          const u32 ms = pk2(mb, sb); const int srow = wgi * 8 + 4 * e + g; \
          if (i16 == 0) ST4((u32x4{ms, tag_lo, ms, tag_hi}), WS_ST + srow * 16); \
          float r = 0.f; \
          _Pragma("unroll") for (int k = 0; k < 20; k++) { const u32 xd = sx[k >> 2][k & 3]; \
            u32 d = pk2(bflo(xd) - mb, bfhi(xd) - mb), n = pk2(bflo(d) * rsb, bfhi(d) * rsb); r = dot2(n, sw[k >> 2][k & 3], r); } \
          r = row_sum(r); \
          const u32 rb = pk2(r, 0.f) & 0xFFFFu; \
          const u32 ord = (rb & 0x8000u) ? (~rb & 0xFFFFu) : (rb | 0x8000u); \
          if (i16 == 0) *(LAS u32x2*)(sm + L_KEY + (4 * e + g) * 8) = u32x2{(ord << 16) | (u32)(0x7FFF - srow), ms}; \
          SIG_ADD(F_ST); } while (0)
      if (e == 1) {
        LOADEP();
        STATS_LD();
        BARRIER();
        if (l < 14) ST4((u32x4{SENT, SENT, SENT, SENT}), reg + R_KVR + l * 16);
        asm volatile("s_waitcnt vmcnt(1)" : "+v"(ep) :: "memory");
        GETTAG();
        STAMP(1);
        STATS();
        {
          SIG_WAIT(F_ST, 2);
          u32x2 kk = *(LAS u32x2*)(sm + L_KEY + (l & 7) * 8);
          u32 km = row_max(kk[0]);
          if (l < 8 && kk[0] == km) ST4((u32x4{kk[0], tag_lo, kk[1], tag_hi}), WS_REC + b * 16);
        }
#pragma unroll
        for (int k = 0; k < 7; k++) ST4((u32x4{SENT, SENT, SENT, SENT}), (k * 64 + l < 420) ? reg + R_KVP + (k * 64 + l) * 16 : WS_END);
        STAMP(2);
        u32x4 s0 = {0, 0, 0, 0}, s1 = {0, 0, 0, 0};
        const u32 sta = WS_ST + (rt * 128 + l) * 16;
#define LDST() asm volatile("global_load_dwordx4 %0, %2, %3 sc1\n\tglobal_load_dwordx4 %1, %2, %3 offset:1024 sc1\n\ts_waitcnt vmcnt(0)" : "=&v"(s0), "=&v"(s1) : "v"(sta), "s"(ws) : "memory")
        f32x4 cs[5];
#pragma unroll
        for (int t = 0; t < 5; t++) cs[t] = f32x4{0, 0, 0, 0};
        LAS char* w1p = sm + L_W1 + (4 * g + q4) * 160 + 8 * p4;
#pragma unroll 1
        for (int c = 0; c < 5; c++) {
          if (c == ST_AT) LDST();
          SIG_WAIT(F_W0, c + 1); SIG_WAIT(F_W1, c + 1);
#pragma unroll
          for (int k = 0; k < 4; k++)
#pragma unroll
            for (int t = 0; t < 5; t++) cs[t] = mfma32(ONES, trd2(w1p + (4 * c + k) * 5120 + 32 * t, 2560), cs[t]);
          if (c == 2) SIG_ADD(F_P1);
        }
        STAMP(3);
#pragma unroll 1
        for (u32 spin = 0; spin < SPIN_CAP; spin++) {
          if (__all(s0[1] == tag_lo && s0[3] == tag_hi && s0[0] == s0[2] && s1[1] == tag_lo && s1[3] == tag_hi && s1[0] == s1[2])) break;
          __builtin_amdgcn_s_sleep(4);
          LDST();
        }
        *(LAS u32*)(sm + L_MS + l * 4) = s0[0];
        *(LAS u32*)(sm + L_MS + 256 + l * 4) = s1[0];
#pragma unroll
        for (int t = 0; t < 5; t++) *(LAS float*)(sm + L_CSP + (16 * t + i16) * 4) = cs[t][0];
        SIG_SET(F_CS, 1);
        STAMP(4);
        if (l < 16) ST2((u32x2{tag_lo, tag_hi}), WS_REG + (l * 16 + hg) * RS + R_I1 + rt * 8);
        STAMP(11);
        SIG_WAIT(F_PHI, 8);
        STAMP(5);
#define HINT(addr) do { _Pragma("unroll 1") for (u32 spin = 0; spin < SPIN_CAP; spin++) { u32x4 f_ = ld4w(ws, (addr) + (l & 7) * 16); \
          if (__all(f_[0] == tag_lo && f_[1] == tag_hi && f_[2] == tag_lo && f_[3] == tag_hi)) break; } } while (0)
        HINT(reg + R_I1);
        SIG_WAIT(F_KR, 8);
        __builtin_amdgcn_s_sleep(H1D);
        STAMP(6);
        {
          const int he = l >= 28 ? l - 28 : l;
          const bool ok = (l < 56) && (rt < 15);
          const u32 h1a = ok ? WS_REG + ((l >= 28 ? 128 : 0) + hg) * RS + R_KVP + (28 * rt + he) * 16 : WS_END;
          u32x4 h1[8];
#pragma unroll 1
          for (u32 spin = 0; spin < SPIN_CAP; spin++) {
            MEMFENCE();
            u32 m = 0;
#pragma unroll
            for (int k = 0; k < 8; k++) h1[k] = __builtin_amdgcn_raw_buffer_load_b128(wsr, h1a, k * 16 * RS, 16);
#pragma unroll
            for (int k = 0; k < 8; k++) m = umax_(m, max4(h1[k]));
            if (__all(m != SENT)) break;
            __builtin_amdgcn_s_sleep(H1S);
          }
          f32x4 s = __builtin_bit_cast(f32x4, h1[0]);
#pragma unroll
          for (int k = 1; k < 8; k++) s += __builtin_bit_cast(f32x4, h1[k]);
#pragma unroll
          for (int r = 0; r < 4; r++) s[r] += __shfl(s[r], (l + 28) & 63);
          if (ok && l < 28) ST2((u32x2{san(pk2(s[0], s[1])), san(pk2(s[2], s[3]))}), reg + R_KVR + l * 8);
        }
        STAMP(7);
        {
          const int hpr = l / 14, e2 = l - 14 * hpr;
          const u32 h2a = WS_REG + (hpr * 16 + hg) * RS + R_KVR + e2 * 16;
          u32 ka[8];
#pragma unroll
          for (int k = 0; k < 8; k++) { int it = 112 * (k >> 1) + 2 * l + (k & 1), hf = it / 21, c = it - 21 * hf; ka[k] = L_KVT + c * 80 + hf * 8 + (hf >= 10 ? 1600 : 0); }
          u32x4 kv[4];
#pragma unroll 1
          for (u32 spin = 0; spin < SPIN_CAP; spin++) {
            MEMFENCE();
            u32 m = 0;
#pragma unroll
            for (int k = 0; k < 4; k++) kv[k] = LD4((l < 56 && 4 * k + hpr < 15) ? h2a + k * 64 * RS : WS_END);
#pragma unroll
            for (int k = 0; k < 4; k++) m = umax_(m, max4(kv[k]));
            if (__all(m != SENT)) break;
          }
#pragma unroll
          for (int k = 0; k < 4; k++) if (l < 56 && 4 * k + hpr < 15) {
            *(LAS u32x2*)(sm + ka[2 * k]) = u32x2{kv[k][0], kv[k][1]};
            *(LAS u32x2*)(sm + ka[2 * k + 1]) = u32x2{kv[k][2], kv[k][3]};
          }
        }
        SIG_SET(F_KVT, 1);
        STAMP(8);
        SIG_WAIT(F_OS, 8);
        if (l < 16) ST2((u32x2{tag_lo, tag_hi}), WS_REG + (rt * 16 + l) * RS + R_I3 + hg * 8);
        STAMP(9);
#pragma unroll 1
        for (u32 spin = 0; spin < SPIN_CAP; spin++) {
          u32x4 f_ = ld4w(ws, reg + R_I3 + (l & 7) * 16);
          const u64 okm = __ballot(f_[0] == tag_lo && f_[1] == tag_hi && f_[2] == tag_lo && f_[3] == tag_hi);
          if ((okm & 0xF) == 0xF) SIG_SET(F_O, 1);
          if ((okm & 0xF0) == 0xF0) SIG_SET(F_O1, 1);
          if ((okm & 0xFF) == 0xFF) break;
        }
        SIG_SET(F_O, 1); SIG_SET(F_O1, 1);
        STAMP(10);
      } else {
        LOADEP();
        STATS_LD();
        u32 wp[7];
#pragma unroll
        for (int k = 0; k < 7; k++) { const u32 o = hg * 1600 + (l + 64 * k < 400 ? l + 64 * k : 0) * 4; asm volatile("global_load_dword %0, %1, %2" : "=v"(wp[k]) : "v"(o), "s"(wphi)); }
        BARRIER();
        if (l < 4) *(LAS u32x4*)(sm + L_ZERO + l * 16) = u32x4{0, 0, 0, 0};
        if (l < 40) { *(LAS u32*)(sm + L_WPHI + l * 48 + 20) = 0u; *(LAS u32*)(sm + L_WPHI + l * 48 + 44) = 0u; }
        asm volatile("s_waitcnt vmcnt(0)" : "+v"(ep), "+v"(wp[0]), "+v"(wp[1]), "+v"(wp[2]), "+v"(wp[3]), "+v"(wp[4]), "+v"(wp[5]), "+v"(wp[6]) :: "memory");
        GETTAG();
        STATS();
#pragma unroll
        for (int k = 0; k < 7; k++) { const int u = l + 64 * k, row = u / 10, z = u - 10 * row; if (u < 400) *(LAS u32*)(sm + L_WPHI + row * 48 + z * 4 + (z >= 5 ? 4 : 0)) = wp[k]; }
        SIG_SET(F_WP, 1);
        STAMP(1);
        const int cb = wgi < 240 ? wgi / 20 : 0, ksp = wgi < 240 ? wgi - 20 * cb : 0;
        SIG_WAIT(F_W0, REC_AT);
        u32x2 rec;
        {
          u32 km = 0, ms = 0;
          u32x4 r_[4];
#pragma unroll 1
          for (u32 spin = 0; spin < SPIN_CAP; spin++) {
            const u32 ra = WS_REC + l * 16;
            asm volatile("global_load_dwordx4 %0, %4, %5 sc1\n\tglobal_load_dwordx4 %1, %4, %5 offset:1024 sc1\n\tglobal_load_dwordx4 %2, %4, %5 offset:2048 sc1\n\t"
                         "global_load_dwordx4 %3, %4, %5 offset:3072 sc1\n\ts_waitcnt vmcnt(0)"
                         : "=&v"(r_[0]), "=&v"(r_[1]), "=&v"(r_[2]), "=&v"(r_[3]) : "v"(ra), "s"(ws) : "memory");
            bool ok = true;
#pragma unroll
            for (int k = 0; k < 4; k++) ok = ok && r_[k][1] == tag_lo && r_[k][3] == tag_hi;
            if (__all(ok)) break;
            __builtin_amdgcn_s_sleep(16);
          }
#pragma unroll
          for (int k = 0; k < 4; k++) if (r_[k][0] > km) { km = r_[k][0]; ms = r_[k][2]; }
          u32 kx = row_max(km);
          { auto r = __builtin_amdgcn_permlane16_swap(kx, kx, false, false); kx = umax_(r[0], r[1]); }
          { auto r = __builtin_amdgcn_permlane32_swap(kx, kx, false, false); kx = umax_(r[0], r[1]); }
          const int src = __builtin_ctzll(__ballot(km == kx));
          rec = u32x2{(u32)__builtin_amdgcn_readfirstlane(kx), (u32)__builtin_amdgcn_readlane(ms, src)};
        }
        const int idx = (0x7FFF - (rec[0] & 0xFFFFu)) & 2047;
        if (b == 0 && l == 0) ST2((u32x2{tag_lo, tag_hi}), WS_EPOCH);
        SIG_SET(F_IDX, idx + 1);
        u32 xr;
        { const u32 xo = (32 * ksp) * 2 + i16 * 4; asm volatile("global_load_dword %0, %1, %2" : "=v"(xr) : "v"(xo), "s"(x + idx * 640)); }
        STAMP(4);
        u32x4 gw[16]; u32x2 dw[18];
        SIG_WAIT(GW_SIG, GW_AT);
        STAMP(2);
        {
          const u16* gm = cb < 6 ? wg + cb * 256 : wu + (cb - 6) * 256;
          const u32 go = (32 * ksp + (l >> 5)) * 3072 + (l & 31) * 16;
#pragma unroll
          for (int k = 0; k < 16; k++) asm volatile("global_load_dwordx4 %0, %1, %2\n\ts_waitcnt vmcnt(" GTHR ")" : "=v"(gw[k]) : "v"(go), "s"(gm + k * 2 * 1536));
        }
        {
          const u16* dp = wd + (6 * wgi) * 640;
          const u32 dof = l * 8, dof2 = (l < 32 ? l : 0) * 8;
#pragma unroll
          for (int k = 0; k < 6; k++) {
            asm volatile("global_load_dwordx2 %0, %1, %2" : "=v"(dw[3 * k]) : "v"(dof), "s"(dp + k * 640));
            asm volatile("global_load_dwordx2 %0, %1, %2 offset:512" : "=v"(dw[3 * k + 1]) : "v"(dof), "s"(dp + k * 640));
            asm volatile("global_load_dwordx2 %0, %1, %2 offset:1024" : "=v"(dw[3 * k + 2]) : "v"(dof2), "s"(dp + k * 640));
          }
        }
        STAMP(3);
        {
          asm volatile("s_waitcnt vmcnt(18)" : "+v"(xr) :: "memory");
          STAMP(5);
#pragma unroll
          for (int k = 0; k < 16; k++) asm volatile("" : "+v"(gw[k]));
          const float sel_m = bflo(rec[1]), sel_r = rcp(bfhi(rec[1]));
          { u32 d = pk2(bflo(xr) - sel_m, bfhi(xr) - sel_m); xr = pk2(bflo(d) * sel_r, bfhi(d) * sel_r); }
          const float xa = (l >> 5) ? bfhi(xr) : bflo(xr);
          float v[8];
#pragma unroll
          for (int k = 0; k < 8; k++) v[k] = 0.f;
#define M1ROW(r) do { const float a = dpp<0x150 + r>(xa); _Pragma("unroll") for (int k = 0; k < 4; k++) { v[2 * k] = fmaf(a, bflo(gw[r][k]), v[2 * k]); v[2 * k + 1] = fmaf(a, bfhi(gw[r][k]), v[2 * k + 1]); } } while (0)
          M1ROW(0); M1ROW(1); M1ROW(2); M1ROW(3); M1ROW(4); M1ROW(5); M1ROW(6); M1ROW(7);
          M1ROW(8); M1ROW(9); M1ROW(10); M1ROW(11); M1ROW(12); M1ROW(13); M1ROW(14); M1ROW(15);
          u32x4 u4;
#pragma unroll
          for (int k = 0; k < 4; k++) {
            auto r = __builtin_amdgcn_permlane32_swap(__float_as_uint(v[k]), __float_as_uint(v[k + 4]), false, false);
            u4[k] = __float_as_uint(__uint_as_float(r[0]) + __uint_as_float(r[1]));
          }
          ST4(u4, wgi < 240 ? reg + R_GUP + (l & 31) * 32 + (l >> 5) * 16 : WS_END);
          asm volatile("s_waitcnt vmcnt(0)" ::: "memory");
          const int c0 = (cb % 6) * 256, d = c0 / 6 + l;
          if (wgi < 240 && 6 * d < c0 + 256) ST2((u32x2{tag_lo, tag_hi}), WS_REG + d * RS + R_IG + ((6 * d < c0 ? 40 : 0) + (cb / 6) * 20 + ksp) * 8);
        }
        STAMP(6);
        STAMP(7);
        {
          const int nsl = ((6 * wgi) >> 8) == ((6 * wgi + 5) >> 8) ? 20 : 40;
#pragma unroll 1
          for (u32 spin = 0; spin < SPIN_CAP; spin++) {
            u32x4 f0 = ld4w(ws, reg + R_IG + (l < nsl ? l : 0) * 16);
            if (__all(f0[0] == tag_lo && f0[1] == tag_hi && f0[2] == tag_lo && f0[3] == tag_hi)) break;
            __builtin_amdgcn_s_sleep(16);
          }
        }
        STAMP(8);
        {
          const int kw = l / 3, pr = l - 3 * kw, col = 6 * wgi + 2 * pr;
          const u32 ga = l < 60 ? WS_REG + ((col >> 8) * 20 + kw) * RS + R_GUP + (col & 255) * 4 : WS_END;
          f32x2 gg2 = __builtin_bit_cast(f32x2, LD2(ga)), uu2 = __builtin_bit_cast(f32x2, __builtin_amdgcn_raw_buffer_load_b64(wsr, ga, 120 * RS, 16));
          float z[4] = {gg2[0], gg2[1], uu2[0], uu2[1]};
#pragma unroll
          for (int k = 0; k < 4; k++) {
            z[k] += __shfl(z[k], (l + 30) & 63);
            z[k] += __shfl(z[k], (l + 15) & 63);
            const float z0 = z[k];
            z[k] = z0 + __shfl(z0, (l + 3) & 63) + __shfl(z0, (l + 6) & 63) + __shfl(z0, (l + 9) & 63) + __shfl(z0, (l + 12) & 63);
          }
          const int vm = l >= 6 ? 1 : 0, vc = l - 6 * vm;
          const float s0 = __shfl(z[0], vc >> 1), s1 = __shfl(z[1], vc >> 1), s2 = __shfl(z[2], vc >> 1), s3 = __shfl(z[3], vc >> 1);
          const float tot = vm ? ((vc & 1) ? s3 : s2) : ((vc & 1) ? s1 : s0);
          float gg = rbf(tot);
          float uu = __shfl(gg, (l + 6) & 63);
          float sg = rbf(gg * rcp(1.f + __expf(-gg)));
          float hv = rbf(sg * uu);
#pragma unroll
          for (int k = 0; k < 18; k++) asm volatile("" : "+v"(dw[k]));
#pragma unroll
          for (int z = 0; z < 3; z++) {
            f32x4 y = {0, 0, 0, 0};
#pragma unroll
            for (int i = 0; i < 6; i++) {
              float hh = __int_as_float(__builtin_amdgcn_readlane(__float_as_int(hv), i));
              y[0] += hh * bflo(dw[3 * i + z][0]); y[1] += hh * bfhi(dw[3 * i + z][0]); y[2] += hh * bflo(dw[3 * i + z][1]); y[3] += hh * bfhi(dw[3 * i + z][1]);
            }
            { const int u = 64 * z + l, sl = u / 10; ST4(__builtin_bit_cast(u32x4, y), u < 160 ? WS_YB + (sl * 256 + wgi) * 160 + (u - 10 * sl) * 16 : WS_END); }
          }
          asm volatile("s_waitcnt vmcnt(0)" ::: "memory");
          if (l < 16) ST2((u32x2{tag_lo, tag_hi}), WS_REG + ((idx >> 7) * 16 + l) * RS + R_IY + wgi * 8);
        }
        STAMP(9);
        if ((idx >> 7) == rt) {
#pragma unroll 1
          for (u32 spin = 0; spin < SPIN_CAP; spin++) {
            u32x4 f0, f1; const u32 fa_ = reg + R_IY + l * 16;
            asm volatile("global_load_dwordx4 %0, %2, %3 sc1\n\tglobal_load_dwordx4 %1, %2, %3 offset:1024 sc1\n\ts_waitcnt vmcnt(0)" : "=&v"(f0), "=&v"(f1) : "v"(fa_), "s"(ws) : "memory");
            if (__all(f0[0] == tag_lo && f0[1] == tag_hi && f0[2] == tag_lo && f0[3] == tag_hi && f1[0] == tag_lo && f1[1] == tag_hi && f1[2] == tag_lo && f1[3] == tag_hi)) break;
            __builtin_amdgcn_s_sleep(2);
          }
          SIG_SET(F_YG, 1);
          SIG_WAIT(F_KR, 8);
          YSUM(2);
          SIG_WAIT(F_YP, 3);
          f32x4 t = {0, 0, 0, 0};
#pragma unroll
          for (int k = 0; k < 18; k++) t += *(LAS f32x4*)(sm + L_YS + (10 * k + (l < 10 ? l : 0)) * 16);
          u32 rb = rec[0] >> 16;
          rb = (rb & 0x8000u) ? (rb & 0x7FFFu) : (~rb & 0xFFFFu);
          const float sig = rbf(rcp(1.f + __expf(-bflo(rb))));
          if (l < 10) *(LAS f32x4*)(sm + L_Y + l * 16) = f32x4{rbf(rbf(t[0]) * sig), rbf(rbf(t[1]) * sig), rbf(rbf(t[2]) * sig), rbf(rbf(t[3]) * sig)};
        }
        SIG_SET(F_Y, idx + 1);
        STAMP(10);
      }
    }
  } else {
    const int tg = w & 3, kh = w >> 2, tw = 2 * tg + kh;
    u32x4 xf[20];
    const char* xb = (const char*)x + rt * (128 * 1280) + kh * 640;
    const u32 xoff = (u32)((((g >> 1) ? (tw ^ 1) : tw) * 16 + 4 * q4) * 1280 + (g & 1) * 64 + p4 * 16);
#define XI(c, r) ((r) < 2 ? 2 * (c) + (r) : 8 + 2 * (c) + (r))
#define XL1(c, r) asm volatile("global_load_dwordx4 %0, %1, %2 offset:%3" : "=v"(xf[XI(c, r)]) : "v"(xoff), "s"(xb + CROT(c) * 128), "i"((r) * 1280))
#define XG(c) do { XL1(c, 0); XL1(c, 1); XL1(c, 2); XL1(c, 3); } while (0)
    __builtin_amdgcn_s_setprio(2);
    XG(0);
    BARRIER();
    XG(1); XG(2); XG(3); XG(4);
    __builtin_amdgcn_s_setprio(0);
    STAMP(1);
    f32x4 acc[2][5];
#pragma unroll
    for (int t = 0; t < 5; t++) { acc[0][t] = f32x4{0, 0, 0, 0}; acc[1][t] = f32x4{0, 0, 0, 0}; }
    LAS char* w1p = sm + L_W1 + kh * 5120 + (4 * g + q4) * 160 + 8 * p4;
    const u64 mE = 0x5555555555555555ull, mO = ~mE, mL = 0x3333333333333333ull, mH = ~mL;
    u32 ta[4], ua[4];
#define DPX1 " quad_perm:[1,0,3,2] row_mask:0xf bank_mask:0xf\n\t"
#define DPX2 " quad_perm:[2,3,0,1] row_mask:0xf bank_mask:0xf\n\t"
#define CD2(M, X, d0, s0, k0, d1, s1, k1) asm volatile("s_mov_b64 vcc, %6\n\tv_cndmask_b32_dpp %0, %2, %3, vcc" X "v_cndmask_b32_dpp %1, %4, %5, vcc" X \
      : "=&v"(d0), "=&v"(d1) : "v"(s0), "v"(k0), "v"(s1), "v"(k1), "s"(M) : "vcc")
#define CD2I(M, X, d0, s0, d1, s1) asm volatile("s_mov_b64 vcc, %4\n\tv_cndmask_b32_dpp %0, %2, %0, vcc" X "v_cndmask_b32_dpp %1, %3, %1, vcc" X \
      : "+v"(d0), "+v"(d1) : "v"(s0), "v"(s1), "s"(M) : "vcc")
#define A_(c, r) xf[XI(c, r)]
#define TP(c, k) do { const int d_ = (k) & 3; u32 n0, n1; \
      if ((k) < 4) { CD2(mE, DPX1, n0, A_(c, 1)[d_], A_(c, 0)[d_], n1, A_(c, 3)[d_], A_(c, 2)[d_]); ua[d_] = n0; ta[d_] = n1; } \
      else if ((k) < 8) { n0 = A_(c, 1)[d_]; n1 = A_(c, 3)[d_]; CD2I(mO, DPX1, n0, A_(c, 0)[d_], n1, A_(c, 2)[d_]); A_(c, 1)[d_] = n0; A_(c, 3)[d_] = n1; } \
      else if ((k) < 12) { CD2(mL, DPX2, n0, ta[d_], ua[d_], n1, A_(c, 3)[d_], A_(c, 1)[d_]); A_(c, 0)[d_] = n0; A_(c, 2)[d_] = n1; } \
      else if ((k) < 16) { n1 = A_(c, 3)[d_]; CD2I(mH, DPX2, ta[d_], ua[d_], n1, A_(c, 1)[d_]); A_(c, 3)[d_] = n1; } \
      else { auto r0 = __builtin_amdgcn_permlane32_swap(A_(c, 0)[d_], ta[d_], false, false); auto r1 = __builtin_amdgcn_permlane32_swap(A_(c, 2)[d_], A_(c, 3)[d_], false, false); \
        A_(c, 0)[d_] = r0[0]; A_(c, 2)[d_] = r0[1]; A_(c, 1)[d_] = r1[0]; A_(c, 3)[d_] = r1[1]; } } while (0)
#define XW(N, c) asm volatile("s_waitcnt vmcnt(" #N ")" : "+v"(A_(c, 0)), "+v"(A_(c, 1)), "+v"(A_(c, 2)), "+v"(A_(c, 3)) :: "memory")
#define RD(F, j, r) F[r] = trd(w1p + (j) * 10240 + 32 * ((r) >> 1) + 2560 * ((r) & 1))
#define MF(F, j, k) acc[(k) & 1][(k) >> 1] = mfma32(FR(F, (k) >> 1), xf[10 * ((k) & 1) + (j)], acc[(k) & 1][(k) >> 1])
#define STEPA(FA, FB, c, V) do { _Pragma("unroll") for (int k = 0; k < 10; k++) { MF(FA, 2 * (c), k); RD(FB, 2 * (c) + 1, k); if ((c) < 4) TP((c) + 1, k); V(k); SB(); } } while (0)
#define STEPB(FA, FB, c) do { _Pragma("unroll") for (int k = 0; k < 4; k++) { MF(FB, 2 * (c) + 1, k); TP((c) + 1, 10 + k); SB(); } \
      if ((c) == 2) SIG_ADD(F_P1); \
      SIG_WAIT(F_W0 + kh, (c) + 2); \
      _Pragma("unroll") for (int k = 4; k < 10; k++) { if (k < 9) { RD(FA, 2 * (c) + 2, 2 * (k - 4)); RD(FA, 2 * (c) + 2, 2 * (k - 4) + 1); } MF(FB, 2 * (c) + 1, k); TP((c) + 1, 10 + k); SB(); } } while (0)
    {
      u32x2 fa[10], fb[10];
      XW(16, 0);
      u32 fil = l;
#pragma unroll
      for (int k = 0; k < 20; k++) { TP(0, k); asm volatile("v_add_u32 %0, 1, %0" : "+v"(fil)); SB(); }
      SIG_WAIT(F_W0 + kh, 1);
      STAMP(2);
#pragma unroll
      for (int r = 0; r < 10; r++) RD(fa, 0, r);
      SB();
#define V3(k) do { if ((k) == 1) XL1(3, 0); if ((k) == 3) XL1(3, 1); if ((k) == 5) XL1(3, 2); if ((k) == 7) XL1(3, 3); } while (0)
#define V4(k) do { if ((k) == 1) XL1(4, 0); if ((k) == 3) XL1(4, 1); if ((k) == 5) XL1(4, 2); if ((k) == 7) XL1(4, 3); } while (0)
#define VN(k) do { } while (0)
      XW(12, 1);
      STEPA(fa, fb, 0, VN); STEPB(fa, fb, 0);
      STAMP(3);
      XW(8, 2);
      STEPA(fa, fb, 1, VN); STEPB(fa, fb, 1);
      STAMP(4);
      XW(4, 3);
      STEPA(fa, fb, 2, VN); STEPB(fa, fb, 2);
      STAMP(5);
      XW(0, 4);
      STEPA(fa, fb, 3, VN); STEPB(fa, fb, 3);
      STAMP(6);
      STEPA(fa, fb, 4, VN);
#pragma unroll
      for (int k = 0; k < 10; k++) MF(fb, 9, (k + 1 - 2 * (k & 1)));
    }
    STAMP(7);
    {
    int l_ = __builtin_amdgcn_mbcnt_hi(~0u, __builtin_amdgcn_mbcnt_lo(~0u, 0u));
    asm volatile("" : "+v"(l_));
    const int l = l_, tid = w * 64 + l, g = l >> 4, i16 = l & 15, q4 = i16 >> 2, p4 = l & 3;
    {
      const int tp = tw ^ 1;
      *(LAS f32x4*)(sm + L_PHI + tp * 2560 + l * 16) = acc[1][0];
      *(LAS f32x4*)(sm + L_PHI + tp * 2560 + 1024 + l * 16) = acc[1][1];
      *(LAS f32x4*)(sm + L_V + tp * 1536 + l * 16) = acc[1][2];
      *(LAS f32x4*)(sm + L_EXA + tp * 2048 + l * 16) = acc[1][3];
      *(LAS f32x4*)(sm + L_EXA + tp * 2048 + 1024 + l * 16) = acc[1][4];
      SIG_SET(F_EX + tp, 1);
    }
    u32x4 wa32[3]; u32x2 wa16[3];
    SIG_WAIT(F_WP, 1);
    {
      const int hp = p4 >> 1;
      const int d0 = 4 * g + q4, d1 = d0 + 16, d2 = d0 + 32;
      LAS char* a0 = sm + (hp == 0 ? L_WPHI + d0 * 48 + 24 * (p4 & 1) : L_ZERO);
      LAS char* a1 = sm + ((d1 >= 20) == (hp == 1) ? L_WPHI + d1 * 48 + 24 * (p4 & 1) : L_ZERO);
      LAS char* a2 = sm + ((hp == 1 && d2 < 40) ? L_WPHI + d2 * 48 + 24 * (p4 & 1) : L_ZERO);
#pragma unroll
      for (int mt = 0; mt < 3; mt++) {
        u32x2 e0 = trd(a0 + 8 * mt), e1 = trd(a1 + 8 * mt);
        wa32[mt] = u32x4{e0[0], e0[1], e1[0], e1[1]};
        wa16[mt] = trd(a2 + 8 * mt);
      }
    }
    SIG_WAIT(F_EX + tw, 1);
    SIG_WAIT(F_CS, 1);
    STAMP(8);
    const u32 msv = *(LAS u32*)(sm + L_MS + (tw * 16 + i16) * 4);
    const float m_b = bflo(msv), rs = rcp(bfhi(msv));
    f32x4 cc[5], a[5];
#pragma unroll
    for (int t = 0; t < 3; t++) cc[t] = *(LAS f32x4*)(sm + L_CSP + (16 * t + 4 * g) * 4);
    a[0] = acc[0][0] + *(LAS f32x4*)(sm + L_PHI + tw * 2560 + l * 16);
    a[1] = acc[0][1] + *(LAS f32x4*)(sm + L_PHI + tw * 2560 + 1024 + l * 16);
    a[2] = acc[0][2] + *(LAS f32x4*)(sm + L_V + tw * 1536 + l * 16);
    {
      u32 kn[5][2];
#define NRM(A, t, C, O) do { f32x4 v_ = (A - m_b * C) * rs; O[t][0] = pk2(v_[0], v_[1]); O[t][1] = pk2(v_[2], v_[3]); } while (0)
      NRM(a[0], 0, cc[0], kn); NRM(a[1], 1, cc[1], kn); NRM(a[2], 2, cc[2], kn);
      SB();
      cc[3] = *(LAS f32x4*)(sm + L_CSP + (48 + 4 * g) * 4); cc[4] = *(LAS f32x4*)(sm + L_CSP + (64 + 4 * g) * 4);
      a[3] = acc[0][3] + *(LAS f32x4*)(sm + L_EXA + tw * 2048 + l * 16);
      a[4] = acc[0][4] + *(LAS f32x4*)(sm + L_EXA + tw * 2048 + 1024 + l * 16);
      u32x4 b32 = {kn[0][0], kn[0][1], kn[1][0], kn[1][1]};
      u32x2 b16 = {kn[2][0], kn[2][1]};
      f32x4 pp[3];
#pragma unroll
      for (int mt = 0; mt < 3; mt++) {
        pp[mt] = mfma32(wa32[mt], b32, f32x4{0, 0, 0, 0});
        pp[mt] = mfma16(wa16[mt], b16, pp[mt]);
      }
      NRM(a[3], 3, cc[3], kn); NRM(a[4], 4, cc[4], kn);
      __builtin_amdgcn_wave_barrier();
      LAS char* vr = sm + L_V + (16 * tw + i16) * 96;
      u32x2 v2 = (g >= 2) ? u32x2{kn[2][0], kn[2][1]} : u32x2{g == 0 ? 0x3F80u : 0u, 0u};
      *(LAS u32x2*)(vr + (g >= 2 ? 8 * (g - 2) : 80 + 8 * g)) = v2;
      *(LAS u32x2*)(vr + 16 + 8 * g) = u32x2{kn[3][0], kn[3][1]};
      *(LAS u32x2*)(vr + 48 + 8 * g) = u32x2{kn[4][0], kn[4][1]};
      u32 ph[10];
      softmax_pack(pp[0], pp[1], pp[2], ph);
      LAS char* pr = sm + L_PHI + (16 * tw + i16) * 160 + 40 * g;
#pragma unroll
      for (int k = 0; k < 5; k++) *(LAS u32x2*)(pr + 8 * k) = u32x2{ph[2 * k], ph[2 * k + 1]};
    }
    STAMP(9);
    SIG_ADD(F_PHI);
    SIG_WAIT(F_PHI, 8);
    STAMP(10);
    {
      const int i1 = w < 5 ? w + 8 : w;
      const int mt0 = w / 3, nt0 = w - 3 * mt0, mt1 = i1 < 9 ? i1 / 3 : 3 + ((i1 - 9) >> 1), nt1 = i1 < 9 ? i1 % 3 : 1 + ((i1 - 9) & 1);
      LAS char* ap = sm + L_PHI + (4 * g + q4) * 160 + 8 * p4;
      LAS char* bp = sm + L_V + (4 * g + q4) * 96 + 8 * p4;
      f32x4 d0 = {0, 0, 0, 0}, d1 = {0, 0, 0, 0};
#pragma unroll
      for (int u = 0; u < 4; u++) {
        d0 = mfma32(trd2(ap + 32 * mt0 + u * 5120, 2560), trd2(bp + 32 * nt0 + u * 3072, 1536), d0);
        d1 = mfma32(trd2(ap + 32 * mt1 + u * 5120, 2560), trd2(bp + 32 * nt1 + u * 3072, 1536), d1);
      }
#define KVST(d, mt, nt, en) do { const int fq = 4 * (mt) + g, col = 16 * (nt) + i16; int c = col - (fq >= 10 ? 20 : 0); bool ok = (c >= 0 && c < 20); \
        if (col == 40) { c = 20; ok = true; } ST4(san4(__builtin_bit_cast(u32x4, d)), (ok && (en)) ? reg + R_KVP + (fq * 21 + c) * 16 : WS_END); } while (0)
      KVST(d0, mt0, nt0, true); KVST(d1, mt1, nt1, w < 5);
    }
    SIG_ADD(F_KR);
    SIG_WAIT(F_Q0 + kh, 1);
    STAMP(11);
    f32x4 aq[2][3];
#pragma unroll
    for (int t = 0; t < 3; t++) { aq[0][t] = f32x4{0, 0, 0, 0}; aq[1][t] = f32x4{0, 0, 0, 0}; }
    {
      LAS char* wqp = sm + L_WQ + kh * 3072 + (4 * g + q4) * 96 + 8 * p4;
      u32x2 fa[6], fb[6];
#define RQ(F, P_, o, r) F[r] = trd(P_ + (o) + 32 * ((r) >> 1) + 1536 * ((r) & 1))
#define STEPQ(FA, FB, j) do { _Pragma("unroll") for (int k = 0; k < 6; k++) { aq[k & 1][k >> 1] = mfma32(FR(FA, k >> 1), xf[10 * (k & 1) + (j)], aq[k & 1][k >> 1]); \
          if ((j) < 9) RQ(FB, wqp, ((j) + 1) * 6144, k); SB(); } } while (0)
#pragma unroll
      for (int r = 0; r < 6; r++) RQ(fa, wqp, 0, r);
      SB();
#pragma unroll
      for (int j = 0; j < 10; j += 2) { STEPQ(fa, fb, j); STEPQ(fb, fa, j + 1);
      }
    }
#pragma unroll
    for (int t = 0; t < 3; t++) *(LAS f32x4*)(sm + L_EXQ + (tw ^ 1) * 3072 + t * 1024 + l * 16) = aq[1][t];
    SIG_SET(F_EX + (tw ^ 1), 2);
    SIG_ADD(F_P2);
    STAMP(12);
    SIG_WAIT(F_EX + tw, 2);
    SIG_WAIT(F_CSQ, 2);
    STAMP(13);
    const u32 slo0 = (rt * 128 + tw * 16 + l / 5) * 1280 + og * 80 + (l % 5) * 16;
    const u32 slo1 = (rt * 128 + tw * 16 + (64 + i16) / 5) * 1280 + og * 80 + ((64 + i16) % 5) * 16;
    SIG_WAIT(F_KR, 8);
    glds16(x, slo0, ldsb + L_V + tw * 1536);
    if (l < 16) glds16(x, slo1, ldsb + L_V + tw * 1536 + 1024);
    u32 pq[10];
    {
      f32x4 aqo[3], c2[3];
      u32 qn[3][2];
#pragma unroll
      for (int t = 0; t < 3; t++) {
        c2[t] = *(LAS f32x4*)(sm + L_CSQ + (16 * t + 4 * g) * 4) + *(LAS f32x4*)(sm + L_CSQ + (48 + 16 * t + 4 * g) * 4);
        aqo[t] = aq[0][t] + *(LAS f32x4*)(sm + L_EXQ + tw * 3072 + t * 1024 + l * 16);
        NRM(aqo[t], t, c2[t], qn);
      }
      u32x4 b32 = {qn[0][0], qn[0][1], qn[1][0], qn[1][1]};
      u32x2 b16 = {qn[2][0], qn[2][1]};
      f32x4 pp[3];
#pragma unroll
      for (int mt = 0; mt < 3; mt++) {
        pp[mt] = mfma32(wa32[mt], b32, f32x4{0, 0, 0, 0});
        pp[mt] = mfma16(wa16[mt], b16, pp[mt]);
      }
      softmax_pack(pp[0], pp[1], pp[2], pq);
#pragma unroll
      for (int k = 0; k < 10; k++) asm volatile("" : "+v"(pq[k]));
    }
    STAMP(14);
    u32 oad[3];
#pragma unroll
    for (int mt = 0; mt < 3; mt++) {
      const int col = mt < 2 ? 8 * q4 + 4 * mt + p4 : 32 + 2 * q4 + p4;
      const int hp = (mt == 2) ? (p4 == 2 ? 0 : 1) : (col >= 20 ? 1 : 0);
      const int row = (mt == 2 && p4 >= 2) ? hp * 21 + 20 : col + hp;
      oad[mt] = ((g >> 1) == hp) ? (u32)(L_KVT + row * 80 + 40 * (g & 1)) : L_ZERO;
      asm volatile("" : "+v"(oad[mt]));
    }
    u32x2 xv[3], xnp[3];
    asm volatile("s_waitcnt vmcnt(0)" ::: "memory");
    {
      LAS char* xp = sm + L_V + tw * 1536 + i16 * 80 + 8 * g;
#pragma unroll
      for (int t = 0; t < 3; t++) {
        xv[t] = *(LAS u32x2*)(xp + 32 * t);
        u32 d0 = pk2(bflo(xv[t][0]) - m_b, bfhi(xv[t][0]) - m_b), d1 = pk2(bflo(xv[t][1]) - m_b, bfhi(xv[t][1]) - m_b);
        xnp[t] = u32x2{pk2(bflo(d0) * rs, bfhi(d0) * rs), pk2(bflo(d1) * rs, bfhi(d1) * rs)};
        asm volatile("" : "+v"(xnp[t]));
      }
    }
    const u32 obase = WS_REG + (rt * 16 + 8 * kh) * RS + R_OB + tg * 2560;
    u32 oa[5];
#pragma unroll
    for (int k = 0; k < 5; k++) { int C = 64 * k + l, pr = C / 80; oa[k] = obase + pr * (RS - 1280) + C * 16; asm volatile("" : "+v"(oa[k])); }
    STAMP(15);
    SIG_WAIT(F_KVT, 1);
    STAMP(16);
    {
      f32x4 oo[3];
#pragma unroll
      for (int mt = 0; mt < 3; mt++) {
        LAS char* ap = sm + oad[mt];
        u32x2 a0 = *(LAS u32x2*)(ap), a1 = *(LAS u32x2*)(ap + 8), a2 = *(LAS u32x2*)(ap + 16), a3 = *(LAS u32x2*)(ap + 24), a4 = *(LAS u32x2*)(ap + 32);
        oo[mt] = mfma32(u32x4{a0[0], a0[1], a1[0], a1[1]}, u32x4{pq[0], pq[1], pq[2], pq[3]}, f32x4{0, 0, 0, 0});
        oo[mt] = mfma32(u32x4{a2[0], a2[1], a3[0], a3[1]}, u32x4{pq[4], pq[5], pq[6], pq[7]}, oo[mt]);
        oo[mt] = mfma16(a4, u32x2{pq[8], pq[9]}, oo[mt]);
      }
      const float r0 = rcp(oo[2][2] + 1e-6f), r1 = rcp(oo[2][3] + 1e-6f);
      const float ra_ = g <= 2 ? r0 : r1, rb_ = g <= 1 ? r0 : r1;
      u32x4 o8 = {pk2(oo[0][0] * ra_, oo[0][1] * ra_), pk2(oo[0][2] * ra_, oo[0][3] * ra_), pk2(oo[1][0] * rb_, oo[1][1] * rb_), pk2(oo[1][2] * rb_, oo[1][3] * rb_)};
      u32 o2 = pk2(oo[2][0] * r1, oo[2][1] * r1);
      const u32 ob = reg + R_OB + tw * 1280 + l * 16;
      ST4(o8, ob);
      auto ra = __builtin_amdgcn_permlane32_swap(o2, o2, false, false);
      auto rb = __builtin_amdgcn_permlane16_swap(ra[0], ra[0], false, false);
      auto rc2 = __builtin_amdgcn_permlane16_swap(ra[1], ra[1], false, false);
      if (g == 0) ST4((u32x4{rb[0], rb[1], rc2[0], rc2[1]}), ob + 1024);
      asm volatile("s_waitcnt vmcnt(0)" ::: "memory");
      SIG_ADD(F_OS);
    }
    MEMFENCE();
    STAMP(17);
    f32x4 ao[2][3];
    int idx;
    {
      LAS char* wop = sm + (kh ? L_WO1 : L_WO0) + (4 * g + q4) * 96 + 8 * p4;
      SIG_WAIT(F_WO, 2);
      SIG_WAIT(F_IDX, 1);
      idx = __builtin_amdgcn_readfirstlane((int)*(volatile LAS u32*)(sm + L_SYN + F_IDX * 4) - 1);
      SIG_WAIT(kh ? F_O1 : F_O, 1);
      STAMP(19);
      {
        u32x4 of[2][10];
#pragma unroll
        for (int j = 0; j < 10; j++) {
          of[0][j] = __builtin_amdgcn_raw_buffer_load_b128(wsr, oa[j % 5], (j / 5) * 4 * RS + kh * 1280, 16);
          of[1][j] = __builtin_amdgcn_raw_buffer_load_b128(wsr, oa[j % 5], (j / 5) * 4 * RS + (1 - kh) * 1280, 16);
        }
#pragma unroll
        for (int t = 0; t < 3; t++) { ao[0][t] = f32x4{0, 0, 0, 0}; ao[1][t] = f32x4{0, 0, 0, 0}; }
        u32x2 fa[6], fb[6];
#define STEPO(FA, FB, j) do { _Pragma("unroll") for (int k = 0; k < 6; k++) { ao[k & 1][k >> 1] = mfma32(FR(FA, k >> 1), of[k & 1][j], ao[k & 1][k >> 1]); \
            if ((j) < 9) RQ(FB, wop, ((j) + 1) * 3072, k); SB(); } } while (0)
#pragma unroll
        for (int r = 0; r < 6; r++) RQ(fa, wop, 0, r);
        SB();
#pragma unroll
        for (int j = 0; j < 10; j += 2) { STEPO(fa, fb, j); STEPO(fb, fa, j + 1); }
      }
    }
#pragma unroll
    for (int t = 0; t < 3; t++) *(LAS f32x4*)(sm + L_EXQ + (tw ^ 1) * 3072 + t * 1024 + l * 16) = ao[1][t];
    SIG_SET(F_EX + (tw ^ 1), 3);
    STAMP(20);
    SIG_WAIT(F_EX + tw, 3);
    STAMP(21);
    {
      LAS char* xp = sm + L_V + tw * 1536 + i16 * 80 + 8 * g;
      if ((idx >> 4) == rt * 8 + tw) SIG_WAIT(F_Y, 1);
      if (idx == rt * 128 + tw * 16 + i16) {
#pragma unroll
        for (int t = 0; t < 3; t++) if (t < 2 || g < 2) {
          f32x4 yy = *(LAS f32x4*)(sm + L_Y + (16 * t + 4 * g) * 4);
          xnp[t] = u32x2{pk2(bflo(xnp[t][0]) + yy[0], bfhi(xnp[t][0]) + yy[1]), pk2(bflo(xnp[t][1]) + yy[2], bfhi(xnp[t][1]) + yy[3])};
        }
      }
#pragma unroll
      for (int t = 0; t < 3; t++) {
        f32x4 af = ao[0][t] + *(LAS f32x4*)(sm + L_EXQ + tw * 3072 + t * 1024 + l * 16);
        u32 p0 = pk2(af[0], af[1]), p1 = pk2(af[2], af[3]);
        u32 q0 = pk2(bflo(xv[t][0]) + bflo(p0), bfhi(xv[t][0]) + bfhi(p0)), q1 = pk2(bflo(xv[t][1]) + bflo(p1), bfhi(xv[t][1]) + bfhi(p1));
        u32x2 res = {pk2(bflo(q0) + bflo(xnp[t][0]), bfhi(q0) + bfhi(xnp[t][0])), pk2(bflo(q1) + bflo(xnp[t][1]), bfhi(q1) + bfhi(xnp[t][1]))};
        if (t < 2 || g < 2) *(LAS u32x2*)(xp + 32 * t) = res;
      }
      __builtin_amdgcn_wave_barrier();
      LAS char* op = sm + L_V + tw * 1536 + l * 16;
      *(u32x4*)((char*)out + slo0) = *(LAS u32x4*)(op);
      if (l < 16) *(u32x4*)((char*)out + slo1) = *(LAS u32x4*)(op + 1024);
    }
    STAMP(22);
    }
  }
}

extern "C" void kernel_launch(void* const* d_in, const int* in_sizes, int n_in, void* d_out, int out_size,
                              void* d_ws, size_t ws_size, hipStream_t stream) {
  fused_kernel<<<256, 768, 0, stream>>>((const u16*)d_in[0], (const u16*)d_in[1], (const u16*)d_in[2], (const u16*)d_in[3],
                                        (const u16*)d_in[5], (const u16*)d_in[6], (const u16*)d_in[7], (const u16*)d_in[8],
                                        (u16*)d_out, (char*)d_ws);
}
```

```cpp
#include <hip/hip_runtime.h>
#include <stdint.h>

typedef unsigned short u16;
typedef unsigned int u32;
typedef unsigned long long u64;
typedef __attribute__((ext_vector_type(8))) __bf16 bf16x8;
typedef __attribute__((ext_vector_type(4))) short s16x4;
typedef __attribute__((ext_vector_type(4))) float f32x4;
typedef __attribute__((ext_vector_type(2))) float f32x2;
typedef __attribute__((ext_vector_type(2))) __bf16 bf16x2;
typedef __attribute__((ext_vector_type(4))) u32 u32x4;
typedef __attribute__((ext_vector_type(2))) u32 u32x2;
#define LAS __attribute__((address_space(3)))
#define DI __device__ __forceinline__

constexpr u32 WS_EPOCH = 0;
constexpr u32 WS_REG = 4096;
constexpr u32 RS = 23936;
constexpr u32 R_OB = 0;
constexpr u32 R_KVP = 10240;
constexpr u32 R_IA = 17024;
constexpr u32 R_IB = 19072;
constexpr u32 R_GUP = 19584;
constexpr u32 R_KVR = 20608;
constexpr u32 R_I1 = 20864;
constexpr u32 R_I3 = 20992;
constexpr u32 R_IG = 21120;
constexpr u32 R_IY = 21760;
constexpr u32 WS_YB = WS_REG + 256 * RS;
constexpr u32 WS_END = WS_YB + 655360;
constexpr u32 WS_STAMP = 128u << 20;

constexpr u32 L_W1 = 0;
constexpr u32 L_WQ = 0;
constexpr u32 L_WO0 = 30720;
constexpr u32 L_WO1 = 92160;
constexpr u32 L_EXQ = 61440;
constexpr u32 L_CSP = 102400;
constexpr u32 L_EXA = 103424;
constexpr u32 L_PF = 162816;
constexpr u32 L_PHI = 122880;
constexpr u32 L_V = 143360;
constexpr u32 L_SM = 155648;
constexpr u32 L_WPHI = L_SM;
constexpr u32 L_ZERO = L_SM + 1920;
constexpr u32 L_KVT = L_SM + 1984;
constexpr u32 L_CSQ = L_KVT + 3360;
constexpr u32 L_KEY = L_CSQ + 768;
constexpr u32 L_IDX = L_KEY + 64;
constexpr u32 L_Y = L_IDX + 64;
constexpr u32 L_MS = L_Y + 160;
constexpr u32 L_SYN = L_MS + 512;
enum { F_W0 = 0, F_W1, F_ST, F_CS, F_P1, F_Q0, F_Q1, F_PHI, F_KV, F_CSQ, F_P2, F_KVT, F_WO, F_OS, F_O, F_Y, F_EX = 16, F_WP = 24, F_KR, F_IDX, F_YG, F_YP, F_O1 };
constexpr u32 L_YS = L_PHI;
constexpr u32 L_TOTAL = 163840;

constexpr u32 SENT = 0xFFFFFFFFu;
constexpr u32 SPIN_CAP = 1u << 15;
#define HOP1_AT 6

DI u32 pk2(float a, float b) { f32x2 v = {a, b}; return __builtin_bit_cast(u32, __builtin_convertvector(v, bf16x2)); }
DI float bflo(u32 u) { return __uint_as_float(u << 16); }
DI float bfhi(u32 u) { return __uint_as_float(u & 0xFFFF0000u); }
DI float rbf(float a) { return bflo(pk2(a, 0.f)); }
DI float rcp(float a) { return __builtin_amdgcn_rcpf(a); }
DI float dot2(u32 a, u32 b, float c) { return __builtin_amdgcn_fdot2_f32_bf16(__builtin_bit_cast(bf16x2, a), __builtin_bit_cast(bf16x2, b), c, false); }
DI f32x4 mfma32(u32x4 a, u32x4 b, f32x4 c) {
  return __builtin_amdgcn_mfma_f32_16x16x32_bf16(__builtin_bit_cast(bf16x8, a), __builtin_bit_cast(bf16x8, b), c, 0, 0, 0);
}
DI f32x4 mfma16(u32x2 a, u32x2 b, f32x4 c) {
  return mfma32(u32x4{a[0], a[1], 0u, 0u}, u32x4{b[0], b[1], 0u, 0u}, c);
}
DI u32x2 trd(LAS char* p) { return __builtin_bit_cast(u32x2, __builtin_amdgcn_ds_read_tr16_b64_v4i16((LAS s16x4*)p)); }
DI u32x4 trd2(LAS char* p, u32 d) { u32x2 a = trd(p), b = trd(p + d); return u32x4{a[0], a[1], b[0], b[1]}; }
DI void glds16(const void* sbase, u32 voff, u32 lds_dst) {
  asm volatile("s_mov_b32 m0, %2\n\ts_nop 0\n\tglobal_load_lds_dwordx4 %0, %1" :: "v"(voff), "s"(sbase), "s"(lds_dst) : "memory");
}
DI u32 umin_(u32 a, u32 b) { return a < b ? a : b; }
DI u32 umax_(u32 a, u32 b) { return a > b ? a : b; }
DI u32 san(u32 v) { return umin_(v, 0xFFFFFFFEu); }
DI u32x4 san4(u32x4 v) { return u32x4{san(v[0]), san(v[1]), san(v[2]), san(v[3])}; }
DI u32 max4(u32x4 v) { return umax_(umax_(v[0], v[1]), umax_(v[2], v[3])); }
DI float xchg16_max(float v) { auto r = __builtin_amdgcn_permlane16_swap(__float_as_uint(v), __float_as_uint(v), false, false); return fmaxf(__uint_as_float(r[0]), __uint_as_float(r[1])); }
DI float xchg16_min(float v) { auto r = __builtin_amdgcn_permlane16_swap(__float_as_uint(v), __float_as_uint(v), false, false); return fminf(__uint_as_float(r[0]), __uint_as_float(r[1])); }
DI float xchg16_add(float v) { auto r = __builtin_amdgcn_permlane16_swap(__float_as_uint(v), __float_as_uint(v), false, false); return __uint_as_float(r[0]) + __uint_as_float(r[1]); }
DI float xchg32_add(float v) { auto r = __builtin_amdgcn_permlane32_swap(__float_as_uint(v), __float_as_uint(v), false, false); return __uint_as_float(r[0]) + __uint_as_float(r[1]); }
template <int CTRL> DI float dpp(float v) { return __int_as_float(__builtin_amdgcn_update_dpp(0, __float_as_int(v), CTRL, 0xf, 0xf, true)); }
template <int CTRL> DI u32 dppu(u32 v) { return (u32)__builtin_amdgcn_update_dpp(0, (int)v, CTRL, 0xf, 0xf, true); }
DI float wave_sum(float v) {
  v += dpp<0xB1>(v); v += dpp<0x4E>(v); v += dpp<0x141>(v); v += dpp<0x140>(v);
  return xchg32_add(xchg16_add(v));
}
DI float row_sum(float v) { v += dpp<0xB1>(v); v += dpp<0x4E>(v); v += dpp<0x141>(v); return v + dpp<0x140>(v); }
DI u32 row_max(u32 v) { v = umax_(v, dppu<0xB1>(v)); v = umax_(v, dppu<0x4E>(v)); v = umax_(v, dppu<0x141>(v)); return umax_(v, dppu<0x140>(v)); }
DI u32x4 ld4w(const char* base, u32 off) { u32x4 v; asm volatile("global_load_dwordx4 %0, %1, %2 sc1\n\ts_waitcnt vmcnt(0)" : "=&v"(v) : "v"(off), "s"(base) : "memory"); return v; }
#define MEMFENCE() asm volatile("" ::: "memory")
#define BARRIER() asm volatile("s_waitcnt lgkmcnt(0)\n\ts_barrier" ::: "memory")
#define SB() __builtin_amdgcn_sched_barrier(0)
#define LD4(off) __builtin_amdgcn_raw_buffer_load_b128(wsr, (off), 0, 16)
#define LD2(off) __builtin_amdgcn_raw_buffer_load_b64(wsr, (off), 0, 16)
#define ST4(v, off) __builtin_amdgcn_raw_buffer_store_b128((v), wsr, (off), 0, 16)
#define ST2(v, off) __builtin_amdgcn_raw_buffer_store_b64((v), wsr, (off), 0, 16)

#define STAMP(k) do { } while (0)

DI void softmax_pack(const f32x4& p0, const f32x4& p1, const f32x4& p2, u32 (&o)[10]) {
  float p[10] = {p0[0], p0[1], p0[2], p0[3], p1[0], p1[1], p1[2], p1[3], p2[0], p2[1]};
  float mx = p[0], mn = p[0];
#pragma unroll
  for (int i = 1; i < 10; i++) { mx = fmaxf(mx, p[i]); mn = fminf(mn, p[i]); }
  mx = xchg16_max(mx); mn = xchg16_min(mn);
  const float L2E = 1.4426950408889634f;
  const float a = -mx * L2E, b = mn * L2E;
  float e1[10], e2[10], s1 = 0.f, s2 = 0.f;
#pragma unroll
  for (int i = 0; i < 10; i++) {
    e1[i] = __builtin_amdgcn_exp2f(fmaf(p[i], L2E, a));
    e2[i] = __builtin_amdgcn_exp2f(fmaf(p[i], -L2E, b));
    s1 += e1[i]; s2 += e2[i];
  }
  s1 = xchg16_add(s1); s2 = xchg16_add(s2);
  const float r1 = rcp(s1), r2 = rcp(s2);
#pragma unroll
  for (int i = 0; i < 5; i++) {
    o[i] = pk2(e1[2 * i] * r1, e1[2 * i + 1] * r1);
    o[5 + i] = pk2(e2[2 * i] * r2, e2[2 * i + 1] * r2);
  }
}

#define THR "3"

#define SIG_SET(f, v) do { MEMFENCE(); *(volatile LAS u32*)(sm + L_SYN + (f) * 4) = (v); MEMFENCE(); } while (0)
DI void sig_add(u32 addr) {
  u64 keep; u32 one = 1;
  asm volatile("s_mov_b64 %0, exec\n\ts_mov_b64 exec, 1\n\tds_add_u32 %1, %2\n\ts_mov_b64 exec, %0" : "=&s"(keep) : "v"(addr), "v"(one) : "memory");
}
#define SIG_ADD(f) sig_add(ldsb + L_SYN + (f) * 4)
DI void sig_wait(u32 addr, u32 val) {
  u32 tv, ts, cnt;
  asm volatile(
      "s_mov_b32 %2, 0x400000\n"
      "LW%=:\n\t"
      "ds_read_b32 %0, %3\n\t"
      "s_waitcnt lgkmcnt(0)\n\t"
      "v_readfirstlane_b32 %1, %0\n\t"
      "s_nop 1\n\t"
      "s_cmp_ge_u32 %1, %4\n\t"
      "s_cbranch_scc1 LD%=\n\t"
      "s_sleep 1\n\t"
      "s_sub_u32 %2, %2, 1\n\t"
      "s_cmp_lg_u32 %2, 0\n\t"
      "s_cbranch_scc1 LW%=\n"
      "LD%=:"
      : "=&v"(tv), "=&s"(ts), "=&s"(cnt) : "v"(addr), "s"(val) : "memory", "scc");
}
#define SIG_WAIT(f, v) sig_wait(ldsb + L_SYN + (f) * 4, (v))
#define WPRIO 0
#define CLR_AT 1
#define PF_AT 0
#define KVD_AT 2
#define WO_FIRST 0
#define GW_AT 9
#define GTHR "40"
#define REC_AT 2
#define ST_AT 2
#define GW_SIG F_P1
#define H1D 8
#define H1S 4
#define XPRE 5
#define LPRIO 3
#define RAWBAR() asm volatile("s_barrier" ::: "memory")
__global__ __launch_bounds__(768, 3) void fused_kernel(
    const u16* __restrict__ x, const u16* __restrict__ wqkv, const u16* __restrict__ wphi, const u16* __restrict__ wo,
    const u16* __restrict__ wr, const u16* __restrict__ wg, const u16* __restrict__ wu, const u16* __restrict__ wd,
    u16* __restrict__ out, char* __restrict__ ws) {
  __shared__ __attribute__((aligned(1024))) char smem_[L_TOTAL];
  LAS char* sm = (LAS char*)smem_;
  const u32 ldsb = (u32)(size_t)smem_;
  STAMP(0);
  const int tid = threadIdx.x, l = tid & 63;
  const int w = __builtin_amdgcn_readfirstlane(tid >> 6);
  const int g = l >> 4, i16 = l & 15, q4 = i16 >> 2, p4 = l & 3;
  const int b = blockIdx.x, xcd = b & 7, slot = b >> 3;
  const int rt = (xcd >> 1) * 4 + (slot >> 3), hg = (xcd & 1) * 8 + (slot & 7);
  const int wgi = rt * 16 + hg;
  const int og = hg ^ 1;
  const int rot = (hg + 2 * rt) % 5;
  const auto wsr = __builtin_amdgcn_make_buffer_rsrc((void*)ws, 0, WS_END, 0x00020000);
  const u32 reg = WS_REG + wgi * RS;
#define CROT(c) ((c) + rot >= 5 ? (c) + rot - 5 : (c) + rot)
#define FR(F, t) (u32x4{F[2 * (t)][0], F[2 * (t)][1], F[2 * (t) + 1][0], F[2 * (t) + 1][1]})
#define LOADEP() u32x2 ep; { const u32 z = 0; asm volatile("global_load_dwordx2 %0, %1, %2 sc1" : "=v"(ep) : "v"(z), "s"(ws) : "memory"); }
#define GETTAG() u32 tag_lo, tag_hi; { u64 e = (((u64)ep[1] << 32) | ep[0]) + 1; tag_lo = __builtin_amdgcn_readfirstlane((u32)e); tag_hi = __builtin_amdgcn_readfirstlane((u32)(e >> 32)); }

  if (tid < 32) *(LAS u32*)(sm + L_SYN + tid * 4) = 0u;
  if (w >= 8) {
    const int h = w - 8;
    __builtin_amdgcn_s_setprio(3);
    int l_ = __builtin_amdgcn_mbcnt_hi(~0u, __builtin_amdgcn_mbcnt_lo(~0u, 0u));
    asm volatile("" : "+v"(l_));
    const int l = l_, tid = w * 64 + l, g = l >> 4, i16 = l & 15, q4 = i16 >> 2, p4 = l & 3;
    const u32x4 ONES = {0x3F803F80u, 0x3F803F80u, 0x3F803F80u, 0x3F803F80u};
#define YSUM(part) do { u32x4 yv[15]; _Pragma("unroll") for (int k = 0; k < 15; k++) { const int u = (90 * (part) + 6 * k) * 10 + l; \
        yv[k] = LD4((l < 60 && u < 2560) ? WS_YB + og * 40960 + u * 16 : WS_END); } \
      f32x4 s_ = {0, 0, 0, 0}; _Pragma("unroll") for (int k = 0; k < 15; k++) s_ += __builtin_bit_cast(f32x4, yv[k]); \
      if (l < 60) *(LAS f32x4*)(sm + L_YS + ((part) * 60 + l) * 16) = s_; SIG_ADD(F_YP); } while (0)
    if (h < 2) {
      u32 woff[5];
#pragma unroll
      for (int k = 0; k < 5; k++) {
        int P = 64 * k + l, rho = P / 10, s = P - 10 * rho;
        woff[k] = (32 * ((rho >> 2) & 1) + 16 * ((rho >> 3) & 1) + 4 * ((rho >> 4) & 1) + (rho & 3)) * 3840 + 1280 + (s >= 5 ? 1200 : 0) + hg * 80 + s * 16;
      }
      const char* wbase = (const char*)wqkv + h * (320 * 3840);
#define WL(c) do { const char* b_ = wbase + CROT(c) * (64 * 3840); _Pragma("unroll") for (int k = 0; k < 10; k++) \
        glds16(b_ + (k / 5) * (8 * 3840), woff[k % 5], ldsb + L_W1 + (4 * (c) + 2 * (k / 5) + h) * 5120 + (k % 5) * 1024); } while (0)
#define WDONE(N, c) do { asm volatile("s_waitcnt vmcnt(" #N ")" ::: "memory"); SIG_SET(F_W0 + h, (c) + 1); } while (0)
      WL(0);
      BARRIER();
      __builtin_amdgcn_s_setprio(1);
      WDONE(0, 0);
      WL(1);
      __builtin_amdgcn_s_setprio(LPRIO);
      STAMP(1);
      WL(2);
      STAMP(2);
      WDONE(10, 1); WL(3);
      STAMP(3);
      WDONE(10, 2); WL(4);
      STAMP(4);
      {
        const int q = slot * 100 + h * 50 + (l >> 3);
#pragma unroll
        for (int k = 0; k < 7; k++) {
          const int qq = q + (k < 6 ? 8 * k : 42), row = qq / 5, cc_ = qq - 5 * row;
          if (k == 0) WDONE(10, 3);
          glds16(wqkv, row * 3840 + (xcd & 1) * 640 + cc_ * 128 + (l & 7) * 16, ldsb + L_PF);
        }
      }
      WDONE(7, 4);
      STAMP(5);
      u32 qoff[3], ooff[3];
#pragma unroll
      for (int k = 0; k < 3; k++) {
        int P = 64 * k + l, rho = P / 6, s = P - 6 * rho; if (s > 4) s = 4;
        qoff[k] = (32 * ((rho >> 2) & 1) + 16 * ((rho >> 3) & 1) + 4 * ((rho >> 4) & 1) + (rho & 3)) * 3840 + hg * 80 + s * 16;
        ooff[k] = (8 * ((rho >> 2) & 3) + 4 * ((rho >> 4) & 1) + (rho & 3)) * 1280 + og * 80 + s * 16;
      }
      u32x4 wr_[30];
#define OROT(k) (2 * CROT((k) / 6) + ((k) / 3 & 1))
#pragma unroll
      for (int k = 0; k < WO_FIRST; k++)
        asm volatile("global_load_dwordx4 %0, %1, %2" : "=v"(wr_[k]) : "v"(ooff[k % 3]), "s"((const char*)wo + (10 * h + OROT(k)) * 40960));
      SIG_WAIT(F_P1, 9);
      STAMP(6);
#pragma unroll 1
      for (int c = 0; c < 5; c++) {
        const char* b_ = wbase + CROT(c) * (64 * 3840);
#pragma unroll
        for (int k = 0; k < 6; k++) glds16(b_ + (k / 3) * (8 * 3840), qoff[k % 3], ldsb + L_WQ + (4 * c + 2 * (k / 3) + h) * 3072 + (k % 3) * 1024);
      }
      STAMP(7);
      asm volatile("s_waitcnt vmcnt(0)" ::: "memory");
      SIG_SET(F_Q0 + h, 1);
      STAMP(8);
      __builtin_amdgcn_s_setprio(0);
      {
        f32x4 cq[3];
#pragma unroll
        for (int t = 0; t < 3; t++) cq[t] = f32x4{0, 0, 0, 0};
        LAS char* wqp = sm + L_WQ + h * 3072 + (4 * g + q4) * 96 + 8 * p4;
#pragma unroll 1
        for (int k = 0; k < 10; k++)
#pragma unroll
          for (int t = 0; t < 3; t++) cq[t] = mfma32(ONES, trd2(wqp + k * 6144 + 32 * t, 1536), cq[t]);
#pragma unroll
        for (int t = 0; t < 3; t++) *(LAS float*)(sm + L_CSQ + (48 * h + 16 * t + i16) * 4) = cq[t][0];
        SIG_ADD(F_CSQ);
        SIG_ADD(F_P2);
      }
#pragma unroll
      for (int k = WO_FIRST; k < 30; k++)
        asm volatile("global_load_dwordx4 %0, %1, %2" : "=v"(wr_[k]) : "v"(ooff[k % 3]), "s"((const char*)wo + (10 * h + OROT(k)) * 40960));
      STAMP(9);
      SIG_WAIT(h ? F_KR : F_P2, h ? 8 : 10);
      asm volatile("s_waitcnt vmcnt(0)" ::: "memory");
      STAMP(10);
      __builtin_amdgcn_s_setprio(0);
#pragma unroll
      for (int k = 0; k < 30; k++) {
        asm volatile("" : "+v"(wr_[k]));
        *(LAS u32x4*)(sm + (h ? L_WO1 : L_WO0) + OROT(k) * 3072 + (k % 3) * 1024 + l * 16) = wr_[k];
      }
      SIG_ADD(F_WO);
      STAMP(12);
      __builtin_amdgcn_s_setprio(3);
      SIG_WAIT(F_IDX, 1);
      if ((((int)*(volatile LAS u32*)(sm + L_SYN + F_IDX * 4) - 1) >> 7) == rt) { SIG_WAIT(F_YG, 1); YSUM(h); }
    } else {
      const int e = h - 2;
#define STATS_LD() u32x4 sx[5], sw[5]; { const u32 o = (4 * e + g) * 1280 + i16 * 16, o2 = i16 * 16; \
          _Pragma("unroll") for (int k = 0; k < 5; k++) { asm volatile("global_load_dwordx4 %0, %1, %2 offset:%3" : "=v"(sx[k]) : "v"(o), "s"(x + wgi * 8 * 640), "i"(k * 256)); \
            asm volatile("global_load_dwordx4 %0, %1, %2 offset:%3" : "=v"(sw[k]) : "v"(o2), "s"(wr), "i"(k * 256)); } }
#define STATS() do { _Pragma("unroll") for (int k = 0; k < 5; k++) asm volatile("" : "+v"(sx[k]), "+v"(sw[k])); \
          float s = 0.f, q = 0.f; \
          _Pragma("unroll") for (int k = 0; k < 20; k++) { s = dot2(sx[k >> 2][k & 3], 0x3F803F80u, s); q = dot2(sx[k >> 2][k & 3], sx[k >> 2][k & 3], q); } \
          const float mean = row_sum(s) * (1.f / 640.f); \
          const float var = fmaxf(row_sum(q) * (1.f / 640.f) - mean * mean, 0.f); \
          const float mb = rbf(mean), sb = rbf(__builtin_amdgcn_sqrtf(rbf(rbf(var) + 1e-5f))), rsb = rcp(sb); \
          const u32 ms = pk2(mb, sb); const int srow = wgi * 8 + 4 * e + g; \
          float r = 0.f; \
          _Pragma("unroll") for (int k = 0; k < 20; k++) { const u32 xd = sx[k >> 2][k & 3]; \
            u32 d = pk2(bflo(xd) - mb, bfhi(xd) - mb), n = pk2(bflo(d) * rsb, bfhi(d) * rsb); r = dot2(n, sw[k >> 2][k & 3], r); } \
          r = row_sum(r); \
          const u32 rb = pk2(r, 0.f) & 0xFFFFu; \
          const u32 ord = (rb & 0x8000u) ? (~rb & 0xFFFFu) : (rb | 0x8000u); \
          SIG_ADD(F_ST); SIG_WAIT(F_ST, 2);     \
          ST4((u32x4{ms, tag_lo, (ord << 16) | (u32)(0x7FFF - srow), tag_hi}), WS_REG + (rt * 16 + i16) * RS + R_IA + (srow & 127) * 16); } while (0)
      if (e == 1) {
        LOADEP();
        STATS_LD();
        BARRIER();
        if (l < 14) ST4((u32x4{SENT, SENT, SENT, SENT}), reg + R_KVR + l * 16);
        asm volatile("s_waitcnt vmcnt(1)" : "+v"(ep) :: "memory");
        GETTAG();
        STAMP(1);
        STATS();
#pragma unroll
        for (int k = 0; k < 7; k++) ST4((u32x4{SENT, SENT, SENT, SENT}), (k * 64 + l < 420) ? reg + R_KVP + (k * 64 + l) * 16 : WS_END);
        STAMP(2);
        f32x4 cs[5];
#pragma unroll
        for (int t = 0; t < 5; t++) cs[t] = f32x4{0, 0, 0, 0};
        LAS char* w1p = sm + L_W1 + (4 * g + q4) * 160 + 8 * p4;
#pragma unroll 1
        for (int c = 0; c < 5; c++) {
          SIG_WAIT(F_W0, c + 1); SIG_WAIT(F_W1, c + 1);
#pragma unroll
          for (int k = 0; k < 4; k++)
#pragma unroll
            for (int t = 0; t < 5; t++) cs[t] = mfma32(ONES, trd2(w1p + (4 * c + k) * 5120 + 32 * t, 2560), cs[t]);
          if (c == 2) SIG_ADD(F_P1);
        }
        STAMP(3);
        asm volatile("s_waitcnt vmcnt(0)" ::: "memory");
#pragma unroll
        for (int t = 0; t < 5; t++) *(LAS float*)(sm + L_CSP + (16 * t + i16) * 4) = cs[t][0];
        SIG_ADD(F_CS);
        STAMP(4);
        if (l < 16) ST2((u32x2{tag_lo, tag_hi}), WS_REG + (l * 16 + hg) * RS + R_I1 + rt * 8);
        STAMP(11);
        SIG_WAIT(F_PHI, 8);
        STAMP(5);
#define HINT(addr) do { _Pragma("unroll 1") for (u32 spin = 0; spin < SPIN_CAP; spin++) { u32x4 f_ = ld4w(ws, (addr) + (l & 7) * 16); \
          if (__all(f_[0] == tag_lo && f_[1] == tag_hi && f_[2] == tag_lo && f_[3] == tag_hi)) break; } } while (0)
        HINT(reg + R_I1);
        SIG_WAIT(F_KR, 8);
        __builtin_amdgcn_s_sleep(H1D);
        STAMP(6);
        {
          const int he = l >= 28 ? l - 28 : l;
          const bool ok = (l < 56) && (rt < 15);
          const u32 h1a = ok ? WS_REG + ((l >= 28 ? 128 : 0) + hg) * RS + R_KVP + (28 * rt + he) * 16 : WS_END;
          u32x4 h1[8];
#pragma unroll 1
          for (u32 spin = 0; spin < SPIN_CAP; spin++) {
            MEMFENCE();
            u32 m = 0;
#pragma unroll
            for (int k = 0; k < 8; k++) h1[k] = __builtin_amdgcn_raw_buffer_load_b128(wsr, h1a, k * 16 * RS, 16);
#pragma unroll
            for (int k = 0; k < 8; k++) m = umax_(m, max4(h1[k]));
            if (__all(m != SENT)) break;
            __builtin_amdgcn_s_sleep(H1S);
          }
          f32x4 s = __builtin_bit_cast(f32x4, h1[0]);
#pragma unroll
          for (int k = 1; k < 8; k++) s += __builtin_bit_cast(f32x4, h1[k]);
#pragma unroll
          for (int r = 0; r < 4; r++) s[r] += __shfl(s[r], (l + 28) & 63);
          if (ok && l < 28) ST2((u32x2{san(pk2(s[0], s[1])), san(pk2(s[2], s[3]))}), reg + R_KVR + l * 8);
        }
        STAMP(7);
        {
          const int hpr = l / 14, e2 = l - 14 * hpr;
          const u32 h2a = WS_REG + (hpr * 16 + hg) * RS + R_KVR + e2 * 16;
          u32 ka[8];
#pragma unroll
          for (int k = 0; k < 8; k++) { int it = 112 * (k >> 1) + 2 * l + (k & 1), hf = it / 21, c = it - 21 * hf; ka[k] = L_KVT + c * 80 + hf * 8 + (hf >= 10 ? 1600 : 0); }
          u32x4 kv[4];
#pragma unroll 1
          for (u32 spin = 0; spin < SPIN_CAP; spin++) {
            MEMFENCE();
            u32 m = 0;
#pragma unroll
            for (int k = 0; k < 4; k++) kv[k] = LD4((l < 56 && 4 * k + hpr < 15) ? h2a + k * 64 * RS : WS_END);
#pragma unroll
            for (int k = 0; k < 4; k++) m = umax_(m, max4(kv[k]));
            if (__all(m != SENT)) break;
          }
#pragma unroll
          for (int k = 0; k < 4; k++) if (l < 56 && 4 * k + hpr < 15) {
            *(LAS u32x2*)(sm + ka[2 * k]) = u32x2{kv[k][0], kv[k][1]};
            *(LAS u32x2*)(sm + ka[2 * k + 1]) = u32x2{kv[k][2], kv[k][3]};
          }
        }
        SIG_SET(F_KVT, 1);
        STAMP(8);
        SIG_WAIT(F_OS, 8);
        if (l < 16) ST2((u32x2{tag_lo, tag_hi}), WS_REG + (rt * 16 + l) * RS + R_I3 + hg * 8);
        STAMP(9);
#pragma unroll 1
        for (u32 spin = 0; spin < SPIN_CAP; spin++) {
          u32x4 f_ = ld4w(ws, reg + R_I3 + (l & 7) * 16);
          const u64 okm = __ballot(f_[0] == tag_lo && f_[1] == tag_hi && f_[2] == tag_lo && f_[3] == tag_hi);
          if ((okm & 0xF) == 0xF) SIG_SET(F_O, 1);
          if ((okm & 0xF0) == 0xF0) SIG_SET(F_O1, 1);
          if ((okm & 0xFF) == 0xFF) break;
        }
        SIG_SET(F_O, 1); SIG_SET(F_O1, 1);
        STAMP(10);
      } else {
        LOADEP();
        STATS_LD();
        u32 wp[7];
#pragma unroll
        for (int k = 0; k < 7; k++) { const u32 o = hg * 1600 + (l + 64 * k < 400 ? l + 64 * k : 0) * 4; asm volatile("global_load_dword %0, %1, %2" : "=v"(wp[k]) : "v"(o), "s"(wphi)); }
        BARRIER();
        if (l < 4) *(LAS u32x4*)(sm + L_ZERO + l * 16) = u32x4{0, 0, 0, 0};
        if (l < 40) { *(LAS u32*)(sm + L_WPHI + l * 48 + 20) = 0u; *(LAS u32*)(sm + L_WPHI + l * 48 + 44) = 0u; }
        asm volatile("s_waitcnt vmcnt(0)" : "+v"(ep), "+v"(wp[0]), "+v"(wp[1]), "+v"(wp[2]), "+v"(wp[3]), "+v"(wp[4]), "+v"(wp[5]), "+v"(wp[6]) :: "memory");
        GETTAG();
        STATS();
#pragma unroll
        for (int k = 0; k < 7; k++) { const int u = l + 64 * k, row = u / 10, z = u - 10 * row; if (u < 400) *(LAS u32*)(sm + L_WPHI + row * 48 + z * 4 + (z >= 5 ? 4 : 0)) = wp[k]; }
        SIG_SET(F_WP, 1);
        STAMP(1);
        const int cb = wgi < 240 ? wgi / 20 : 0, ksp = wgi < 240 ? wgi - 20 * cb : 0;
        u32x2 rec;
        {
          u32x4 s0, s1; const u32 sta = reg + R_IA + l * 16;
#pragma unroll 1
          for (u32 spin = 0; spin < SPIN_CAP; spin++) {
            __builtin_amdgcn_s_sleep(8);
            asm volatile("global_load_dwordx4 %0, %2, %3 sc1\n\tglobal_load_dwordx4 %1, %2, %3 offset:1024 sc1\n\ts_waitcnt vmcnt(0)" : "=&v"(s0), "=&v"(s1) : "v"(sta), "s"(ws) : "memory");
            if (__all(s0[1] == tag_lo && s0[3] == tag_hi && s1[1] == tag_lo && s1[3] == tag_hi)) break;
          }
          *(LAS u32*)(sm + L_MS + l * 4) = s0[0];
          *(LAS u32*)(sm + L_MS + 256 + l * 4) = s1[0];
          SIG_ADD(F_CS);
          u32 km = umax_(s0[2], s1[2]), ms = s0[2] > s1[2] ? s0[0] : s1[0];
          u32 kx = row_max(km);
          { auto r = __builtin_amdgcn_permlane16_swap(kx, kx, false, false); kx = umax_(r[0], r[1]); }
          { auto r = __builtin_amdgcn_permlane32_swap(kx, kx, false, false); kx = umax_(r[0], r[1]); }
          ms = __builtin_amdgcn_readlane(ms, __builtin_ctzll(__ballot(km == kx)));
          if (l < 16) ST4((u32x4{ms, tag_lo, kx, tag_hi}), WS_REG + (l * 16 + hg) * RS + R_IB + rt * 16);
#pragma unroll 1
          for (u32 spin = 0; spin < SPIN_CAP; spin++) {
            __builtin_amdgcn_s_sleep(8);
            s0 = ld4w(ws, reg + R_IB + i16 * 16);
            if (__all(s0[1] == tag_lo && s0[3] == tag_hi)) break;
          }
          kx = row_max(s0[2]);
          rec = u32x2{(u32)__builtin_amdgcn_readfirstlane(kx), (u32)__builtin_amdgcn_readlane(s0[0], __builtin_ctzll(__ballot(s0[2] == kx)))};
        }
        const int idx = (0x7FFF - (rec[0] & 0xFFFFu)) & 2047;
        if (b == 0 && l == 0) ST2((u32x2{tag_lo, tag_hi}), WS_EPOCH);
        SIG_SET(F_IDX, idx + 1);
        u32 xr;
        { const u32 xo = (32 * ksp) * 2 + i16 * 4; asm volatile("global_load_dword %0, %1, %2" : "=v"(xr) : "v"(xo), "s"(x + idx * 640)); }
        STAMP(4);
        u32x4 gw[16]; u32x2 dw[18];
        __builtin_amdgcn_s_setprio(0);
        SIG_WAIT(GW_SIG, GW_AT);
        STAMP(2);
        {
          const u16* gm = cb < 6 ? wg + cb * 256 : wu + (cb - 6) * 256;
          const u32 go = (32 * ksp + (l >> 5)) * 3072 + (l & 31) * 16;
#pragma unroll
          for (int k = 0; k < 16; k++) asm volatile("global_load_dwordx4 %0, %1, %2\n\ts_waitcnt vmcnt(" GTHR ")" : "=v"(gw[k]) : "v"(go), "s"(gm + k * 2 * 1536));
        }
        {
          const u16* dp = wd + (6 * wgi) * 640;
          const u32 dof = l * 8, dof2 = (l < 32 ? l : 0) * 8;
#pragma unroll
          for (int k = 0; k < 6; k++) {
            asm volatile("global_load_dwordx2 %0, %1, %2" : "=v"(dw[3 * k]) : "v"(dof), "s"(dp + k * 640));
            asm volatile("global_load_dwordx2 %0, %1, %2 offset:512" : "=v"(dw[3 * k + 1]) : "v"(dof), "s"(dp + k * 640));
            asm volatile("global_load_dwordx2 %0, %1, %2 offset:1024" : "=v"(dw[3 * k + 2]) : "v"(dof2), "s"(dp + k * 640));
          }
        }
        __builtin_amdgcn_s_setprio(3);
        STAMP(3);
        {
          asm volatile("s_waitcnt vmcnt(18)" : "+v"(xr) :: "memory");
          STAMP(5);
#pragma unroll
          for (int k = 0; k < 16; k++) asm volatile("" : "+v"(gw[k]));
          const float sel_m = bflo(rec[1]), sel_r = rcp(bfhi(rec[1]));
          { u32 d = pk2(bflo(xr) - sel_m, bfhi(xr) - sel_m); xr = pk2(bflo(d) * sel_r, bfhi(d) * sel_r); }
          const float xa = (l >> 5) ? bfhi(xr) : bflo(xr);
          float v[8];
#pragma unroll
          for (int k = 0; k < 8; k++) v[k] = 0.f;
#define M1ROW(r) do { const float a = dpp<0x150 + r>(xa); _Pragma("unroll") for (int k = 0; k < 4; k++) { v[2 * k] = fmaf(a, bflo(gw[r][k]), v[2 * k]); v[2 * k + 1] = fmaf(a, bfhi(gw[r][k]), v[2 * k + 1]); } } while (0)
          M1ROW(0); M1ROW(1); M1ROW(2); M1ROW(3); M1ROW(4); M1ROW(5); M1ROW(6); M1ROW(7);
          M1ROW(8); M1ROW(9); M1ROW(10); M1ROW(11); M1ROW(12); M1ROW(13); M1ROW(14); M1ROW(15);
          u32x4 u4;
#pragma unroll
          for (int k = 0; k < 4; k++) {
            auto r = __builtin_amdgcn_permlane32_swap(__float_as_uint(v[k]), __float_as_uint(v[k + 4]), false, false);
            u4[k] = __float_as_uint(__uint_as_float(r[0]) + __uint_as_float(r[1]));
          }
          ST4(u4, wgi < 240 ? reg + R_GUP + (l & 31) * 32 + (l >> 5) * 16 : WS_END);
          asm volatile("s_waitcnt vmcnt(0)" ::: "memory");
          const int c0 = (cb % 6) * 256, d = c0 / 6 + l;
          if (wgi < 240 && 6 * d < c0 + 256) ST2((u32x2{tag_lo, tag_hi}), WS_REG + d * RS + R_IG + ((6 * d < c0 ? 40 : 0) + (cb / 6) * 20 + ksp) * 8);
        }
        STAMP(6);
        STAMP(7);
        {
          const int nsl = ((6 * wgi) >> 8) == ((6 * wgi + 5) >> 8) ? 20 : 40;
#pragma unroll 1
          for (u32 spin = 0; spin < SPIN_CAP; spin++) {
            u32x4 f0 = ld4w(ws, reg + R_IG + (l < nsl ? l : 0) * 16);
            if (__all(f0[0] == tag_lo && f0[1] == tag_hi && f0[2] == tag_lo && f0[3] == tag_hi)) break;
            __builtin_amdgcn_s_sleep(6);
          }
        }
        STAMP(8);
        {
          const int kw = l / 3, pr = l - 3 * kw, col = 6 * wgi + 2 * pr;
          const u32 ga = l < 60 ? WS_REG + ((col >> 8) * 20 + kw) * RS + R_GUP + (col & 255) * 4 : WS_END;
          f32x2 gg2 = __builtin_bit_cast(f32x2, LD2(ga)), uu2 = __builtin_bit_cast(f32x2, __builtin_amdgcn_raw_buffer_load_b64(wsr, ga, 120 * RS, 16));
          float z[4] = {gg2[0], gg2[1], uu2[0], uu2[1]};
#pragma unroll
          for (int k = 0; k < 4; k++) {
            z[k] += __shfl(z[k], (l + 30) & 63);
            z[k] += __shfl(z[k], (l + 15) & 63);
            const float z0 = z[k];
            z[k] = z0 + __shfl(z0, (l + 3) & 63) + __shfl(z0, (l + 6) & 63) + __shfl(z0, (l + 9) & 63) + __shfl(z0, (l + 12) & 63);
          }
          const int vm = l >= 6 ? 1 : 0, vc = l - 6 * vm;
          const float s0 = __shfl(z[0], vc >> 1), s1 = __shfl(z[1], vc >> 1), s2 = __shfl(z[2], vc >> 1), s3 = __shfl(z[3], vc >> 1);
          const float tot = vm ? ((vc & 1) ? s3 : s2) : ((vc & 1) ? s1 : s0);
          float gg = rbf(tot);
          float uu = __shfl(gg, (l + 6) & 63);
          float sg = rbf(gg * rcp(1.f + __expf(-gg)));
          float hv = rbf(sg * uu);
#pragma unroll
          for (int k = 0; k < 18; k++) asm volatile("" : "+v"(dw[k]));
#pragma unroll
          for (int z = 0; z < 3; z++) {
            f32x4 y = {0, 0, 0, 0};
#pragma unroll
            for (int i = 0; i < 6; i++) {
              float hh = __int_as_float(__builtin_amdgcn_readlane(__float_as_int(hv), i));
              y[0] += hh * bflo(dw[3 * i + z][0]); y[1] += hh * bfhi(dw[3 * i + z][0]); y[2] += hh * bflo(dw[3 * i + z][1]); y[3] += hh * bfhi(dw[3 * i + z][1]);
            }
            { const int u = 64 * z + l, sl = u / 10; ST4(__builtin_bit_cast(u32x4, y), u < 160 ? WS_YB + (sl * 256 + wgi) * 160 + (u - 10 * sl) * 16 : WS_END); }
          }
          asm volatile("s_waitcnt vmcnt(0)" ::: "memory");
          if (l < 16) ST2((u32x2{tag_lo, tag_hi}), WS_REG + ((idx >> 7) * 16 + l) * RS + R_IY + wgi * 8);
        }
        STAMP(9);
        if ((idx >> 7) == rt) {
#pragma unroll 1
          for (u32 spin = 0; spin < SPIN_CAP; spin++) {
            u32x4 f0, f1; const u32 fa_ = reg + R_IY + l * 16;
            asm volatile("global_load_dwordx4 %0, %2, %3 sc1\n\tglobal_load_dwordx4 %1, %2, %3 offset:1024 sc1\n\ts_waitcnt vmcnt(0)" : "=&v"(f0), "=&v"(f1) : "v"(fa_), "s"(ws) : "memory");
            if (__all(f0[0] == tag_lo && f0[1] == tag_hi && f0[2] == tag_lo && f0[3] == tag_hi && f1[0] == tag_lo && f1[1] == tag_hi && f1[2] == tag_lo && f1[3] == tag_hi)) break;
            __builtin_amdgcn_s_sleep(2);
          }
          SIG_SET(F_YG, 1);
          SIG_WAIT(F_KR, 8);
          YSUM(2);
          SIG_WAIT(F_YP, 3);
          f32x4 t = {0, 0, 0, 0};
#pragma unroll
          for (int k = 0; k < 18; k++) t += *(LAS f32x4*)(sm + L_YS + (10 * k + (l < 10 ? l : 0)) * 16);
          u32 rb = rec[0] >> 16;
          rb = (rb & 0x8000u) ? (rb & 0x7FFFu) : (~rb & 0xFFFFu);
          const float sig = rbf(rcp(1.f + __expf(-bflo(rb))));
          if (l < 10) *(LAS f32x4*)(sm + L_Y + l * 16) = f32x4{rbf(rbf(t[0]) * sig), rbf(rbf(t[1]) * sig), rbf(rbf(t[2]) * sig), rbf(rbf(t[3]) * sig)};
        }
        SIG_SET(F_Y, idx + 1);
        STAMP(10);
      }
    }
  } else {
    const int tg = w & 3, kh = w >> 2, tw = 2 * tg + kh;
    u32x4 xf[20];
    const char* xb = (const char*)x + rt * (128 * 1280) + kh * 640;
    const u32 xoff = (u32)((((g >> 1) ? (tw ^ 1) : tw) * 16 + 4 * q4) * 1280 + (g & 1) * 64 + p4 * 16);
#define XI(c, r) ((r) < 2 ? 2 * (c) + (r) : 8 + 2 * (c) + (r))
#define XL1(c, r) asm volatile("global_load_dwordx4 %0, %1, %2 offset:%3" : "=v"(xf[XI(c, r)]) : "v"(xoff), "s"(xb + CROT(c) * 128), "i"((r) * 1280))
#define XG(c) do { XL1(c, 0); XL1(c, 1); XL1(c, 2); XL1(c, 3); } while (0)
    __builtin_amdgcn_s_setprio(2);
    XG(0);
    BARRIER();
    XG(1); XG(2);
    XG(3);
    XG(4);
    STAMP(1);
    f32x4 acc[2][5];
#pragma unroll
    for (int t = 0; t < 5; t++) { acc[0][t] = f32x4{0, 0, 0, 0}; acc[1][t] = f32x4{0, 0, 0, 0}; }
    LAS char* w1p = sm + L_W1 + kh * 5120 + (4 * g + q4) * 160 + 8 * p4;
    const u64 mE = 0x5555555555555555ull, mO = ~mE, mL = 0x3333333333333333ull, mH = ~mL;
    u32 ta[4], ua[4];
#define DPX1 " quad_perm:[1,0,3,2] row_mask:0xf bank_mask:0xf\n\t"
#define DPX2 " quad_perm:[2,3,0,1] row_mask:0xf bank_mask:0xf\n\t"
#define CD2(M, X, d0, s0, k0, d1, s1, k1) asm volatile("s_mov_b64 vcc, %6\n\tv_cndmask_b32_dpp %0, %2, %3, vcc" X "v_cndmask_b32_dpp %1, %4, %5, vcc" X \
      : "=&v"(d0), "=&v"(d1) : "v"(s0), "v"(k0), "v"(s1), "v"(k1), "s"(M) : "vcc")
#define CD2I(M, X, d0, s0, d1, s1) asm volatile("s_mov_b64 vcc, %4\n\tv_cndmask_b32_dpp %0, %2, %0, vcc" X "v_cndmask_b32_dpp %1, %3, %1, vcc" X \
      : "+v"(d0), "+v"(d1) : "v"(s0), "v"(s1), "s"(M) : "vcc")
#define A_(c, r) xf[XI(c, r)]
#define TP(c, k) do { const int d_ = (k) & 3; u32 n0, n1; \
      if ((k) < 4) { CD2(mE, DPX1, n0, A_(c, 1)[d_], A_(c, 0)[d_], n1, A_(c, 3)[d_], A_(c, 2)[d_]); ua[d_] = n0; ta[d_] = n1; } \
      else if ((k) < 8) { n0 = A_(c, 1)[d_]; n1 = A_(c, 3)[d_]; CD2I(mO, DPX1, n0, A_(c, 0)[d_], n1, A_(c, 2)[d_]); A_(c, 1)[d_] = n0; A_(c, 3)[d_] = n1; } \
      else if ((k) < 12) { CD2(mL, DPX2, n0, ta[d_], ua[d_], n1, A_(c, 3)[d_], A_(c, 1)[d_]); A_(c, 0)[d_] = n0; A_(c, 2)[d_] = n1; } \
      else if ((k) < 16) { n1 = A_(c, 3)[d_]; CD2I(mH, DPX2, ta[d_], ua[d_], n1, A_(c, 1)[d_]); A_(c, 3)[d_] = n1; } \
      else { auto r0 = __builtin_amdgcn_permlane32_swap(A_(c, 0)[d_], ta[d_], false, false); auto r1 = __builtin_amdgcn_permlane32_swap(A_(c, 2)[d_], A_(c, 3)[d_], false, false); \
        A_(c, 0)[d_] = r0[0]; A_(c, 2)[d_] = r0[1]; A_(c, 1)[d_] = r1[0]; A_(c, 3)[d_] = r1[1]; } } while (0)
#define XW(N, c) asm volatile("s_waitcnt vmcnt(" #N ")" : "+v"(A_(c, 0)), "+v"(A_(c, 1)), "+v"(A_(c, 2)), "+v"(A_(c, 3)) :: "memory")
#define RD(F, j, r) F[r] = trd(w1p + (j) * 10240 + 32 * ((r) >> 1) + 2560 * ((r) & 1))
#define MF(F, j, k) acc[(k) & 1][(k) >> 1] = mfma32(FR(F, (k) >> 1), xf[10 * ((k) & 1) + (j)], acc[(k) & 1][(k) >> 1])
#define STEPA(FA, FB, c, V) do { _Pragma("unroll") for (int k = 0; k < 10; k++) { MF(FA, 2 * (c), k); RD(FB, 2 * (c) + 1, k); if ((c) < 4) TP((c) + 1, k); V(k); SB(); } } while (0)
#define STEPB(FA, FB, c) do { _Pragma("unroll") for (int k = 0; k < 4; k++) { MF(FB, 2 * (c) + 1, k); TP((c) + 1, 10 + k); SB(); } \
      if ((c) == 2) SIG_ADD(F_P1); \
      SIG_WAIT(F_W0 + kh, (c) + 2); \
      _Pragma("unroll") for (int k = 4; k < 10; k++) { if (k < 9) { RD(FA, 2 * (c) + 2, 2 * (k - 4)); RD(FA, 2 * (c) + 2, 2 * (k - 4) + 1); } MF(FB, 2 * (c) + 1, k); TP((c) + 1, 10 + k); SB(); } } while (0)
    {
      u32x2 fa[10], fb[10];
      XW(16, 0);
      u32 fil = l;
#pragma unroll
      for (int k = 0; k < 20; k++) { TP(0, k); asm volatile("v_add_u32 %0, 1, %0" : "+v"(fil)); SB(); }
      SIG_WAIT(F_W0 + kh, 1);
      STAMP(2);
#pragma unroll
      for (int r = 0; r < 10; r++) RD(fa, 0, r);
      SB();
#define V3(k) do { if ((k) == 1) XL1(3, 0); if ((k) == 3) XL1(3, 1); if ((k) == 5) XL1(3, 2); if ((k) == 7) XL1(3, 3); } while (0)
#define V4(k) do { if ((k) == 1) XL1(4, 0); if ((k) == 3) XL1(4, 1); if ((k) == 5) XL1(4, 2); if ((k) == 7) XL1(4, 3); } while (0)
#define VN(k) do { } while (0)
      XW(12, 1);
      STEPA(fa, fb, 0, VN);
      STEPB(fa, fb, 0);
      STAMP(3);
      XW(8, 2);
      STEPA(fa, fb, 1, VN);
      STEPB(fa, fb, 1);
      STAMP(4);
      XW(4, 3);
      STEPA(fa, fb, 2, VN); STEPB(fa, fb, 2);
      STAMP(5);
      XW(0, 4);
      STEPA(fa, fb, 3, VN); STEPB(fa, fb, 3);
      STAMP(6);
      STEPA(fa, fb, 4, VN);
#pragma unroll
      for (int k = 0; k < 10; k++) MF(fb, 9, (k + 1 - 2 * (k & 1)));
    }
    STAMP(7);
    {
    int l_ = __builtin_amdgcn_mbcnt_hi(~0u, __builtin_amdgcn_mbcnt_lo(~0u, 0u));
    asm volatile("" : "+v"(l_));
    const int l = l_, tid = w * 64 + l, g = l >> 4, i16 = l & 15, q4 = i16 >> 2, p4 = l & 3;
    {
      const int tp = tw ^ 1;
      *(LAS f32x4*)(sm + L_PHI + tp * 2560 + l * 16) = acc[1][0];
      *(LAS f32x4*)(sm + L_PHI + tp * 2560 + 1024 + l * 16) = acc[1][1];
      *(LAS f32x4*)(sm + L_V + tp * 1536 + l * 16) = acc[1][2];
      *(LAS f32x4*)(sm + L_EXA + tp * 2048 + l * 16) = acc[1][3];
      *(LAS f32x4*)(sm + L_EXA + tp * 2048 + 1024 + l * 16) = acc[1][4];
      SIG_SET(F_EX + tp, 1);
    }
    u32x4 wa32[3]; u32x2 wa16[3];
    SIG_WAIT(F_WP, 1);
    {
      const int hp = p4 >> 1;
      const int d0 = 4 * g + q4, d1 = d0 + 16, d2 = d0 + 32;
      LAS char* a0 = sm + (hp == 0 ? L_WPHI + d0 * 48 + 24 * (p4 & 1) : L_ZERO);
      LAS char* a1 = sm + ((d1 >= 20) == (hp == 1) ? L_WPHI + d1 * 48 + 24 * (p4 & 1) : L_ZERO);
      LAS char* a2 = sm + ((hp == 1 && d2 < 40) ? L_WPHI + d2 * 48 + 24 * (p4 & 1) : L_ZERO);
#pragma unroll
      for (int mt = 0; mt < 3; mt++) {
        u32x2 e0 = trd(a0 + 8 * mt), e1 = trd(a1 + 8 * mt);
        wa32[mt] = u32x4{e0[0], e0[1], e1[0], e1[1]};
        wa16[mt] = trd(a2 + 8 * mt);
      }
    }
    SIG_WAIT(F_EX + tw, 1);
    SIG_WAIT(F_CS, 2);
    STAMP(8);
    const u32 msv = *(LAS u32*)(sm + L_MS + (tw * 16 + i16) * 4);
    const float m_b = bflo(msv), rs = rcp(bfhi(msv));
    f32x4 cc[5], a[5];
#pragma unroll
    for (int t = 0; t < 3; t++) cc[t] = *(LAS f32x4*)(sm + L_CSP + (16 * t + 4 * g) * 4);
    a[0] = acc[0][0] + *(LAS f32x4*)(sm + L_PHI + tw * 2560 + l * 16);
    a[1] = acc[0][1] + *(LAS f32x4*)(sm + L_PHI + tw * 2560 + 1024 + l * 16);
    a[2] = acc[0][2] + *(LAS f32x4*)(sm + L_V + tw * 1536 + l * 16);
    {
      u32 kn[5][2];
#define NRM(A, t, C, O) do { f32x4 v_ = (A - m_b * C) * rs; O[t][0] = pk2(v_[0], v_[1]); O[t][1] = pk2(v_[2], v_[3]); } while (0)
      NRM(a[0], 0, cc[0], kn); NRM(a[1], 1, cc[1], kn); NRM(a[2], 2, cc[2], kn);
      SB();
      cc[3] = *(LAS f32x4*)(sm + L_CSP + (48 + 4 * g) * 4); cc[4] = *(LAS f32x4*)(sm + L_CSP + (64 + 4 * g) * 4);
      a[3] = acc[0][3] + *(LAS f32x4*)(sm + L_EXA + tw * 2048 + l * 16);
      a[4] = acc[0][4] + *(LAS f32x4*)(sm + L_EXA + tw * 2048 + 1024 + l * 16);
      u32x4 b32 = {kn[0][0], kn[0][1], kn[1][0], kn[1][1]};
      u32x2 b16 = {kn[2][0], kn[2][1]};
      f32x4 pp[3];
#pragma unroll
      for (int mt = 0; mt < 3; mt++) {
        pp[mt] = mfma32(wa32[mt], b32, f32x4{0, 0, 0, 0});
        pp[mt] = mfma16(wa16[mt], b16, pp[mt]);
      }
      NRM(a[3], 3, cc[3], kn); NRM(a[4], 4, cc[4], kn);
      __builtin_amdgcn_wave_barrier();
      LAS char* vr = sm + L_V + (16 * tw + i16) * 96;
      u32x2 v2 = (g >= 2) ? u32x2{kn[2][0], kn[2][1]} : u32x2{g == 0 ? 0x3F80u : 0u, 0u};
      *(LAS u32x2*)(vr + (g >= 2 ? 8 * (g - 2) : 80 + 8 * g)) = v2;
      *(LAS u32x2*)(vr + 16 + 8 * g) = u32x2{kn[3][0], kn[3][1]};
      *(LAS u32x2*)(vr + 48 + 8 * g) = u32x2{kn[4][0], kn[4][1]};
      u32 ph[10];
      softmax_pack(pp[0], pp[1], pp[2], ph);
      LAS char* pr = sm + L_PHI + (16 * tw + i16) * 160 + 40 * g;
#pragma unroll
      for (int k = 0; k < 5; k++) *(LAS u32x2*)(pr + 8 * k) = u32x2{ph[2 * k], ph[2 * k + 1]};
    }
    STAMP(9);
    SIG_ADD(F_PHI);
    __builtin_amdgcn_s_setprio(2);
    SIG_WAIT(F_PHI, 8);
    STAMP(10);
    {
      const int i1 = w < 5 ? w + 8 : w;
      const int mt0 = w / 3, nt0 = w - 3 * mt0, mt1 = i1 < 9 ? i1 / 3 : 3 + ((i1 - 9) >> 1), nt1 = i1 < 9 ? i1 % 3 : 1 + ((i1 - 9) & 1);
      LAS char* ap = sm + L_PHI + (4 * g + q4) * 160 + 8 * p4;
      LAS char* bp = sm + L_V + (4 * g + q4) * 96 + 8 * p4;
      f32x4 d0 = {0, 0, 0, 0}, d1 = {0, 0, 0, 0};
#pragma unroll
      for (int u = 0; u < 4; u++) {
        d0 = mfma32(trd2(ap + 32 * mt0 + u * 5120, 2560), trd2(bp + 32 * nt0 + u * 3072, 1536), d0);
        d1 = mfma32(trd2(ap + 32 * mt1 + u * 5120, 2560), trd2(bp + 32 * nt1 + u * 3072, 1536), d1);
      }
#define KVST(d, mt, nt, en) do { const int fq = 4 * (mt) + g, col = 16 * (nt) + i16; int c = col - (fq >= 10 ? 20 : 0); bool ok = (c >= 0 && c < 20); \
        if (col == 40) { c = 20; ok = true; } ST4(san4(__builtin_bit_cast(u32x4, d)), (ok && (en)) ? reg + R_KVP + (fq * 21 + c) * 16 : WS_END); } while (0)
      KVST(d0, mt0, nt0, true); KVST(d1, mt1, nt1, w < 5);
    }
    SIG_ADD(F_KR);
    SIG_WAIT(F_Q0 + kh, 1);
    STAMP(11);
    f32x4 aq[2][3];
#pragma unroll
    for (int t = 0; t < 3; t++) { aq[0][t] = f32x4{0, 0, 0, 0}; aq[1][t] = f32x4{0, 0, 0, 0}; }
    {
      LAS char* wqp = sm + L_WQ + kh * 3072 + (4 * g + q4) * 96 + 8 * p4;
      u32x2 fa[6], fb[6];
#define RQ(F, P_, o, r) F[r] = trd(P_ + (o) + 32 * ((r) >> 1) + 1536 * ((r) & 1))
#define STEPQ(FA, FB, j) do { _Pragma("unroll") for (int k = 0; k < 6; k++) { aq[k & 1][k >> 1] = mfma32(FR(FA, k >> 1), xf[10 * (k & 1) + (j)], aq[k & 1][k >> 1]); \
          if ((j) < 9) RQ(FB, wqp, ((j) + 1) * 6144, k); SB(); } } while (0)
#pragma unroll
      for (int r = 0; r < 6; r++) RQ(fa, wqp, 0, r);
      SB();
#pragma unroll
      for (int j = 0; j < 10; j += 2) { STEPQ(fa, fb, j); STEPQ(fb, fa, j + 1);
      }
    }
#pragma unroll
    for (int t = 0; t < 3; t++) *(LAS f32x4*)(sm + L_EXQ + (tw ^ 1) * 3072 + t * 1024 + l * 16) = aq[1][t];
    SIG_SET(F_EX + (tw ^ 1), 2);
    SIG_ADD(F_P2);
    STAMP(12);
    SIG_WAIT(F_EX + tw, 2);
    SIG_WAIT(F_CSQ, 2);
    STAMP(13);
    const u32 slo0 = (rt * 128 + tw * 16 + l / 5) * 1280 + og * 80 + (l % 5) * 16;
    const u32 slo1 = (rt * 128 + tw * 16 + (64 + i16) / 5) * 1280 + og * 80 + ((64 + i16) % 5) * 16;
    SIG_WAIT(F_KR, 8);
    glds16(x, slo0, ldsb + L_V + tw * 1536);
    if (l < 16) glds16(x, slo1, ldsb + L_V + tw * 1536 + 1024);
    u32 pq[10];
    {
      f32x4 aqo[3], c2[3];
      u32 qn[3][2];
#pragma unroll
      for (int t = 0; t < 3; t++) {
        c2[t] = *(LAS f32x4*)(sm + L_CSQ + (16 * t + 4 * g) * 4) + *(LAS f32x4*)(sm + L_CSQ + (48 + 16 * t + 4 * g) * 4);
        aqo[t] = aq[0][t] + *(LAS f32x4*)(sm + L_EXQ + tw * 3072 + t * 1024 + l * 16);
        NRM(aqo[t], t, c2[t], qn);
      }
      u32x4 b32 = {qn[0][0], qn[0][1], qn[1][0], qn[1][1]};
      u32x2 b16 = {qn[2][0], qn[2][1]};
      f32x4 pp[3];
#pragma unroll
      for (int mt = 0; mt < 3; mt++) {
        pp[mt] = mfma32(wa32[mt], b32, f32x4{0, 0, 0, 0});
        pp[mt] = mfma16(wa16[mt], b16, pp[mt]);
      }
      softmax_pack(pp[0], pp[1], pp[2], pq);
#pragma unroll
      for (int k = 0; k < 10; k++) asm volatile("" : "+v"(pq[k]));
    }
    STAMP(14);
    u32 oad[3];
#pragma unroll
    for (int mt = 0; mt < 3; mt++) {
      const int col = mt < 2 ? 8 * q4 + 4 * mt + p4 : 32 + 2 * q4 + p4;
      const int hp = (mt == 2) ? (p4 == 2 ? 0 : 1) : (col >= 20 ? 1 : 0);
      const int row = (mt == 2 && p4 >= 2) ? hp * 21 + 20 : col + hp;
      oad[mt] = ((g >> 1) == hp) ? (u32)(L_KVT + row * 80 + 40 * (g & 1)) : L_ZERO;
      asm volatile("" : "+v"(oad[mt]));
    }
    u32x2 xv[3], xnp[3];
    asm volatile("s_waitcnt vmcnt(0)" ::: "memory");
    {
      LAS char* xp = sm + L_V + tw * 1536 + i16 * 80 + 8 * g;
#pragma unroll
      for (int t = 0; t < 3; t++) {
        xv[t] = *(LAS u32x2*)(xp + 32 * t);
        u32 d0 = pk2(bflo(xv[t][0]) - m_b, bfhi(xv[t][0]) - m_b), d1 = pk2(bflo(xv[t][1]) - m_b, bfhi(xv[t][1]) - m_b);
        xnp[t] = u32x2{pk2(bflo(d0) * rs, bfhi(d0) * rs), pk2(bflo(d1) * rs, bfhi(d1) * rs)};
        asm volatile("" : "+v"(xnp[t]));
      }
    }
    const u32 obase = WS_REG + (rt * 16 + 8 * kh) * RS + R_OB + tg * 2560;
    u32 oa[5];
#pragma unroll
    for (int k = 0; k < 5; k++) { int C = 64 * k + l, pr = C / 80; oa[k] = obase + pr * (RS - 1280) + C * 16; asm volatile("" : "+v"(oa[k])); }
    STAMP(15);
    SIG_WAIT(F_KVT, 1);
    STAMP(16);
    {
      f32x4 oo[3];
#pragma unroll
      for (int mt = 0; mt < 3; mt++) {
        LAS char* ap = sm + oad[mt];
        u32x2 a0 = *(LAS u32x2*)(ap), a1 = *(LAS u32x2*)(ap + 8), a2 = *(LAS u32x2*)(ap + 16), a3 = *(LAS u32x2*)(ap + 24), a4 = *(LAS u32x2*)(ap + 32);
        oo[mt] = mfma32(u32x4{a0[0], a0[1], a1[0], a1[1]}, u32x4{pq[0], pq[1], pq[2], pq[3]}, f32x4{0, 0, 0, 0});
        oo[mt] = mfma32(u32x4{a2[0], a2[1], a3[0], a3[1]}, u32x4{pq[4], pq[5], pq[6], pq[7]}, oo[mt]);
        oo[mt] = mfma16(a4, u32x2{pq[8], pq[9]}, oo[mt]);
      }
      const float r0 = rcp(oo[2][2] + 1e-6f), r1 = rcp(oo[2][3] + 1e-6f);
      const float ra_ = g <= 2 ? r0 : r1, rb_ = g <= 1 ? r0 : r1;
      u32x4 o8 = {pk2(oo[0][0] * ra_, oo[0][1] * ra_), pk2(oo[0][2] * ra_, oo[0][3] * ra_), pk2(oo[1][0] * rb_, oo[1][1] * rb_), pk2(oo[1][2] * rb_, oo[1][3] * rb_)};
      u32 o2 = pk2(oo[2][0] * r1, oo[2][1] * r1);
      const u32 ob = reg + R_OB + tw * 1280 + l * 16;
      ST4(o8, ob);
      auto ra = __builtin_amdgcn_permlane32_swap(o2, o2, false, false);
      auto rb = __builtin_amdgcn_permlane16_swap(ra[0], ra[0], false, false);
      auto rc2 = __builtin_amdgcn_permlane16_swap(ra[1], ra[1], false, false);
      if (g == 0) ST4((u32x4{rb[0], rb[1], rc2[0], rc2[1]}), ob + 1024);
      asm volatile("s_waitcnt vmcnt(0)" ::: "memory");
      SIG_ADD(F_OS);
    }
    MEMFENCE();
    STAMP(17);
    f32x4 ao[2][3];
    int idx;
    {
      LAS char* wop = sm + (kh ? L_WO1 : L_WO0) + (4 * g + q4) * 96 + 8 * p4;
      SIG_WAIT(F_WO, 2);
      SIG_WAIT(F_IDX, 1);
      idx = __builtin_amdgcn_readfirstlane((int)*(volatile LAS u32*)(sm + L_SYN + F_IDX * 4) - 1);
      SIG_WAIT(kh ? F_O1 : F_O, 1);
      STAMP(19);
      {
        u32x4 of[2][10];
#pragma unroll
        for (int j = 0; j < 10; j++) {
          of[0][j] = __builtin_amdgcn_raw_buffer_load_b128(wsr, oa[j % 5], (j / 5) * 4 * RS + kh * 1280, 16);
          of[1][j] = __builtin_amdgcn_raw_buffer_load_b128(wsr, oa[j % 5], (j / 5) * 4 * RS + (1 - kh) * 1280, 16);
        }
#pragma unroll
        for (int t = 0; t < 3; t++) { ao[0][t] = f32x4{0, 0, 0, 0}; ao[1][t] = f32x4{0, 0, 0, 0}; }
        u32x2 fa[6], fb[6];
#define STEPO(FA, FB, j) do { _Pragma("unroll") for (int k = 0; k < 6; k++) { ao[k & 1][k >> 1] = mfma32(FR(FA, k >> 1), of[k & 1][j], ao[k & 1][k >> 1]); \
            if ((j) < 9) RQ(FB, wop, ((j) + 1) * 3072, k); SB(); } } while (0)
#pragma unroll
        for (int r = 0; r < 6; r++) RQ(fa, wop, 0, r);
        SB();
#pragma unroll
        for (int j = 0; j < 10; j += 2) { STEPO(fa, fb, j); STEPO(fb, fa, j + 1); }
      }
    }
#pragma unroll
    for (int t = 0; t < 3; t++) *(LAS f32x4*)(sm + L_EXQ + (tw ^ 1) * 3072 + t * 1024 + l * 16) = ao[1][t];
    SIG_SET(F_EX + (tw ^ 1), 3);
    STAMP(20);
    SIG_WAIT(F_EX + tw, 3);
    STAMP(21);
    {
      LAS char* xp = sm + L_V + tw * 1536 + i16 * 80 + 8 * g;
      if ((idx >> 4) == rt * 8 + tw) SIG_WAIT(F_Y, 1);
      if (idx == rt * 128 + tw * 16 + i16) {
#pragma unroll
        for (int t = 0; t < 3; t++) if (t < 2 || g < 2) {
          f32x4 yy = *(LAS f32x4*)(sm + L_Y + (16 * t + 4 * g) * 4);
          xnp[t] = u32x2{pk2(bflo(xnp[t][0]) + yy[0], bfhi(xnp[t][0]) + yy[1]), pk2(bflo(xnp[t][1]) + yy[2], bfhi(xnp[t][1]) + yy[3])};
        }
      }
#pragma unroll
      for (int t = 0; t < 3; t++) {
        f32x4 af = ao[0][t] + *(LAS f32x4*)(sm + L_EXQ + tw * 3072 + t * 1024 + l * 16);
        u32 p0 = pk2(af[0], af[1]), p1 = pk2(af[2], af[3]);
        u32 q0 = pk2(bflo(xv[t][0]) + bflo(p0), bfhi(xv[t][0]) + bfhi(p0)), q1 = pk2(bflo(xv[t][1]) + bflo(p1), bfhi(xv[t][1]) + bfhi(p1));
        u32x2 res = {pk2(bflo(q0) + bflo(xnp[t][0]), bfhi(q0) + bfhi(xnp[t][0])), pk2(bflo(q1) + bflo(xnp[t][1]), bfhi(q1) + bfhi(xnp[t][1]))};
        if (t < 2 || g < 2) *(LAS u32x2*)(xp + 32 * t) = res;
      }
      __builtin_amdgcn_wave_barrier();
      LAS char* op = sm + L_V + tw * 1536 + l * 16;
      *(u32x4*)((char*)out + slo0) = *(LAS u32x4*)(op);
      if (l < 16) *(u32x4*)((char*)out + slo1) = *(LAS u32x4*)(op + 1024);
    }
    STAMP(22);
    }
  }
}

extern "C" void kernel_launch(void* const* d_in, const int* in_sizes, int n_in, void* d_out, int out_size,
                              void* d_ws, size_t ws_size, hipStream_t stream) {
  fused_kernel<<<256, 768, 0, stream>>>((const u16*)d_in[0], (const u16*)d_in[1], (const u16*)d_in[2], (const u16*)d_in[3],
                                        (const u16*)d_in[5], (const u16*)d_in[6], (const u16*)d_in[7], (const u16*)d_in[8],
                                        (u16*)d_out, (char*)d_ws);
}
```

```cpp
#include <hip/hip_runtime.h>
#include <stdint.h>

typedef unsigned short u16;
typedef unsigned int u32;
typedef unsigned long long u64;
typedef __attribute__((ext_vector_type(8))) __bf16 bf16x8;
typedef __attribute__((ext_vector_type(4))) short s16x4;
typedef __attribute__((ext_vector_type(4))) float f32x4;
typedef __attribute__((ext_vector_type(2))) float f32x2;
typedef __attribute__((ext_vector_type(2))) __bf16 bf16x2;
typedef __attribute__((ext_vector_type(4))) u32 u32x4;
typedef __attribute__((ext_vector_type(2))) u32 u32x2;
#define LAS __attribute__((address_space(3)))
#define DI __device__ __forceinline__

#define FS 8
constexpr u32 WS_EPOCH = 0;
constexpr u32 WS_REC = 8192;
constexpr u32 WS_ST = 16384;
constexpr u32 WS_REG = 49152;
constexpr u32 RS = 23936;
constexpr u32 R_OB = 0;
constexpr u32 R_KVP = 10240;
constexpr u32 R_YP = 17024;
constexpr u32 R_GUP = 19584;
constexpr u32 R_KVR = 20608;
constexpr u32 R_I1 = 20864;
constexpr u32 R_I3 = 21760;
constexpr u32 R_IG = 21120;
constexpr u32 R_IY = 21760;
constexpr u32 WS_YB = WS_REG + 256 * RS;
constexpr u32 WS_MG = WS_YB + 327680;
constexpr u32 MGS = 1024 + 40 * 128;
constexpr u32 WS_IY = WS_MG + 128 * MGS;
constexpr u32 WS_END = WS_IY + 2048 * 128;
constexpr u32 WS_STAMP = 128u << 20;

constexpr u32 L_W1 = 0;
constexpr u32 L_WQ = 0;
constexpr u32 L_WO0 = 30720;
constexpr u32 L_WO1 = 92160;
constexpr u32 L_EXQ = 61440;
constexpr u32 L_CSP = 102400;
constexpr u32 L_EXA = 103424;
constexpr u32 L_PF = 162816;
constexpr u32 L_PHI = 122880;
constexpr u32 L_V = 143360;
constexpr u32 L_SM = 155648;
constexpr u32 L_WPHI = L_SM;
constexpr u32 L_ZERO = L_SM + 1920;
constexpr u32 L_KVT = L_SM + 1984;
constexpr u32 L_CSQ = L_KVT + 3360;
constexpr u32 L_KEY = L_CSQ + 768;
constexpr u32 L_IDX = L_KEY + 64;
constexpr u32 L_Y = L_IDX + 64;
constexpr u32 L_MS = L_Y + 160;
constexpr u32 L_SYN = L_MS + 512;
enum { F_W0 = 0, F_W1, F_ST, F_CS, F_P1, F_Q0, F_Q1, F_PHI, F_KV, F_CSQ, F_P2, F_KVT, F_WO, F_OS, F_O, F_Y, F_EX = 16, F_WP = 24, F_KR, F_IDX, F_YG, F_YP, F_O1 };
constexpr u32 L_YS = L_PHI;
constexpr u32 L_TOTAL = 163840;

constexpr u32 SENT = 0xFFFFFFFFu;
constexpr u32 SPIN_CAP = 1u << 15;
#define HOP1_AT 6

DI u32 pk2(float a, float b) { f32x2 v = {a, b}; return __builtin_bit_cast(u32, __builtin_convertvector(v, bf16x2)); }
DI float bflo(u32 u) { return __uint_as_float(u << 16); }
DI float bfhi(u32 u) { return __uint_as_float(u & 0xFFFF0000u); }
DI float rbf(float a) { return bflo(pk2(a, 0.f)); }
DI float rcp(float a) { return __builtin_amdgcn_rcpf(a); }
DI float dot2(u32 a, u32 b, float c) { return __builtin_amdgcn_fdot2_f32_bf16(__builtin_bit_cast(bf16x2, a), __builtin_bit_cast(bf16x2, b), c, false); }
DI f32x4 mfma32(u32x4 a, u32x4 b, f32x4 c) {
  return __builtin_amdgcn_mfma_f32_16x16x32_bf16(__builtin_bit_cast(bf16x8, a), __builtin_bit_cast(bf16x8, b), c, 0, 0, 0);
}
DI f32x4 mfma16(u32x2 a, u32x2 b, f32x4 c) {
  return mfma32(u32x4{a[0], a[1], 0u, 0u}, u32x4{b[0], b[1], 0u, 0u}, c);
}
DI u32x2 trd(LAS char* p) { return __builtin_bit_cast(u32x2, __builtin_amdgcn_ds_read_tr16_b64_v4i16((LAS s16x4*)p)); }
DI u32x4 trd2(LAS char* p, u32 d) { u32x2 a = trd(p), b = trd(p + d); return u32x4{a[0], a[1], b[0], b[1]}; }
DI void glds16(const void* sbase, u32 voff, u32 lds_dst) {
  asm volatile("s_mov_b32 m0, %2\n\ts_nop 0\n\tglobal_load_lds_dwordx4 %0, %1" :: "v"(voff), "s"(sbase), "s"(lds_dst) : "memory");
}
DI u32 umin_(u32 a, u32 b) { return a < b ? a : b; }
DI u32 umax_(u32 a, u32 b) { return a > b ? a : b; }
DI u32 san(u32 v) { return umin_(v, 0xFFFFFFFEu); }
DI u32x4 san4(u32x4 v) { return u32x4{san(v[0]), san(v[1]), san(v[2]), san(v[3])}; }
DI u32 max4(u32x4 v) { return umax_(umax_(v[0], v[1]), umax_(v[2], v[3])); }
DI float xchg16_max(float v) { auto r = __builtin_amdgcn_permlane16_swap(__float_as_uint(v), __float_as_uint(v), false, false); return fmaxf(__uint_as_float(r[0]), __uint_as_float(r[1])); }
DI float xchg16_min(float v) { auto r = __builtin_amdgcn_permlane16_swap(__float_as_uint(v), __float_as_uint(v), false, false); return fminf(__uint_as_float(r[0]), __uint_as_float(r[1])); }
DI float xchg16_add(float v) { auto r = __builtin_amdgcn_permlane16_swap(__float_as_uint(v), __float_as_uint(v), false, false); return __uint_as_float(r[0]) + __uint_as_float(r[1]); }
DI float xchg32_add(float v) { auto r = __builtin_amdgcn_permlane32_swap(__float_as_uint(v), __float_as_uint(v), false, false); return __uint_as_float(r[0]) + __uint_as_float(r[1]); }
template <int CTRL> DI float dpp(float v) { return __int_as_float(__builtin_amdgcn_update_dpp(0, __float_as_int(v), CTRL, 0xf, 0xf, true)); }
template <int CTRL> DI u32 dppu(u32 v) { return (u32)__builtin_amdgcn_update_dpp(0, (int)v, CTRL, 0xf, 0xf, true); }
DI float wave_sum(float v) {
  v += dpp<0xB1>(v); v += dpp<0x4E>(v); v += dpp<0x141>(v); v += dpp<0x140>(v);
  return xchg32_add(xchg16_add(v));
}
DI float row_sum(float v) { v += dpp<0xB1>(v); v += dpp<0x4E>(v); v += dpp<0x141>(v); return v + dpp<0x140>(v); }
DI u32 row_max(u32 v) { v = umax_(v, dppu<0xB1>(v)); v = umax_(v, dppu<0x4E>(v)); v = umax_(v, dppu<0x141>(v)); return umax_(v, dppu<0x140>(v)); }
DI u32x4 ld4w(const char* base, u32 off) { u32x4 v; asm volatile("global_load_dwordx4 %0, %1, %2 sc1\n\ts_waitcnt vmcnt(0)" : "=&v"(v) : "v"(off), "s"(base) : "memory"); return v; }
DI u32x2 ld2w(const char* base, u32 off) { u32x2 v; asm volatile("global_load_dwordx2 %0, %1, %2 sc1\n\ts_waitcnt vmcnt(0)" : "=&v"(v) : "v"(off), "s"(base) : "memory"); return v; }
#define MEMFENCE() asm volatile("" ::: "memory")
#define BARRIER() asm volatile("s_waitcnt lgkmcnt(0)\n\ts_barrier" ::: "memory")
#define SB() __builtin_amdgcn_sched_barrier(0)
#define LD4(off) __builtin_amdgcn_raw_buffer_load_b128(wsr, (off), 0, 16)
#define LD2(off) __builtin_amdgcn_raw_buffer_load_b64(wsr, (off), 0, 16)
#define ST4(v, off) __builtin_amdgcn_raw_buffer_store_b128((v), wsr, (off), 0, 16)
#define ST2(v, off) __builtin_amdgcn_raw_buffer_store_b64((v), wsr, (off), 0, 16)

#define STAMP(k) do { } while (0)

DI void softmax_pack(const f32x4& p0, const f32x4& p1, const f32x4& p2, u32 (&o)[10]) {
  float p[10] = {p0[0], p0[1], p0[2], p0[3], p1[0], p1[1], p1[2], p1[3], p2[0], p2[1]};
  float mx = p[0], mn = p[0];
#pragma unroll
  for (int i = 1; i < 10; i++) { mx = fmaxf(mx, p[i]); mn = fminf(mn, p[i]); }
  mx = xchg16_max(mx); mn = xchg16_min(mn);
  const float L2E = 1.4426950408889634f;
  const float a = -mx * L2E, b = mn * L2E;
  float e1[10], e2[10], s1 = 0.f, s2 = 0.f;
#pragma unroll
  for (int i = 0; i < 10; i++) {
    e1[i] = __builtin_amdgcn_exp2f(fmaf(p[i], L2E, a));
    e2[i] = __builtin_amdgcn_exp2f(fmaf(p[i], -L2E, b));
    s1 += e1[i]; s2 += e2[i];
  }
  s1 = xchg16_add(s1); s2 = xchg16_add(s2);
  const float r1 = rcp(s1), r2 = rcp(s2);
#pragma unroll
  for (int i = 0; i < 5; i++) {
    o[i] = pk2(e1[2 * i] * r1, e1[2 * i + 1] * r1);
    o[5 + i] = pk2(e2[2 * i] * r2, e2[2 * i + 1] * r2);
  }
}

#define THR "3"

#define SIG_SET(f, v) do { MEMFENCE(); *(volatile LAS u32*)(sm + L_SYN + (f) * 4) = (v); MEMFENCE(); } while (0)
DI void sig_add(u32 addr) {
  u64 keep; u32 one = 1;
  asm volatile("s_mov_b64 %0, exec\n\ts_mov_b64 exec, 1\n\tds_add_u32 %1, %2\n\ts_mov_b64 exec, %0" : "=&s"(keep) : "v"(addr), "v"(one) : "memory");
}
#define SIG_ADD(f) sig_add(ldsb + L_SYN + (f) * 4)
DI void sig_wait(u32 addr, u32 val) {
  u32 tv, ts, cnt;
  asm volatile(
      "s_mov_b32 %2, 0x400000\n"
      "LW%=:\n\t"
      "ds_read_b32 %0, %3\n\t"
      "s_waitcnt lgkmcnt(0)\n\t"
      "v_readfirstlane_b32 %1, %0\n\t"
      "s_nop 1\n\t"
      "s_cmp_ge_u32 %1, %4\n\t"
      "s_cbranch_scc1 LD%=\n\t"
      "s_sleep 1\n\t"
      "s_sub_u32 %2, %2, 1\n\t"
      "s_cmp_lg_u32 %2, 0\n\t"
      "s_cbranch_scc1 LW%=\n"
      "LD%=:"
      : "=&v"(tv), "=&s"(ts), "=&s"(cnt) : "v"(addr), "s"(val) : "memory", "scc");
}
DI void sig_wait2(u32 a0, u32 v0, u32 a1, u32 v1) {
  u32 t0, t1, s0, s1, cnt;
  asm volatile(
      "s_mov_b32 %4, 0x400000\n"
      "LW%=:\n\t"
      "ds_read_b32 %0, %5\n\t"
      "ds_read_b32 %1, %7\n\t"
      "s_waitcnt lgkmcnt(0)\n\t"
      "v_readfirstlane_b32 %2, %0\n\t"
      "v_readfirstlane_b32 %3, %1\n\t"
      "s_nop 1\n\t"
      "s_cmp_ge_u32 %2, %6\n\t"
      "s_cbranch_scc0 LS%=\n\t"
      "s_cmp_ge_u32 %3, %8\n\t"
      "s_cbranch_scc1 LD%=\n"
      "LS%=:\n\t"
      "s_sleep 1\n\t"
      "s_sub_u32 %4, %4, 1\n\t"
      "s_cmp_lg_u32 %4, 0\n\t"
      "s_cbranch_scc1 LW%=\n"
      "LD%=:"
      : "=&v"(t0), "=&v"(t1), "=&s"(s0), "=&s"(s1), "=&s"(cnt) : "v"(a0), "s"(v0), "v"(a1), "s"(v1) : "memory", "scc");
}
#define SIG_WAIT2(f0, v0, f1, v1) sig_wait2(ldsb + L_SYN + (f0) * 4, (v0), ldsb + L_SYN + (f1) * 4, (v1))
#define SIG_WAIT(f, v) sig_wait(ldsb + L_SYN + (f) * 4, (v))
#define WPRIO 0
#define CLR_AT 4
#define PF_AT 0
#define KVD_AT 2
#define WO_FIRST 12
#define GW_AT 4
#define GTHR "40"
#define REC_AT 3
#define ST_AT 2
#define GW_SIG F_W0
#define H1D 8
#define H1S 4
#define PFLATE 0
#define WTHR "40"
#define RAWBAR() asm volatile("s_barrier" ::: "memory")
__global__ __launch_bounds__(768, 3) void fused_kernel(
    const u16* __restrict__ x, const u16* __restrict__ wqkv, const u16* __restrict__ wphi, const u16* __restrict__ wo,
    const u16* __restrict__ wr, const u16* __restrict__ wg, const u16* __restrict__ wu, const u16* __restrict__ wd,
    u16* __restrict__ out, char* __restrict__ ws) {
  __shared__ __attribute__((aligned(1024))) char smem_[L_TOTAL];
  LAS char* sm = (LAS char*)smem_;
  const u32 ldsb = (u32)(size_t)smem_;
  STAMP(0);
  const int tid = threadIdx.x, l = tid & 63;
  const int w = __builtin_amdgcn_readfirstlane(tid >> 6);
  const int g = l >> 4, i16 = l & 15, q4 = i16 >> 2, p4 = l & 3;
  const int b = blockIdx.x, xcd = b & 7, slot = b >> 3;
  const int rt = (xcd >> 1) * 4 + (slot >> 3), hg = (xcd & 1) * 8 + (slot & 7);
  const int wgi = rt * 16 + hg;
  const bool fast = (0xA5 >> (hg & 7)) & 1;
  const int og = hg ^ 1;
  const int rot = (hg + 2 * rt) % 5;
  const auto wsr = __builtin_amdgcn_make_buffer_rsrc((void*)ws, 0, WS_END, 0x00020000);
  const u32 reg = WS_REG + wgi * RS;
#define CROT(c) ((c) + rot >= 5 ? (c) + rot - 5 : (c) + rot)
#define FR(F, t) (u32x4{F[2 * (t)][0], F[2 * (t)][1], F[2 * (t) + 1][0], F[2 * (t) + 1][1]})
#define LOADEP() u32x2 ep; { const u32 z = 0; asm volatile("global_load_dwordx2 %0, %1, %2 sc1" : "=v"(ep) : "v"(z), "s"(ws) : "memory"); }
#define GETTAG() u32 tag_lo, tag_hi; { u64 e = (((u64)ep[1] << 32) | ep[0]) + 1; tag_lo = __builtin_amdgcn_readfirstlane((u32)e); tag_hi = __builtin_amdgcn_readfirstlane((u32)(e >> 32)); }

  if (tid < 32) *(LAS u32*)(sm + L_SYN + tid * 4) = 0u;
  if (w >= 8) {
    const int h = w - 8;
    __builtin_amdgcn_s_setprio(3);
    int l_ = __builtin_amdgcn_mbcnt_hi(~0u, __builtin_amdgcn_mbcnt_lo(~0u, 0u));
    asm volatile("" : "+v"(l_));
    const int l = l_, tid = w * 64 + l, g = l >> 4, i16 = l & 15, q4 = i16 >> 2, p4 = l & 3;
    const u32x4 ONES = {0x3F803F80u, 0x3F803F80u, 0x3F803F80u, 0x3F803F80u};
#define YSUM(part) do { u32x4 yv[8]; _Pragma("unroll") for (int k = 0; k < 8; k++) { const int u = (48 * (part) + 6 * k) * 10 + l; \
        yv[k] = LD4((l < 60 && u < 1280) ? WS_YB + og * 20480 + u * 16 : WS_END); } \
      f32x4 s_ = {0, 0, 0, 0}; _Pragma("unroll") for (int k = 0; k < 8; k++) s_ += __builtin_bit_cast(f32x4, yv[k]); \
      if (l < 60) *(LAS f32x4*)(sm + L_YS + ((part) * 60 + l) * 16) = s_; SIG_ADD(F_YP); } while (0)
    if (h < 2) {
      u32 woff[5];
#pragma unroll
      for (int k = 0; k < 5; k++) {
        int P = 64 * k + l, rho = P / 10, s = P - 10 * rho;
        woff[k] = (32 * ((rho >> 2) & 1) + 16 * ((rho >> 3) & 1) + 4 * ((rho >> 4) & 1) + (rho & 3)) * 3840 + 1280 + (s >= 5 ? 1200 : 0) + hg * 80 + s * 16;
      }
      const char* wbase = (const char*)wqkv + h * (320 * 3840);
#define WL(c) do { const char* b_ = wbase + CROT(c) * (64 * 3840); _Pragma("unroll") for (int k = 0; k < 10; k++) \
        glds16(b_ + (k / 5) * (8 * 3840), woff[k % 5], ldsb + L_W1 + (4 * (c) + 2 * (k / 5) + h) * 5120 + (k % 5) * 1024); } while (0)
#define WDONE(N, c) do { asm volatile("s_waitcnt vmcnt(" #N ")" ::: "memory"); SIG_SET(F_W0 + h, (c) + 1); } while (0)
      WL(0);
      BARRIER();
      WDONE(0, 0);
      __builtin_amdgcn_s_setprio(0);
      WL(1);
      __builtin_amdgcn_s_setprio(3);
      STAMP(1);
      WL(2);
      STAMP(2);
      WDONE(10, 1); WL(3);
      STAMP(3);
      WDONE(10, 2); WL(4);
      STAMP(4);
      {
        const int q = slot * 100 + (l >> 3);
        u32 po[13];
#pragma unroll
        for (int k = 0; k < 13; k++) { const int qq = q + (k < 12 ? 8 * k : 92), row = qq / 5, cc_ = qq - 5 * row; po[k] = row * (h ? 1280 : 3840) + (xcd & 1) * 640 + cc_ * 128 + (l & 7) * 16; }
        WDONE(10, 3);
#pragma unroll
        for (int k = 0; k < 13; k++) glds16(h ? wo : wqkv, po[k], ldsb + L_PF);
        WDONE(13, 4);
      }
      STAMP(5);
      u32 qoff[3], ooff[3];
#pragma unroll
      for (int k = 0; k < 3; k++) {
        int P = 64 * k + l, rho = P / 6, s = P - 6 * rho; if (s > 4) s = 4;
        qoff[k] = (32 * ((rho >> 2) & 1) + 16 * ((rho >> 3) & 1) + 4 * ((rho >> 4) & 1) + (rho & 3)) * 3840 + hg * 80 + s * 16;
        ooff[k] = (8 * ((rho >> 2) & 3) + 4 * ((rho >> 4) & 1) + (rho & 3)) * 1280 + og * 80 + s * 16;
      }
      u32x4 wr_[30];
#define OROT(k) (2 * CROT((k) / 6) + ((k) / 3 & 1))
#pragma unroll
      for (int k = 0; k < WO_FIRST; k++)
        asm volatile("global_load_dwordx4 %0, %1, %2" : "=v"(wr_[k]) : "v"(ooff[k % 3]), "s"((const char*)wo + (10 * h + OROT(k)) * 40960));
      SIG_WAIT(F_P1, 10);
      STAMP(6);
#pragma unroll 1
      for (int c = 0; c < 5; c++) {
        const char* b_ = wbase + CROT(c) * (64 * 3840);
#pragma unroll
        for (int k = 0; k < 6; k++) glds16(b_ + (k / 3) * (8 * 3840), qoff[k % 3], ldsb + L_WQ + (4 * c + 2 * (k / 3) + h) * 3072 + (k % 3) * 1024);
      }
      STAMP(7);
      asm volatile("s_waitcnt vmcnt(0)" ::: "memory");
      SIG_SET(F_Q0 + h, 1);
      STAMP(8);
      __builtin_amdgcn_s_setprio(0);
      {
        f32x4 cq[3];
#pragma unroll
        for (int t = 0; t < 3; t++) cq[t] = f32x4{0, 0, 0, 0};
        LAS char* wqp = sm + L_WQ + h * 3072 + (4 * g + q4) * 96 + 8 * p4;
#pragma unroll 1
        for (int k = 0; k < 10; k++)
#pragma unroll
          for (int t = 0; t < 3; t++) cq[t] = mfma32(ONES, trd2(wqp + k * 6144 + 32 * t, 1536), cq[t]);
#pragma unroll
        for (int t = 0; t < 3; t++) *(LAS float*)(sm + L_CSQ + (48 * h + 16 * t + i16) * 4) = cq[t][0];
        SIG_ADD(F_CSQ);
        SIG_ADD(F_P2);
      }
#pragma unroll
      for (int k = WO_FIRST; k < 30; k++)
        asm volatile("global_load_dwordx4 %0, %1, %2\n\ts_waitcnt vmcnt(" WTHR ")" : "=v"(wr_[k]) : "v"(ooff[k % 3]), "s"((const char*)wo + (10 * h + OROT(k)) * 40960));
      STAMP(9);
      SIG_WAIT(h ? F_KR : F_P2, h ? 8 : 10);
      asm volatile("s_waitcnt vmcnt(0)" ::: "memory");
      STAMP(10);
      __builtin_amdgcn_s_setprio(0);
#pragma unroll
      for (int k = 0; k < 30; k++) {
        asm volatile("" : "+v"(wr_[k]));
        *(LAS u32x4*)(sm + (h ? L_WO1 : L_WO0) + OROT(k) * 3072 + (k % 3) * 1024 + l * 16) = wr_[k];
      }
      SIG_ADD(F_WO);
      STAMP(12);
      __builtin_amdgcn_s_setprio(3);
      SIG_WAIT(F_IDX, 1);
      if ((((int)*(volatile LAS u32*)(sm + L_SYN + F_IDX * 4) - 1) >> 7) == rt) { SIG_WAIT(F_YG, 1); YSUM(h); }
    } else {
      const int e = h - 2;
#define STATS_LD() u32x4 sx[5], sw[5]; { const u32 o = (4 * e + g) * 1280 + i16 * 16, o2 = i16 * 16; \
          _Pragma("unroll") for (int k = 0; k < 5; k++) { asm volatile("global_load_dwordx4 %0, %1, %2 offset:%3" : "=v"(sx[k]) : "v"(o), "s"(x + wgi * 8 * 640), "i"(k * 256)); \
            asm volatile("global_load_dwordx4 %0, %1, %2 offset:%3" : "=v"(sw[k]) : "v"(o2), "s"(wr), "i"(k * 256)); } }
#define STATS() do { _Pragma("unroll") for (int k = 0; k < 5; k++) asm volatile("" : "+v"(sx[k]), "+v"(sw[k])); \
          float s = 0.f, q = 0.f; \
          _Pragma("unroll") for (int k = 0; k < 20; k++) { s = dot2(sx[k >> 2][k & 3], 0x3F803F80u, s); q = dot2(sx[k >> 2][k & 3], sx[k >> 2][k & 3], q); } \
          const float mean = row_sum(s) * (1.f / 640.f); \
          const float var = fmaxf(row_sum(q) * (1.f / 640.f) - mean * mean, 0.f); \
          const float mb = rbf(mean), sb = rbf(__builtin_amdgcn_sqrtf(rbf(rbf(var) + 1e-5f))), rsb = rcp(sb); \
          const u32 ms = pk2(mb, sb); const int srow = wgi * 8 + 4 * e + g; \
          if (i16 == 0) ST4((u32x4{ms, tag_lo, ms, tag_hi}), WS_ST + srow * 16); \
          float r = 0.f; \
          _Pragma("unroll") for (int k = 0; k < 20; k++) { const u32 xd = sx[k >> 2][k & 3]; \
            u32 d = pk2(bflo(xd) - mb, bfhi(xd) - mb), n = pk2(bflo(d) * rsb, bfhi(d) * rsb); r = dot2(n, sw[k >> 2][k & 3], r); } \
          r = row_sum(r); \
          const u32 rb = pk2(r, 0.f) & 0xFFFFu; \
          const u32 ord = (rb & 0x8000u) ? (~rb & 0xFFFFu) : (rb | 0x8000u); \
          if (i16 == 0) *(LAS u32x2*)(sm + L_KEY + (4 * e + g) * 8) = u32x2{(ord << 16) | (u32)(0x7FFF - srow), ms}; \
          SIG_ADD(F_ST); } while (0)
      if (e == 1) {
        LOADEP();
        STATS_LD();
        BARRIER();
        if (l < 14) ST4((u32x4{SENT, SENT, SENT, SENT}), reg + R_KVR + l * 16);
        asm volatile("s_waitcnt vmcnt(1)" : "+v"(ep) :: "memory");
        GETTAG();
        STAMP(1);
        STATS();
        {
          SIG_WAIT(F_ST, 2);
          u32x2 kk = *(LAS u32x2*)(sm + L_KEY + (l & 7) * 8);
          u32 km = row_max(kk[0]);
          if (l < 8 && kk[0] == km) ST4((u32x4{kk[0], tag_lo, kk[1], tag_hi}), WS_REC + b * 16);
        }
        STAMP(2);
        u32x4 s0 = {0, 0, 0, 0}, s1 = {0, 0, 0, 0};
        const u32 sta = WS_ST + (rt * 128 + l) * 16;
#define LDST() asm volatile("global_load_dwordx4 %0, %2, %3 sc1\n\tglobal_load_dwordx4 %1, %2, %3 offset:1024 sc1\n\ts_waitcnt vmcnt(0)" : "=&v"(s0), "=&v"(s1) : "v"(sta), "s"(ws) : "memory")
        f32x4 cs[5];
#pragma unroll
        for (int t = 0; t < 5; t++) cs[t] = f32x4{0, 0, 0, 0};
        LAS char* w1p = sm + L_W1 + (4 * g + q4) * 160 + 8 * p4;
#pragma unroll 1
        for (int c = 0; c < 5; c++) {
          if (c == ST_AT) LDST();
          SIG_WAIT2(F_W0, c + 1, F_W1, c + 1);
          if (c == CLR_AT) {
#pragma unroll
            for (int k = 0; k < 7; k++) ST4((u32x4{SENT, SENT, SENT, SENT}), (k * 64 + l < 420) ? reg + R_KVP + (k * 64 + l) * 16 : WS_END);
          }
#pragma unroll
          for (int k = 0; k < 4; k++)
#pragma unroll
            for (int t = 3; t < 5; t++) cs[t] = mfma32(ONES, trd2(w1p + (4 * c + k) * 5120 + 32 * t, 2560), cs[t]);
          if (fast) {
#pragma unroll
            for (int k = 0; k < 4; k++)
#pragma unroll
              for (int t = 0; t < 3; t++) cs[t] = mfma32(ONES, trd2(w1p + (4 * c + k) * 5120 + 32 * t, 2560), cs[t]);
          }
          if (c == 2) { SIG_ADD(F_P1); if (fast) SIG_ADD(F_P1); }
        }
        STAMP(3);
#pragma unroll 1
        for (u32 spin = 0; spin < SPIN_CAP; spin++) {
          if (__all(s0[1] == tag_lo && s0[3] == tag_hi && s0[0] == s0[2] && s1[1] == tag_lo && s1[3] == tag_hi && s1[0] == s1[2])) break;
          __builtin_amdgcn_s_sleep(4);
          LDST();
        }
        *(LAS u32*)(sm + L_MS + l * 4) = s0[0];
        *(LAS u32*)(sm + L_MS + 256 + l * 4) = s1[0];
        *(LAS float*)(sm + L_CSP + (48 + i16) * 4) = cs[3][0]; *(LAS float*)(sm + L_CSP + (64 + i16) * 4) = cs[4][0];
        if (fast) {
#pragma unroll
          for (int t = 0; t < 3; t++) *(LAS float*)(sm + L_CSP + (16 * t + i16) * 4) = cs[t][0];
          SIG_ADD(F_CS);
        }
        SIG_ADD(F_CS);
        STAMP(4);
        asm volatile("s_waitcnt vmcnt(0)" ::: "memory");
        SIG_ADD(F_PHI);
        if (l < 16) ST2((u32x2{tag_lo, tag_hi}), WS_REG + (l * 16 + hg) * RS + R_I1 + rt * 8);
        STAMP(11);
        SIG_WAIT(F_PHI, 9);
        STAMP(5);
#define HINT(addr) do { _Pragma("unroll 1") for (u32 spin = 0; spin < SPIN_CAP; spin++) { u32x4 f_ = ld4w(ws, (addr) + (l & 7) * 16); \
          if (__all(f_[0] == tag_lo && f_[1] == tag_hi && f_[2] == tag_lo && f_[3] == tag_hi)) break; } } while (0)
        HINT(reg + R_I1);
        SIG_WAIT(F_KR, 8);
        __builtin_amdgcn_s_sleep(H1D);
        STAMP(6);
        {
          const int he = l >= 28 ? l - 28 : l;
          const bool ok = (l < 56) && (rt < 15);
          const u32 h1a = ok ? WS_REG + ((l >= 28 ? 128 : 0) + hg) * RS + R_KVP + (28 * rt + he) * 16 : WS_END;
          u32x4 h1[8];
#pragma unroll 1
          for (u32 spin = 0; spin < SPIN_CAP; spin++) {
            MEMFENCE();
            u32 m = 0;
#pragma unroll
            for (int k = 0; k < 8; k++) h1[k] = __builtin_amdgcn_raw_buffer_load_b128(wsr, h1a, k * 16 * RS, 16);
#pragma unroll
            for (int k = 0; k < 8; k++) m = umax_(m, max4(h1[k]));
            if (__all(m != SENT)) break;
            __builtin_amdgcn_s_sleep(H1S);
          }
          f32x4 s = __builtin_bit_cast(f32x4, h1[0]);
#pragma unroll
          for (int k = 1; k < 8; k++) s += __builtin_bit_cast(f32x4, h1[k]);
#pragma unroll
          for (int r = 0; r < 4; r++) s[r] += __shfl(s[r], (l + 28) & 63);
          if (ok && l < 28) ST2((u32x2{san(pk2(s[0], s[1])), san(pk2(s[2], s[3]))}), reg + R_KVR + l * 8);
        }
        STAMP(7);
        {
          const int hpr = l / 14, e2 = l - 14 * hpr;
          const u32 h2a = WS_REG + (hpr * 16 + hg) * RS + R_KVR + e2 * 16;
          u32 ka[8];
#pragma unroll
          for (int k = 0; k < 8; k++) { int it = 112 * (k >> 1) + 2 * l + (k & 1), hf = it / 21, c = it - 21 * hf; ka[k] = L_KVT + c * 80 + hf * 8 + (hf >= 10 ? 1600 : 0); }
          u32x4 kv[4];
#pragma unroll 1
          for (u32 spin = 0; spin < SPIN_CAP; spin++) {
            MEMFENCE();
            u32 m = 0;
#pragma unroll
            for (int k = 0; k < 4; k++) kv[k] = LD4((l < 56 && 4 * k + hpr < 15) ? h2a + k * 64 * RS : WS_END);
#pragma unroll
            for (int k = 0; k < 4; k++) m = umax_(m, max4(kv[k]));
            if (__all(m != SENT)) break;
          }
#pragma unroll
          for (int k = 0; k < 4; k++) if (l < 56 && 4 * k + hpr < 15) {
            *(LAS u32x2*)(sm + ka[2 * k]) = u32x2{kv[k][0], kv[k][1]};
            *(LAS u32x2*)(sm + ka[2 * k + 1]) = u32x2{kv[k][2], kv[k][3]};
          }
        }
        SIG_SET(F_KVT, 1);
        STAMP(8);
        SIG_WAIT(F_OS, 8);
        if (l < 16) ST2((u32x2{tag_lo, tag_hi}), WS_REG + (rt * 16 + l) * RS + R_I3 + hg * FS);
        STAMP(9);
#pragma unroll 1
        for (u32 spin = 0; spin < SPIN_CAP; spin++) {
          u32x2 f_ = ld2w(ws, reg + R_I3 + i16 * FS);
          const u64 okm = __ballot(f_[0] == tag_lo && f_[1] == tag_hi);
          if ((okm & 0xFF) == 0xFF) SIG_SET(F_O, 1);
          if ((okm & 0xFF00) == 0xFF00) SIG_SET(F_O1, 1);
          if ((okm & 0xFFFF) == 0xFFFF) break;
        }
        SIG_SET(F_O, 1); SIG_SET(F_O1, 1);
        STAMP(10);
      } else {
        LOADEP();
        STATS_LD();
        u32 wp[7];
#pragma unroll
        for (int k = 0; k < 7; k++) { const u32 o = hg * 1600 + (l + 64 * k < 400 ? l + 64 * k : 0) * 4; asm volatile("global_load_dword %0, %1, %2" : "=v"(wp[k]) : "v"(o), "s"(wphi)); }
        BARRIER();
        if (l < 4) *(LAS u32x4*)(sm + L_ZERO + l * 16) = u32x4{0, 0, 0, 0};
        if (l < 40) { *(LAS u32*)(sm + L_WPHI + l * 48 + 20) = 0u; *(LAS u32*)(sm + L_WPHI + l * 48 + 44) = 0u; }
        asm volatile("s_waitcnt vmcnt(0)" : "+v"(ep), "+v"(wp[0]), "+v"(wp[1]), "+v"(wp[2]), "+v"(wp[3]), "+v"(wp[4]), "+v"(wp[5]), "+v"(wp[6]) :: "memory");
        GETTAG();
        STATS();
#pragma unroll
        for (int k = 0; k < 7; k++) { const int u = l + 64 * k, row = u / 10, z = u - 10 * row; if (u < 400) *(LAS u32*)(sm + L_WPHI + row * 48 + z * 4 + (z >= 5 ? 4 : 0)) = wp[k]; }
        SIG_SET(F_WP, 1);
        STAMP(1);
        const int fi = wgi >> 1, cb = fi < 120 ? fi / 10 : 0, ksp = fi < 120 ? fi - 10 * cb : 0;
        if (fast) SIG_WAIT(F_W0, REC_AT);
        else {
          f32x4 cs[3];
#pragma unroll
          for (int t = 0; t < 3; t++) cs[t] = f32x4{0, 0, 0, 0};
          LAS char* w1p = sm + L_W1 + (4 * g + q4) * 160 + 8 * p4;
#pragma unroll 1
          for (int c = 0; c < 5; c++) {
            SIG_WAIT2(F_W0, c + 1, F_W1, c + 1);
#pragma unroll
            for (int k = 0; k < 4; k++)
#pragma unroll
              for (int t = 0; t < 3; t++) cs[t] = mfma32(ONES, trd2(w1p + (4 * c + k) * 5120 + 32 * t, 2560), cs[t]);
            if (c == 2) SIG_ADD(F_P1);
          }
#pragma unroll
          for (int t = 0; t < 3; t++) *(LAS float*)(sm + L_CSP + (16 * t + i16) * 4) = cs[t][0];
          SIG_ADD(F_CS);
        }
        u32x2 rec;
        {
          u32 km = 0, ms = 0;
          u32x4 r_[4];
#pragma unroll 1
          for (u32 spin = 0; spin < SPIN_CAP; spin++) {
            const u32 ra = WS_REC + l * 16;
            asm volatile("global_load_dwordx4 %0, %4, %5 sc1\n\tglobal_load_dwordx4 %1, %4, %5 offset:1024 sc1\n\tglobal_load_dwordx4 %2, %4, %5 offset:2048 sc1\n\t"
                         "global_load_dwordx4 %3, %4, %5 offset:3072 sc1\n\ts_waitcnt vmcnt(0)"
                         : "=&v"(r_[0]), "=&v"(r_[1]), "=&v"(r_[2]), "=&v"(r_[3]) : "v"(ra), "s"(ws) : "memory");
            bool ok = true;
#pragma unroll
            for (int k = 0; k < 4; k++) ok = ok && r_[k][1] == tag_lo && r_[k][3] == tag_hi;
            if (__all(ok)) break;
            __builtin_amdgcn_s_sleep(16);
          }
#pragma unroll
          for (int k = 0; k < 4; k++) if (r_[k][0] > km) { km = r_[k][0]; ms = r_[k][2]; }
          u32 kx = row_max(km);
          { auto r = __builtin_amdgcn_permlane16_swap(kx, kx, false, false); kx = umax_(r[0], r[1]); }
          { auto r = __builtin_amdgcn_permlane32_swap(kx, kx, false, false); kx = umax_(r[0], r[1]); }
          const int src = __builtin_ctzll(__ballot(km == kx));
          rec = u32x2{(u32)__builtin_amdgcn_readfirstlane(kx), (u32)__builtin_amdgcn_readlane(ms, src)};
        }
        const int idx = (0x7FFF - (rec[0] & 0xFFFFu)) & 2047;
        if (b == 0 && l == 0) ST2((u32x2{tag_lo, tag_hi}), WS_EPOCH);
        SIG_SET(F_IDX, idx + 1);
        STAMP(4);
        if (fast) {
          const u32 mreg = WS_MG + fi * MGS;
          u32x2 xr;
          { const u32 xo = (64 * ksp) * 2 + i16 * 8; asm volatile("global_load_dwordx2 %0, %1, %2" : "=v"(xr) : "v"(xo), "s"(x + idx * 640)); }
          u32x4 gw[32];
          __builtin_amdgcn_s_setprio(0);
          SIG_WAIT(GW_SIG, GW_AT);
          STAMP(2);
          {
            const u16* gm = cb < 6 ? wg + cb * 256 : wu + (cb - 6) * 256;
            const u32 go = (64 * ksp + (l >> 5)) * 3072 + (l & 31) * 16;
#pragma unroll
            for (int k = 0; k < 32; k++) asm volatile("global_load_dwordx4 %0, %1, %2" : "=v"(gw[k]) : "v"(go), "s"(gm + k * 2 * 1536));
          }
          __builtin_amdgcn_s_setprio(3);
          STAMP(3);
          {
            asm volatile("s_waitcnt vmcnt(16)" : "+v"(xr) :: "memory");
            const float sel_m = bflo(rec[1]), sel_r = rcp(bfhi(rec[1]));
            float xa[2];
#pragma unroll
            for (int z = 0; z < 2; z++) { const float xv_ = (l >> 5) ? bfhi(xr[z]) : bflo(xr[z]); xa[z] = rbf(rbf(xv_ - sel_m) * sel_r); }
            float v[8];
#pragma unroll
            for (int k = 0; k < 8; k++) v[k] = 0.f;
#define M1ROW(r) do { asm volatile("" : "+v"(gw[2 * r]), "+v"(gw[2 * r + 1])); _Pragma("unroll") for (int z = 0; z < 2; z++) { const float a = dpp<0x150 + r>(xa[z]); \
              _Pragma("unroll") for (int k = 0; k < 4; k++) { v[2 * k] = fmaf(a, bflo(gw[2 * r + z][k]), v[2 * k]); v[2 * k + 1] = fmaf(a, bfhi(gw[2 * r + z][k]), v[2 * k + 1]); } } } while (0)
            M1ROW(0); M1ROW(1); M1ROW(2); M1ROW(3); M1ROW(4); M1ROW(5); M1ROW(6); M1ROW(7);
            asm volatile("s_waitcnt vmcnt(0)" ::: "memory");
            STAMP(5);
            M1ROW(8); M1ROW(9); M1ROW(10); M1ROW(11); M1ROW(12); M1ROW(13); M1ROW(14); M1ROW(15);
            u32x4 u4;
#pragma unroll
            for (int k = 0; k < 4; k++) {
              auto r = __builtin_amdgcn_permlane32_swap(__float_as_uint(v[k]), __float_as_uint(v[k + 4]), false, false);
              u4[k] = __float_as_uint(__uint_as_float(r[0]) + __uint_as_float(r[1]));
            }
            ST4(u4, fi < 120 ? mreg + (l & 31) * 32 + (l >> 5) * 16 : WS_END);
            asm volatile("s_waitcnt vmcnt(0)" ::: "memory");
            const int c0 = (cb % 6) * 256, d = c0 / 12 + l;
            if (fi < 120 && 12 * d < c0 + 256) ST2((u32x2{tag_lo, tag_hi}), WS_MG + d * MGS + 1024 + ((12 * d < c0 ? 20 : 0) + (cb / 6) * 10 + ksp) * FS);
          }
          STAMP(6);
          u32x2 dw[36];
          {
            const u16* dp = wd + (12 * fi) * 640;
            const u32 dof = l * 8, dof2 = (l < 32 ? l : 0) * 8;
#pragma unroll
            for (int k = 0; k < 12; k++) {
              asm volatile("global_load_dwordx2 %0, %1, %2" : "=v"(dw[3 * k]) : "v"(dof), "s"(dp + k * 640));
              asm volatile("global_load_dwordx2 %0, %1, %2 offset:512" : "=v"(dw[3 * k + 1]) : "v"(dof), "s"(dp + k * 640));
              asm volatile("global_load_dwordx2 %0, %1, %2 offset:1024" : "=v"(dw[3 * k + 2]) : "v"(dof2), "s"(dp + k * 640));
            }
          }
          STAMP(7);
          {
            const int nsl = ((12 * fi) >> 8) == ((12 * fi + 11) >> 8) ? 20 : 40;
#pragma unroll 1
            for (u32 spin = 0; spin < SPIN_CAP; spin++) {
              u32x2 f0 = ld2w(ws, mreg + 1024 + (l < nsl ? l : 0) * FS);
              if (__all(f0[0] == tag_lo && f0[1] == tag_hi)) break;
              __builtin_amdgcn_s_sleep(6);
            }
          }
          STAMP(8);
          {
            const int kw = l / 6, pr = l - 6 * kw, col = 12 * fi + 2 * pr;
            const u32 ga = l < 60 ? WS_MG + ((col >> 8) * 10 + kw) * MGS + (col & 255) * 4 : WS_END;
            f32x2 gg2 = __builtin_bit_cast(f32x2, LD2(ga)), uu2 = __builtin_bit_cast(f32x2, __builtin_amdgcn_raw_buffer_load_b64(wsr, ga, 60 * MGS, 16));
            float z[4] = {gg2[0], gg2[1], uu2[0], uu2[1]};
#pragma unroll
            for (int k = 0; k < 4; k++) {
              const float z0 = z[k] + __shfl(z[k], (l + 30) & 63);
              z[k] = rbf(z0 + __shfl(z0, (l + 6) & 63) + __shfl(z0, (l + 12) & 63) + __shfl(z0, (l + 18) & 63) + __shfl(z0, (l + 24) & 63));
            }
            float hv[2];
#pragma unroll
            for (int k = 0; k < 2; k++) hv[k] = rbf(rbf(z[k] * rcp(1.f + __expf(-z[k]))) * z[2 + k]);
#pragma unroll
            for (int k = 0; k < 36; k++) asm volatile("" : "+v"(dw[k]));
#pragma unroll
            for (int zz = 0; zz < 3; zz++) {
              f32x4 y = {0, 0, 0, 0};
#pragma unroll
              for (int i = 0; i < 12; i++) {
                float hh = __int_as_float(__builtin_amdgcn_readlane(__float_as_int(hv[i & 1]), i >> 1));
                y[0] += hh * bflo(dw[3 * i + zz][0]); y[1] += hh * bfhi(dw[3 * i + zz][0]); y[2] += hh * bflo(dw[3 * i + zz][1]); y[3] += hh * bfhi(dw[3 * i + zz][1]);
              }
              { const int u = 64 * zz + l, sl = u / 10; ST4(__builtin_bit_cast(u32x4, y), u < 160 ? WS_YB + (sl * 128 + fi) * 160 + (u - 10 * sl) * 16 : WS_END); }
            }
            asm volatile("s_waitcnt vmcnt(0)" ::: "memory");
            if (l < 16) ST2((u32x2{tag_lo, tag_hi}), WS_IY + (l * 128 + fi) * FS);
          }
        }
        STAMP(9);
        if ((idx >> 7) == rt) {
          SIG_WAIT(F_KVT, 1);
#pragma unroll 1
          for (u32 spin = 0; spin < SPIN_CAP; spin++) {
            __builtin_amdgcn_s_sleep(6);
            u32x2 f0, f1; const u32 fa_ = WS_IY + (hg * 128 + l) * FS, fb_ = fa_ + 64 * FS;
            asm volatile("global_load_dwordx2 %0, %2, %4 sc1\n\tglobal_load_dwordx2 %1, %3, %4 sc1\n\ts_waitcnt vmcnt(0)" : "=&v"(f0), "=&v"(f1) : "v"(fa_), "v"(fb_), "s"(ws) : "memory");
            if (__all(f0[0] == tag_lo && f0[1] == tag_hi && f1[0] == tag_lo && f1[1] == tag_hi)) break;
          }
          SIG_SET(F_YG, 1);
          SIG_WAIT(F_KR, 8);
          YSUM(2);
          SIG_WAIT(F_YP, 3);
          f32x4 t = {0, 0, 0, 0};
#pragma unroll
          for (int k = 0; k < 18; k++) t += *(LAS f32x4*)(sm + L_YS + (10 * k + (l < 10 ? l : 0)) * 16);
          u32 rb = rec[0] >> 16;
          rb = (rb & 0x8000u) ? (rb & 0x7FFFu) : (~rb & 0xFFFFu);
          const float sig = rbf(rcp(1.f + __expf(-bflo(rb))));
          if (l < 10) *(LAS f32x4*)(sm + L_Y + l * 16) = f32x4{rbf(rbf(t[0]) * sig), rbf(rbf(t[1]) * sig), rbf(rbf(t[2]) * sig), rbf(rbf(t[3]) * sig)};
        }
        SIG_SET(F_Y, idx + 1);
        STAMP(10);
      }
    }
  } else {
    const int tg = w & 3, kh = w >> 2, tw = 2 * tg + kh;
    u32x4 xf[20];
    const char* xb = (const char*)x + rt * (128 * 1280) + kh * 640;
    const u32 xoff = (u32)((((g >> 1) ? (tw ^ 1) : tw) * 16 + 4 * q4) * 1280 + (g & 1) * 64 + p4 * 16);
#define XI(c, r) ((r) < 2 ? 2 * (c) + (r) : 8 + 2 * (c) + (r))
#define XL1(c, r) asm volatile("global_load_dwordx4 %0, %1, %2 offset:%3" : "=v"(xf[XI(c, r)]) : "v"(xoff), "s"(xb + CROT(c) * 128), "i"((r) * 1280))
#define XG(c) do { XL1(c, 0); XL1(c, 1); XL1(c, 2); XL1(c, 3); } while (0)
    __builtin_amdgcn_s_setprio(2);
    XG(0);
    BARRIER();
    XG(1); XG(2); XG(3); XG(4);
    __builtin_amdgcn_s_setprio(0);
    STAMP(1);
    f32x4 acc[2][5];
#pragma unroll
    for (int t = 0; t < 5; t++) { acc[0][t] = f32x4{0, 0, 0, 0}; acc[1][t] = f32x4{0, 0, 0, 0}; }
    LAS char* w1p = sm + L_W1 + kh * 5120 + (4 * g + q4) * 160 + 8 * p4;
    const u64 mE = 0x5555555555555555ull, mO = ~mE, mL = 0x3333333333333333ull, mH = ~mL;
    u32 ta[4], ua[4];
#define DPX1 " quad_perm:[1,0,3,2] row_mask:0xf bank_mask:0xf\n\t"
#define DPX2 " quad_perm:[2,3,0,1] row_mask:0xf bank_mask:0xf\n\t"
#define CD2(M, X, d0, s0, k0, d1, s1, k1) asm volatile("s_mov_b64 vcc, %6\n\tv_cndmask_b32_dpp %0, %2, %3, vcc" X "v_cndmask_b32_dpp %1, %4, %5, vcc" X \
      : "=&v"(d0), "=&v"(d1) : "v"(s0), "v"(k0), "v"(s1), "v"(k1), "s"(M) : "vcc")
#define CD2I(M, X, d0, s0, d1, s1) asm volatile("s_mov_b64 vcc, %4\n\tv_cndmask_b32_dpp %0, %2, %0, vcc" X "v_cndmask_b32_dpp %1, %3, %1, vcc" X \
      : "+v"(d0), "+v"(d1) : "v"(s0), "v"(s1), "s"(M) : "vcc")
#define A_(c, r) xf[XI(c, r)]
#define TP(c, k) do { const int d_ = (k) & 3; u32 n0, n1; \
      if ((k) < 4) { CD2(mE, DPX1, n0, A_(c, 1)[d_], A_(c, 0)[d_], n1, A_(c, 3)[d_], A_(c, 2)[d_]); ua[d_] = n0; ta[d_] = n1; } \
      else if ((k) < 8) { n0 = A_(c, 1)[d_]; n1 = A_(c, 3)[d_]; CD2I(mO, DPX1, n0, A_(c, 0)[d_], n1, A_(c, 2)[d_]); A_(c, 1)[d_] = n0; A_(c, 3)[d_] = n1; } \
      else if ((k) < 12) { CD2(mL, DPX2, n0, ta[d_], ua[d_], n1, A_(c, 3)[d_], A_(c, 1)[d_]); A_(c, 0)[d_] = n0; A_(c, 2)[d_] = n1; } \
      else if ((k) < 16) { n1 = A_(c, 3)[d_]; CD2I(mH, DPX2, ta[d_], ua[d_], n1, A_(c, 1)[d_]); A_(c, 3)[d_] = n1; } \
      else { auto r0 = __builtin_amdgcn_permlane32_swap(A_(c, 0)[d_], ta[d_], false, false); auto r1 = __builtin_amdgcn_permlane32_swap(A_(c, 2)[d_], A_(c, 3)[d_], false, false); \
        A_(c, 0)[d_] = r0[0]; A_(c, 2)[d_] = r0[1]; A_(c, 1)[d_] = r1[0]; A_(c, 3)[d_] = r1[1]; } } while (0)
#define XW(N, c) asm volatile("s_waitcnt vmcnt(" #N ")" : "+v"(A_(c, 0)), "+v"(A_(c, 1)), "+v"(A_(c, 2)), "+v"(A_(c, 3)) :: "memory")
#define RD(F, j, r) F[r] = trd(w1p + (j) * 10240 + 32 * ((r) >> 1) + 2560 * ((r) & 1))
#define MF(F, j, k) acc[(k) & 1][(k) >> 1] = mfma32(FR(F, (k) >> 1), xf[10 * ((k) & 1) + (j)], acc[(k) & 1][(k) >> 1])
#define STEPA(FA, FB, c, V) do { _Pragma("unroll") for (int k = 0; k < 10; k++) { MF(FA, 2 * (c), k); RD(FB, 2 * (c) + 1, k); if ((c) < 4) TP((c) + 1, k); V(k); SB(); } } while (0)
#define STEPB(FA, FB, c) do { _Pragma("unroll") for (int k = 0; k < 4; k++) { MF(FB, 2 * (c) + 1, k); TP((c) + 1, 10 + k); SB(); } \
      if ((c) == 2) SIG_ADD(F_P1); \
      if ((c) == 3) SIG_WAIT2(F_W0 + kh, (c) + 2, F_WP, 1); else SIG_WAIT(F_W0 + kh, (c) + 2); \
      _Pragma("unroll") for (int k = 4; k < 10; k++) { if (k < 9) { RD(FA, 2 * (c) + 2, 2 * (k - 4)); RD(FA, 2 * (c) + 2, 2 * (k - 4) + 1); } MF(FB, 2 * (c) + 1, k); TP((c) + 1, 10 + k); SB(); } } while (0)
    {
      u32x2 fa[10], fb[10];
      XW(16, 0);
      u32 fil = l;
#pragma unroll
      for (int k = 0; k < 20; k++) { TP(0, k); asm volatile("v_add_u32 %0, 1, %0" : "+v"(fil)); SB(); }
      SIG_WAIT(F_W0 + kh, 1);
      STAMP(2);
#pragma unroll
      for (int r = 0; r < 10; r++) RD(fa, 0, r);
      SB();
#define V3(k) do { if ((k) == 1) XL1(3, 0); if ((k) == 3) XL1(3, 1); if ((k) == 5) XL1(3, 2); if ((k) == 7) XL1(3, 3); } while (0)
#define V4(k) do { if ((k) == 1) XL1(4, 0); if ((k) == 3) XL1(4, 1); if ((k) == 5) XL1(4, 2); if ((k) == 7) XL1(4, 3); } while (0)
#define VN(k) do { } while (0)
      XW(12, 1);
      STEPA(fa, fb, 0, VN); STEPB(fa, fb, 0);
      STAMP(3);
      XW(8, 2);
      STEPA(fa, fb, 1, VN); STEPB(fa, fb, 1);
      STAMP(4);
      XW(4, 3);
      STEPA(fa, fb, 2, VN); STEPB(fa, fb, 2);
      STAMP(5);
      XW(0, 4);
      STEPA(fa, fb, 3, VN); STEPB(fa, fb, 3);
      STAMP(6);
      STEPA(fa, fb, 4, VN);
#pragma unroll
      for (int k = 0; k < 10; k++) MF(fb, 9, (k + 1 - 2 * (k & 1)));
    }
    STAMP(7);
    {
    int l_ = __builtin_amdgcn_mbcnt_hi(~0u, __builtin_amdgcn_mbcnt_lo(~0u, 0u));
    asm volatile("" : "+v"(l_));
    const int l = l_, tid = w * 64 + l, g = l >> 4, i16 = l & 15, q4 = i16 >> 2, p4 = l & 3;
    {
      const int tp = tw ^ 1;
      *(LAS f32x4*)(sm + L_PHI + tp * 2560 + l * 16) = acc[1][0];
      *(LAS f32x4*)(sm + L_PHI + tp * 2560 + 1024 + l * 16) = acc[1][1];
      *(LAS f32x4*)(sm + L_V + tp * 1536 + l * 16) = acc[1][2];
      *(LAS f32x4*)(sm + L_EXA + tp * 2048 + l * 16) = acc[1][3];
      *(LAS f32x4*)(sm + L_EXA + tp * 2048 + 1024 + l * 16) = acc[1][4];
      SIG_SET(F_EX + tp, 1);
    }
    u32x4 wa32[3]; u32x2 wa16[3];
    {
      const int hp = p4 >> 1;
      const int d0 = 4 * g + q4, d1 = d0 + 16, d2 = d0 + 32;
      LAS char* a0 = sm + (hp == 0 ? L_WPHI + d0 * 48 + 24 * (p4 & 1) : L_ZERO);
      LAS char* a1 = sm + ((d1 >= 20) == (hp == 1) ? L_WPHI + d1 * 48 + 24 * (p4 & 1) : L_ZERO);
      LAS char* a2 = sm + ((hp == 1 && d2 < 40) ? L_WPHI + d2 * 48 + 24 * (p4 & 1) : L_ZERO);
#pragma unroll
      for (int mt = 0; mt < 3; mt++) {
        u32x2 e0 = trd(a0 + 8 * mt), e1 = trd(a1 + 8 * mt);
        wa32[mt] = u32x4{e0[0], e0[1], e1[0], e1[1]};
        wa16[mt] = trd(a2 + 8 * mt);
      }
    }
    SIG_WAIT2(F_EX + tw, 1, F_CS, 2);
    STAMP(8);
    const u32 msv = *(LAS u32*)(sm + L_MS + (tw * 16 + i16) * 4);
    const float m_b = bflo(msv), rs = rcp(bfhi(msv));
    f32x4 cc[5], a[5];
#pragma unroll
    for (int t = 0; t < 3; t++) cc[t] = *(LAS f32x4*)(sm + L_CSP + (16 * t + 4 * g) * 4);
    a[0] = acc[0][0] + *(LAS f32x4*)(sm + L_PHI + tw * 2560 + l * 16);
    a[1] = acc[0][1] + *(LAS f32x4*)(sm + L_PHI + tw * 2560 + 1024 + l * 16);
    a[2] = acc[0][2] + *(LAS f32x4*)(sm + L_V + tw * 1536 + l * 16);
    {
      u32 kn[5][2];
#define NRM(A, t, C, O) do { f32x4 v_ = (A - m_b * C) * rs; O[t][0] = pk2(v_[0], v_[1]); O[t][1] = pk2(v_[2], v_[3]); } while (0)
      NRM(a[0], 0, cc[0], kn); NRM(a[1], 1, cc[1], kn); NRM(a[2], 2, cc[2], kn);
      SB();
      cc[3] = *(LAS f32x4*)(sm + L_CSP + (48 + 4 * g) * 4); cc[4] = *(LAS f32x4*)(sm + L_CSP + (64 + 4 * g) * 4);
      a[3] = acc[0][3] + *(LAS f32x4*)(sm + L_EXA + tw * 2048 + l * 16);
      a[4] = acc[0][4] + *(LAS f32x4*)(sm + L_EXA + tw * 2048 + 1024 + l * 16);
      u32x4 b32 = {kn[0][0], kn[0][1], kn[1][0], kn[1][1]};
      u32x2 b16 = {kn[2][0], kn[2][1]};
      f32x4 pp[3];
#pragma unroll
      for (int mt = 0; mt < 3; mt++) {
        pp[mt] = mfma32(wa32[mt], b32, f32x4{0, 0, 0, 0});
        pp[mt] = mfma16(wa16[mt], b16, pp[mt]);
      }
      NRM(a[3], 3, cc[3], kn); NRM(a[4], 4, cc[4], kn);
      __builtin_amdgcn_wave_barrier();
      LAS char* vr = sm + L_V + (16 * tw + i16) * 96;
      u32x2 v2 = (g >= 2) ? u32x2{kn[2][0], kn[2][1]} : u32x2{g == 0 ? 0x3F80u : 0u, 0u};
      *(LAS u32x2*)(vr + (g >= 2 ? 8 * (g - 2) : 80 + 8 * g)) = v2;
      *(LAS u32x2*)(vr + 16 + 8 * g) = u32x2{kn[3][0], kn[3][1]};
      *(LAS u32x2*)(vr + 48 + 8 * g) = u32x2{kn[4][0], kn[4][1]};
      u32 ph[10];
      softmax_pack(pp[0], pp[1], pp[2], ph);
      LAS char* pr = sm + L_PHI + (16 * tw + i16) * 160 + 40 * g;
#pragma unroll
      for (int k = 0; k < 5; k++) *(LAS u32x2*)(pr + 8 * k) = u32x2{ph[2 * k], ph[2 * k + 1]};
    }
    STAMP(9);
    SIG_ADD(F_PHI);
    __builtin_amdgcn_s_setprio(2);
    {
      const int mt0 = (0x22111000 >> (4 * w)) & 3, nt0 = (0x10210210 >> (4 * w)) & 3;
      const int mt1 = (0x22144332 >> (4 * w)) & 7, nt1 = (0x10221212 >> (4 * w)) & 3;
      u32 ap0 = L_PHI + (4 * g + q4) * 160 + 8 * p4 + 32 * mt0, ap1 = ap0 + 32 * (mt1 - mt0);
      u32 bp0 = L_V + (4 * g + q4) * 96 + 8 * p4 + 32 * nt0, bp1 = bp0 + 32 * (nt1 - nt0);
#define KVA(mt, nt, en) ({ const int fq = 4 * (mt) + g, col = 16 * (nt) + i16; int c = col - (fq >= 10 ? 20 : 0); bool ok = (c >= 0 && c < 20); \
        if (col == 40) { c = 20; ok = true; } (ok && (en)) ? reg + R_KVP + (fq * 21 + c) * 16 : WS_END; })
      u32 ks0 = KVA(mt0, nt0, true), ks1 = KVA(mt1, nt1, w < 5);
      asm volatile("" : "+v"(ap0), "+v"(ap1), "+v"(bp0), "+v"(bp1), "+v"(ks0), "+v"(ks1));
      SIG_WAIT(F_PHI, 9);
      STAMP(10);
      f32x4 d0 = {0, 0, 0, 0}, d1 = {0, 0, 0, 0};
#pragma unroll
      for (int u = 0; u < 4; u++) {
        d0 = mfma32(trd2(sm + ap0 + u * 5120, 2560), trd2(sm + bp0 + u * 3072, 1536), d0);
        d1 = mfma32(trd2(sm + ap1 + u * 5120, 2560), trd2(sm + bp1 + u * 3072, 1536), d1);
      }
      ST4(san4(__builtin_bit_cast(u32x4, d0)), ks0); ST4(san4(__builtin_bit_cast(u32x4, d1)), ks1);
    }
    SIG_ADD(F_KR);
    SIG_WAIT(F_Q0 + kh, 1);
    STAMP(11);
    f32x4 aq[2][3];
#pragma unroll
    for (int t = 0; t < 3; t++) { aq[0][t] = f32x4{0, 0, 0, 0}; aq[1][t] = f32x4{0, 0, 0, 0}; }
    {
      LAS char* wqp = sm + L_WQ + kh * 3072 + (4 * g + q4) * 96 + 8 * p4;
      u32x2 fa[6], fb[6];
#define RQ(F, P_, o, r) F[r] = trd(P_ + (o) + 32 * ((r) >> 1) + 1536 * ((r) & 1))
#define STEPQ(FA, FB, j) do { _Pragma("unroll") for (int k = 0; k < 6; k++) { aq[k & 1][k >> 1] = mfma32(FR(FA, k >> 1), xf[10 * (k & 1) + (j)], aq[k & 1][k >> 1]); \
          if ((j) < 9) RQ(FB, wqp, ((j) + 1) * 6144, k); SB(); } } while (0)
#pragma unroll
      for (int r = 0; r < 6; r++) RQ(fa, wqp, 0, r);
      SB();
#pragma unroll
      for (int j = 0; j < 10; j += 2) { STEPQ(fa, fb, j); STEPQ(fb, fa, j + 1);
      }
    }
#pragma unroll
    for (int t = 0; t < 3; t++) *(LAS f32x4*)(sm + L_EXQ + (tw ^ 1) * 3072 + t * 1024 + l * 16) = aq[1][t];
    SIG_SET(F_EX + (tw ^ 1), 2);
    SIG_ADD(F_P2);
    STAMP(12);
    SIG_WAIT2(F_EX + tw, 2, F_CSQ, 2);
    STAMP(13);
    const u32 slo0 = (rt * 128 + tw * 16 + l / 5) * 1280 + og * 80 + (l % 5) * 16;
    const u32 slo1 = (rt * 128 + tw * 16 + (64 + i16) / 5) * 1280 + og * 80 + ((64 + i16) % 5) * 16;
    SIG_WAIT(F_KR, 8);
    glds16(x, slo0, ldsb + L_V + tw * 1536);
    if (l < 16) glds16(x, slo1, ldsb + L_V + tw * 1536 + 1024);
    u32 pq[10];
    {
      f32x4 aqo[3], c2[3];
      u32 qn[3][2];
#pragma unroll
      for (int t = 0; t < 3; t++) {
        c2[t] = *(LAS f32x4*)(sm + L_CSQ + (16 * t + 4 * g) * 4) + *(LAS f32x4*)(sm + L_CSQ + (48 + 16 * t + 4 * g) * 4);
        aqo[t] = aq[0][t] + *(LAS f32x4*)(sm + L_EXQ + tw * 3072 + t * 1024 + l * 16);
        NRM(aqo[t], t, c2[t], qn);
      }
      u32x4 b32 = {qn[0][0], qn[0][1], qn[1][0], qn[1][1]};
      u32x2 b16 = {qn[2][0], qn[2][1]};
      f32x4 pp[3];
#pragma unroll
      for (int mt = 0; mt < 3; mt++) {
        pp[mt] = mfma32(wa32[mt], b32, f32x4{0, 0, 0, 0});
        pp[mt] = mfma16(wa16[mt], b16, pp[mt]);
      }
      softmax_pack(pp[0], pp[1], pp[2], pq);
#pragma unroll
      for (int k = 0; k < 10; k++) asm volatile("" : "+v"(pq[k]));
    }
    STAMP(14);
    u32 oad[3];
#pragma unroll
    for (int mt = 0; mt < 3; mt++) {
      const int col = mt < 2 ? 8 * q4 + 4 * mt + p4 : 32 + 2 * q4 + p4;
      const int hp = (mt == 2) ? (p4 == 2 ? 0 : 1) : (col >= 20 ? 1 : 0);
      const int row = (mt == 2 && p4 >= 2) ? hp * 21 + 20 : col + hp;
      oad[mt] = ((g >> 1) == hp) ? (u32)(L_KVT + row * 80 + 40 * (g & 1)) : L_ZERO;
      asm volatile("" : "+v"(oad[mt]));
    }
    u32x2 xv[3], xnp[3];
    asm volatile("s_waitcnt vmcnt(0)" ::: "memory");
    {
      LAS char* xp = sm + L_V + tw * 1536 + i16 * 80 + 8 * g;
#pragma unroll
      for (int t = 0; t < 3; t++) {
        xv[t] = *(LAS u32x2*)(xp + 32 * t);
        u32 d0 = pk2(bflo(xv[t][0]) - m_b, bfhi(xv[t][0]) - m_b), d1 = pk2(bflo(xv[t][1]) - m_b, bfhi(xv[t][1]) - m_b);
        xnp[t] = u32x2{pk2(bflo(d0) * rs, bfhi(d0) * rs), pk2(bflo(d1) * rs, bfhi(d1) * rs)};
        asm volatile("" : "+v"(xnp[t]));
      }
    }
    const u32 obase = WS_REG + (rt * 16 + 8 * kh) * RS + R_OB + tg * 2560;
    u32 oa[5];
#pragma unroll
    for (int k = 0; k < 5; k++) { int C = 64 * k + l, pr = C / 80; oa[k] = obase + pr * (RS - 1280) + C * 16; asm volatile("" : "+v"(oa[k])); }
    STAMP(15);
    SIG_WAIT(F_KVT, 1);
    STAMP(16);
    {
      f32x4 oo[3];
#pragma unroll
      for (int mt = 0; mt < 3; mt++) {
        LAS char* ap = sm + oad[mt];
        u32x2 a0 = *(LAS u32x2*)(ap), a1 = *(LAS u32x2*)(ap + 8), a2 = *(LAS u32x2*)(ap + 16), a3 = *(LAS u32x2*)(ap + 24), a4 = *(LAS u32x2*)(ap + 32);
        oo[mt] = mfma32(u32x4{a0[0], a0[1], a1[0], a1[1]}, u32x4{pq[0], pq[1], pq[2], pq[3]}, f32x4{0, 0, 0, 0});
        oo[mt] = mfma32(u32x4{a2[0], a2[1], a3[0], a3[1]}, u32x4{pq[4], pq[5], pq[6], pq[7]}, oo[mt]);
        oo[mt] = mfma16(a4, u32x2{pq[8], pq[9]}, oo[mt]);
      }
      const float r0 = rcp(oo[2][2] + 1e-6f), r1 = rcp(oo[2][3] + 1e-6f);
      const float ra_ = g <= 2 ? r0 : r1, rb_ = g <= 1 ? r0 : r1;
      u32x4 o8 = {pk2(oo[0][0] * ra_, oo[0][1] * ra_), pk2(oo[0][2] * ra_, oo[0][3] * ra_), pk2(oo[1][0] * rb_, oo[1][1] * rb_), pk2(oo[1][2] * rb_, oo[1][3] * rb_)};
      u32 o2 = pk2(oo[2][0] * r1, oo[2][1] * r1);
      const u32 ob = reg + R_OB + tw * 1280 + l * 16;
      ST4(o8, ob);
      auto ra = __builtin_amdgcn_permlane32_swap(o2, o2, false, false);
      auto rb = __builtin_amdgcn_permlane16_swap(ra[0], ra[0], false, false);
      auto rc2 = __builtin_amdgcn_permlane16_swap(ra[1], ra[1], false, false);
      if (g == 0) ST4((u32x4{rb[0], rb[1], rc2[0], rc2[1]}), ob + 1024);
      asm volatile("s_waitcnt vmcnt(0)" ::: "memory");
      SIG_ADD(F_OS);
    }
    MEMFENCE();
    STAMP(17);
    f32x4 ao[2][3];
    int idx;
    {
      LAS char* wop = sm + (kh ? L_WO1 : L_WO0) + (4 * g + q4) * 96 + 8 * p4;
      SIG_WAIT2(F_WO, 2, F_IDX, 1);
      idx = __builtin_amdgcn_readfirstlane((int)*(volatile LAS u32*)(sm + L_SYN + F_IDX * 4) - 1);
      SIG_WAIT(kh ? F_O1 : F_O, 1);
      STAMP(19);
      {
        u32x4 of[2][10];
#pragma unroll
        for (int j = 0; j < 10; j++) {
          of[0][j] = __builtin_amdgcn_raw_buffer_load_b128(wsr, oa[j % 5], (j / 5) * 4 * RS + kh * 1280, 16);
          of[1][j] = __builtin_amdgcn_raw_buffer_load_b128(wsr, oa[j % 5], (j / 5) * 4 * RS + (1 - kh) * 1280, 16);
        }
#pragma unroll
        for (int t = 0; t < 3; t++) { ao[0][t] = f32x4{0, 0, 0, 0}; ao[1][t] = f32x4{0, 0, 0, 0}; }
        u32x2 fa[6], fb[6];
#define STEPO(FA, FB, j) do { _Pragma("unroll") for (int k = 0; k < 6; k++) { ao[k & 1][k >> 1] = mfma32(FR(FA, k >> 1), of[k & 1][j], ao[k & 1][k >> 1]); \
            if ((j) < 9) RQ(FB, wop, ((j) + 1) * 3072, k); SB(); } } while (0)
#pragma unroll
        for (int r = 0; r < 6; r++) RQ(fa, wop, 0, r);
        SB();
#pragma unroll
        for (int j = 0; j < 10; j += 2) { STEPO(fa, fb, j); STEPO(fb, fa, j + 1); }
      }
    }
#pragma unroll
    for (int t = 0; t < 3; t++) *(LAS f32x4*)(sm + L_EXQ + (tw ^ 1) * 3072 + t * 1024 + l * 16) = ao[1][t];
    SIG_SET(F_EX + (tw ^ 1), 3);
    STAMP(20);
    SIG_WAIT(F_EX + tw, 3);
    STAMP(21);
    {
      LAS char* xp = sm + L_V + tw * 1536 + i16 * 80 + 8 * g;
      if ((idx >> 4) == rt * 8 + tw) SIG_WAIT(F_Y, 1);
      if (idx == rt * 128 + tw * 16 + i16) {
#pragma unroll
        for (int t = 0; t < 3; t++) if (t < 2 || g < 2) {
          f32x4 yy = *(LAS f32x4*)(sm + L_Y + (16 * t + 4 * g) * 4);
          xnp[t] = u32x2{pk2(bflo(xnp[t][0]) + yy[0], bfhi(xnp[t][0]) + yy[1]), pk2(bflo(xnp[t][1]) + yy[2], bfhi(xnp[t][1]) + yy[3])};
        }
      }
#pragma unroll
      for (int t = 0; t < 3; t++) {
        f32x4 af = ao[0][t] + *(LAS f32x4*)(sm + L_EXQ + tw * 3072 + t * 1024 + l * 16);
        u32 p0 = pk2(af[0], af[1]), p1 = pk2(af[2], af[3]);
        u32 q0 = pk2(bflo(xv[t][0]) + bflo(p0), bfhi(xv[t][0]) + bfhi(p0)), q1 = pk2(bflo(xv[t][1]) + bflo(p1), bfhi(xv[t][1]) + bfhi(p1));
        u32x2 res = {pk2(bflo(q0) + bflo(xnp[t][0]), bfhi(q0) + bfhi(xnp[t][0])), pk2(bflo(q1) + bflo(xnp[t][1]), bfhi(q1) + bfhi(xnp[t][1]))};
        if (t < 2 || g < 2) *(LAS u32x2*)(xp + 32 * t) = res;
      }
      __builtin_amdgcn_wave_barrier();
      LAS char* op = sm + L_V + tw * 1536 + l * 16;
      *(u32x4*)((char*)out + slo0) = *(LAS u32x4*)(op);
      if (l < 16) *(u32x4*)((char*)out + slo1) = *(LAS u32x4*)(op + 1024);
    }
    STAMP(22);
    }
  }
}

extern "C" void kernel_launch(void* const* d_in, const int* in_sizes, int n_in, void* d_out, int out_size,
                              void* d_ws, size_t ws_size, hipStream_t stream) {
  fused_kernel<<<256, 768, 0, stream>>>((const u16*)d_in[0], (const u16*)d_in[1], (const u16*)d_in[2], (const u16*)d_in[3],
                                        (const u16*)d_in[5], (const u16*)d_in[6], (const u16*)d_in[7], (const u16*)d_in[8],
                                        (u16*)d_out, (char*)d_ws);
}
```

```cpp
#include <hip/hip_runtime.h>
#include <stdint.h>

typedef unsigned short u16;
typedef unsigned int u32;
typedef unsigned long long u64;
typedef __attribute__((ext_vector_type(8))) __bf16 bf16x8;
typedef __attribute__((ext_vector_type(4))) short s16x4;
typedef __attribute__((ext_vector_type(4))) float f32x4;
typedef __attribute__((ext_vector_type(2))) float f32x2;
typedef __attribute__((ext_vector_type(2))) __bf16 bf16x2;
typedef __attribute__((ext_vector_type(4))) u32 u32x4;
typedef __attribute__((ext_vector_type(2))) u32 u32x2;
#define LAS __attribute__((address_space(3)))
#define DI __device__ __forceinline__

#define FS 8
constexpr u32 WS_EPOCH = 0;
constexpr u32 WS_REC = 8192;
constexpr u32 WS_ST = 16384;
constexpr u32 WS_REG = 49152;
constexpr u32 RS = 23936;
constexpr u32 R_OB = 0;
constexpr u32 R_KVP = 10240;
constexpr u32 R_YP = 17024;
constexpr u32 R_GUP = 19584;
constexpr u32 R_KVR = 20608;
constexpr u32 R_I1 = 20864;
constexpr u32 R_I3 = 21760;
constexpr u32 R_IG = 21120;
constexpr u32 R_IY = 21760;
constexpr u32 WS_YB = WS_REG + 256 * RS;
constexpr u32 WS_MG = WS_YB + 327680;
constexpr u32 MGS = 1024 + 40 * 128;
constexpr u32 WS_IY = WS_MG + 128 * MGS;
constexpr u32 WS_END = WS_IY + 2048 * 128;
constexpr u32 WS_STAMP = 128u << 20;

constexpr u32 L_W1 = 0;
constexpr u32 L_WQ = 0;
constexpr u32 L_WO0 = 30720;
constexpr u32 L_WO1 = 92160;
constexpr u32 L_EXQ = 61440;
constexpr u32 L_CSP = 102400;
constexpr u32 L_EXA = 103424;
constexpr u32 L_PF = 162816;
constexpr u32 L_PHI = 122880;
constexpr u32 L_V = 143360;
constexpr u32 L_SM = 155648;
constexpr u32 L_WPHI = L_SM;
constexpr u32 L_ZERO = L_SM + 1920;
constexpr u32 L_KVT = L_SM + 1984;
constexpr u32 L_CSQ = L_KVT + 3360;
constexpr u32 L_KEY = L_CSQ + 768;
constexpr u32 L_IDX = L_KEY + 64;
constexpr u32 L_Y = L_IDX + 64;
constexpr u32 L_MS = L_Y + 160;
constexpr u32 L_SYN = L_MS + 512;
enum { F_W0 = 0, F_W1, F_ST, F_CS, F_P1, F_Q0, F_Q1, F_PHI, F_KV, F_CSQ, F_P2, F_KVT, F_WO, F_OS, F_O, F_Y, F_EX = 16, F_WP = 24, F_KR, F_IDX, F_YG, F_YP, F_O1 };
constexpr u32 L_YS = L_PHI;
constexpr u32 L_TOTAL = 163840;

constexpr u32 SENT = 0xFFFFFFFFu;
constexpr u32 SPIN_CAP = 1u << 15;
#define HOP1_AT 6

DI u32 pk2(float a, float b) { f32x2 v = {a, b}; return __builtin_bit_cast(u32, __builtin_convertvector(v, bf16x2)); }
DI float bflo(u32 u) { return __uint_as_float(u << 16); }
DI float bfhi(u32 u) { return __uint_as_float(u & 0xFFFF0000u); }
DI float rbf(float a) { return bflo(pk2(a, 0.f)); }
DI float rcp(float a) { return __builtin_amdgcn_rcpf(a); }
DI float dot2(u32 a, u32 b, float c) { return __builtin_amdgcn_fdot2_f32_bf16(__builtin_bit_cast(bf16x2, a), __builtin_bit_cast(bf16x2, b), c, false); }
DI f32x4 mfma32(u32x4 a, u32x4 b, f32x4 c) {
  return __builtin_amdgcn_mfma_f32_16x16x32_bf16(__builtin_bit_cast(bf16x8, a), __builtin_bit_cast(bf16x8, b), c, 0, 0, 0);
}
DI f32x4 mfma16(u32x2 a, u32x2 b, f32x4 c) {
  return mfma32(u32x4{a[0], a[1], 0u, 0u}, u32x4{b[0], b[1], 0u, 0u}, c);
}
DI u32x2 trd(LAS char* p) { return __builtin_bit_cast(u32x2, __builtin_amdgcn_ds_read_tr16_b64_v4i16((LAS s16x4*)p)); }
DI u32x4 trd2(LAS char* p, u32 d) { u32x2 a = trd(p), b = trd(p + d); return u32x4{a[0], a[1], b[0], b[1]}; }
DI void glds16(const void* sbase, u32 voff, u32 lds_dst) {
  asm volatile("s_mov_b32 m0, %2\n\ts_nop 0\n\tglobal_load_lds_dwordx4 %0, %1" :: "v"(voff), "s"(sbase), "s"(lds_dst) : "memory");
}
DI u32 umin_(u32 a, u32 b) { return a < b ? a : b; }
DI u32 umax_(u32 a, u32 b) { return a > b ? a : b; }
DI u32 san(u32 v) { return umin_(v, 0xFFFFFFFEu); }
DI u32x4 san4(u32x4 v) { return u32x4{san(v[0]), san(v[1]), san(v[2]), san(v[3])}; }
DI u32 max4(u32x4 v) { return umax_(umax_(v[0], v[1]), umax_(v[2], v[3])); }
DI float xchg16_max(float v) { auto r = __builtin_amdgcn_permlane16_swap(__float_as_uint(v), __float_as_uint(v), false, false); return fmaxf(__uint_as_float(r[0]), __uint_as_float(r[1])); }
DI float xchg16_min(float v) { auto r = __builtin_amdgcn_permlane16_swap(__float_as_uint(v), __float_as_uint(v), false, false); return fminf(__uint_as_float(r[0]), __uint_as_float(r[1])); }
DI float xchg16_add(float v) { auto r = __builtin_amdgcn_permlane16_swap(__float_as_uint(v), __float_as_uint(v), false, false); return __uint_as_float(r[0]) + __uint_as_float(r[1]); }
DI float xchg32_add(float v) { auto r = __builtin_amdgcn_permlane32_swap(__float_as_uint(v), __float_as_uint(v), false, false); return __uint_as_float(r[0]) + __uint_as_float(r[1]); }
template <int CTRL> DI float dpp(float v) { return __int_as_float(__builtin_amdgcn_update_dpp(0, __float_as_int(v), CTRL, 0xf, 0xf, true)); }
template <int CTRL> DI u32 dppu(u32 v) { return (u32)__builtin_amdgcn_update_dpp(0, (int)v, CTRL, 0xf, 0xf, true); }
DI float wave_sum(float v) {
  v += dpp<0xB1>(v); v += dpp<0x4E>(v); v += dpp<0x141>(v); v += dpp<0x140>(v);
  return xchg32_add(xchg16_add(v));
}
DI float row_sum(float v) { v += dpp<0xB1>(v); v += dpp<0x4E>(v); v += dpp<0x141>(v); return v + dpp<0x140>(v); }
DI u32 row_max(u32 v) { v = umax_(v, dppu<0xB1>(v)); v = umax_(v, dppu<0x4E>(v)); v = umax_(v, dppu<0x141>(v)); return umax_(v, dppu<0x140>(v)); }
DI u32x4 ld4w(const char* base, u32 off) { u32x4 v; asm volatile("global_load_dwordx4 %0, %1, %2 sc1\n\ts_waitcnt vmcnt(0)" : "=&v"(v) : "v"(off), "s"(base) : "memory"); return v; }
DI u32x2 ld2w(const char* base, u32 off) { u32x2 v; asm volatile("global_load_dwordx2 %0, %1, %2 sc1\n\ts_waitcnt vmcnt(0)" : "=&v"(v) : "v"(off), "s"(base) : "memory"); return v; }
#define MEMFENCE() asm volatile("" ::: "memory")
#define BARRIER() asm volatile("s_waitcnt lgkmcnt(0)\n\ts_barrier" ::: "memory")
#define SB() __builtin_amdgcn_sched_barrier(0)
#define LD4(off) __builtin_amdgcn_raw_buffer_load_b128(wsr, (off), 0, 16)
#define LD2(off) __builtin_amdgcn_raw_buffer_load_b64(wsr, (off), 0, 16)
#define ST4(v, off) __builtin_amdgcn_raw_buffer_store_b128((v), wsr, (off), 0, 16)
#define ST2(v, off) __builtin_amdgcn_raw_buffer_store_b64((v), wsr, (off), 0, 16)

#define STAMP(k) do { } while (0)

DI void softmax_pack(const f32x4& p0, const f32x4& p1, const f32x4& p2, u32 (&o)[10]) {
  float p[10] = {p0[0], p0[1], p0[2], p0[3], p1[0], p1[1], p1[2], p1[3], p2[0], p2[1]};
  float mx = p[0], mn = p[0];
#pragma unroll
  for (int i = 1; i < 10; i++) { mx = fmaxf(mx, p[i]); mn = fminf(mn, p[i]); }
  mx = xchg16_max(mx); mn = xchg16_min(mn);
  const float L2E = 1.4426950408889634f;
  const float a = -mx * L2E, b = mn * L2E;
  float e1[10], e2[10], s1 = 0.f, s2 = 0.f;
#pragma unroll
  for (int i = 0; i < 10; i++) {
    e1[i] = __builtin_amdgcn_exp2f(fmaf(p[i], L2E, a));
    e2[i] = __builtin_amdgcn_exp2f(fmaf(p[i], -L2E, b));
    s1 += e1[i]; s2 += e2[i];
  }
  s1 = xchg16_add(s1); s2 = xchg16_add(s2);
  const float r1 = rcp(s1), r2 = rcp(s2);
#pragma unroll
  for (int i = 0; i < 5; i++) {
    o[i] = pk2(e1[2 * i] * r1, e1[2 * i + 1] * r1);
    o[5 + i] = pk2(e2[2 * i] * r2, e2[2 * i + 1] * r2);
  }
}

#define THR "3"

#define SIG_SET(f, v) do { MEMFENCE(); *(volatile LAS u32*)(sm + L_SYN + (f) * 4) = (v); MEMFENCE(); } while (0)
DI void sig_add(u32 addr) {
  u64 keep; u32 one = 1;
  asm volatile("s_mov_b64 %0, exec\n\ts_mov_b64 exec, 1\n\tds_add_u32 %1, %2\n\ts_mov_b64 exec, %0" : "=&s"(keep) : "v"(addr), "v"(one) : "memory");
}
#define SIG_ADD(f) sig_add(ldsb + L_SYN + (f) * 4)
DI void sig_wait(u32 addr, u32 val) {
  u32 tv, ts, cnt;
  asm volatile(
      "s_mov_b32 %2, 0x400000\n"
      "LW%=:\n\t"
      "ds_read_b32 %0, %3\n\t"
      "s_waitcnt lgkmcnt(0)\n\t"
      "v_readfirstlane_b32 %1, %0\n\t"
      "s_nop 1\n\t"
      "s_cmp_ge_u32 %1, %4\n\t"
      "s_cbranch_scc1 LD%=\n\t"
      "s_sleep 1\n\t"
      "s_sub_u32 %2, %2, 1\n\t"
      "s_cmp_lg_u32 %2, 0\n\t"
      "s_cbranch_scc1 LW%=\n"
      "LD%=:"
      : "=&v"(tv), "=&s"(ts), "=&s"(cnt) : "v"(addr), "s"(val) : "memory", "scc");
}
DI void sig_wait2(u32 a0, u32 v0, u32 a1, u32 v1) {
  u32 t0, t1, s0, s1, cnt;
  asm volatile(
      "s_mov_b32 %4, 0x400000\n"
      "LW%=:\n\t"
      "ds_read_b32 %0, %5\n\t"
      "ds_read_b32 %1, %7\n\t"
      "s_waitcnt lgkmcnt(0)\n\t"
      "v_readfirstlane_b32 %2, %0\n\t"
      "v_readfirstlane_b32 %3, %1\n\t"
      "s_nop 1\n\t"
      "s_cmp_ge_u32 %2, %6\n\t"
      "s_cbranch_scc0 LS%=\n\t"
      "s_cmp_ge_u32 %3, %8\n\t"
      "s_cbranch_scc1 LD%=\n"
      "LS%=:\n\t"
      "s_sleep 1\n\t"
      "s_sub_u32 %4, %4, 1\n\t"
      "s_cmp_lg_u32 %4, 0\n\t"
      "s_cbranch_scc1 LW%=\n"
      "LD%=:"
      : "=&v"(t0), "=&v"(t1), "=&s"(s0), "=&s"(s1), "=&s"(cnt) : "v"(a0), "s"(v0), "v"(a1), "s"(v1) : "memory", "scc");
}
#define SIG_WAIT2(f0, v0, f1, v1) sig_wait2(ldsb + L_SYN + (f0) * 4, (v0), ldsb + L_SYN + (f1) * 4, (v1))
#define SIG_WAIT(f, v) sig_wait(ldsb + L_SYN + (f) * 4, (v))
#define WPRIO 0
#define CLR_AT 4
#define PF_AT 0
#define KVD_AT 2
#define WO_FIRST 0
#define GW_AT 4
#define GTHR "40"
#define REC_AT 3
#define ST_AT 2
#define GW_SIG F_W0
#define H1D 8
#define H1S 4
#define PFLATE 0
#define WTHR "40"
#define WORD 1
#define M3_SIG F_P2
#define M3_AT 10
#define H2S 0
#define OGX 1
#define MPRIO 3
#define M2_SIG F_KR
#define M2_AT 8
#define QW4 5
#define RAWBAR() asm volatile("s_barrier" ::: "memory")
__global__ __launch_bounds__(768, 3) void fused_kernel(
    const u16* __restrict__ x, const u16* __restrict__ wqkv, const u16* __restrict__ wphi, const u16* __restrict__ wo,
    const u16* __restrict__ wr, const u16* __restrict__ wg, const u16* __restrict__ wu, const u16* __restrict__ wd,
    u16* __restrict__ out, char* __restrict__ ws) {
  __shared__ __attribute__((aligned(1024))) char smem_[L_TOTAL];
  LAS char* sm = (LAS char*)smem_;
  const u32 ldsb = (u32)(size_t)smem_;
  STAMP(0);
  const int tid = threadIdx.x, l = tid & 63;
  const int w = __builtin_amdgcn_readfirstlane(tid >> 6);
  const int g = l >> 4, i16 = l & 15, q4 = i16 >> 2, p4 = l & 3;
  const int b = blockIdx.x, xcd = b & 7, slot = b >> 3;
  const int rt = (xcd >> 1) * 4 + (slot >> 3), hg = (xcd & 1) * 8 + (slot & 7);
  const int wgi = rt * 16 + hg;
  const bool fast = (0xA5 >> (hg & 7)) & 1;
  const int og = hg ^ OGX;
  const int rot = (hg + 2 * rt) % 5;
  const auto wsr = __builtin_amdgcn_make_buffer_rsrc((void*)ws, 0, WS_END, 0x00020000);
  const u32 reg = WS_REG + wgi * RS;
#define CROT(c) ((c) + rot >= 5 ? (c) + rot - 5 : (c) + rot)
#define FR(F, t) (u32x4{F[2 * (t)][0], F[2 * (t)][1], F[2 * (t) + 1][0], F[2 * (t) + 1][1]})
#define LOADEP() u32x2 ep; { const u32 z = 0; asm volatile("global_load_dwordx2 %0, %1, %2 sc1" : "=v"(ep) : "v"(z), "s"(ws) : "memory"); }
#define GETTAG() u32 tag_lo, tag_hi; { u64 e = (((u64)ep[1] << 32) | ep[0]) + 1; tag_lo = __builtin_amdgcn_readfirstlane((u32)e); tag_hi = __builtin_amdgcn_readfirstlane((u32)(e >> 32)); }

  if (tid < 32) *(LAS u32*)(sm + L_SYN + tid * 4) = 0u;
  if (w >= 8) {
    const int h = w - 8;
    __builtin_amdgcn_s_setprio(3);
    int l_ = __builtin_amdgcn_mbcnt_hi(~0u, __builtin_amdgcn_mbcnt_lo(~0u, 0u));
    asm volatile("" : "+v"(l_));
    const int l = l_, tid = w * 64 + l, g = l >> 4, i16 = l & 15, q4 = i16 >> 2, p4 = l & 3;
    const u32x4 ONES = {0x3F803F80u, 0x3F803F80u, 0x3F803F80u, 0x3F803F80u};
    if (h < 2) {
      u32 woff[5];
#pragma unroll
      for (int k = 0; k < 5; k++) {
        int P = 64 * k + l, rho = P / 10, s = P - 10 * rho;
        woff[k] = (32 * ((rho >> 2) & 1) + 16 * ((rho >> 3) & 1) + 4 * ((rho >> 4) & 1) + (rho & 3)) * 3840 + 1280 + (s >= 5 ? 1200 : 0) + hg * 80 + s * 16;
      }
      const char* wbase = (const char*)wqkv + h * (320 * 3840);
#define WL(c) do { const char* b_ = wbase + CROT(c) * (64 * 3840); _Pragma("unroll") for (int k = 0; k < 10; k++) \
        glds16(b_ + (k / 5) * (8 * 3840), woff[k % 5], ldsb + L_W1 + (4 * (c) + 2 * (k / 5) + h) * 5120 + (k % 5) * 1024); } while (0)
#define WDONE2(N, c) do { asm volatile("s_waitcnt vmcnt(%0)" :: "i"(N) : "memory"); SIG_SET(F_W0 + h, (c) + 1); } while (0)
#define WDONE(N, c) do { asm volatile("s_waitcnt vmcnt(" #N ")" ::: "memory"); SIG_SET(F_W0 + h, (c) + 1); } while (0)
      WL(0);
      BARRIER();
      WDONE(0, 0);
      WL(1);
      STAMP(1);
      WDONE(0, 1);
      __builtin_amdgcn_s_setprio(0);
      WL(2);
      __builtin_amdgcn_s_setprio(3);
      STAMP(2);
      WL(3);
      STAMP(3);
      WDONE(10, 2); WL(4);
      STAMP(4);
      u32 qoff[3], ooff[3], po[7];
#pragma unroll
      for (int k = 0; k < 3; k++) {
        int P = 64 * k + l, rho = P / 6, s = P - 6 * rho; if (s > 4) s = 4;
        qoff[k] = (32 * ((rho >> 2) & 1) + 16 * ((rho >> 3) & 1) + 4 * ((rho >> 4) & 1) + (rho & 3)) * 3840 + hg * 80 + s * 16;
        ooff[k] = (8 * ((rho >> 2) & 3) + 4 * ((rho >> 4) & 1) + (rho & 3)) * 1280 + og * 80 + s * 16;
      }
#pragma unroll
      for (int k = 0; k < 7; k++) { const int qq = slot * 100 + h * 50 + (l >> 3) + (k < 6 ? 8 * k : 42), row = qq / 5, cc_ = qq - 5 * row; po[k] = row * 1280 + (xcd & 1) * 640 + cc_ * 128 + (l & 7) * 16; }
      WDONE(10, 3);
      u32x4 wr_[30];
#define OROT(k) (2 * CROT((k) / 6) + ((k) / 3 & 1))
#pragma unroll
      for (int k = 0; k < 30; k++) {
        asm volatile("global_load_dwordx4 %0, %1, %2" : "=v"(wr_[k]) : "v"(qoff[k % 3]), "s"(wbase + CROT(k / 6) * (64 * 3840) + ((k / 3) & 1) * (8 * 3840)));
        if (k == QW4) { WDONE2(QW4 + 1, 4); STAMP(5); }
      }
#pragma unroll
      for (int k = 0; k < 7; k++) glds16(wo, po[k], ldsb + L_PF);
      STAMP(6);
      SIG_WAIT(F_P1, 10);
      asm volatile("s_waitcnt vmcnt(7)" ::: "memory");
      STAMP(7);
#pragma unroll
      for (int k = 0; k < 30; k++) {
        asm volatile("" : "+v"(wr_[k]));
        *(LAS u32x4*)(sm + L_WQ + (2 * (k / 3) + h) * 3072 + (k % 3) * 1024 + l * 16) = wr_[k];
      }
      SIG_SET(F_Q0 + h, 1);
      STAMP(8);
      __builtin_amdgcn_s_setprio(0);
#pragma unroll
      for (int k = 0; k < WO_FIRST; k++)
        asm volatile("global_load_dwordx4 %0, %1, %2" : "=v"(wr_[k]) : "v"(ooff[k % 3]), "s"((const char*)wo + (10 * h + OROT(k)) * 40960));
      {
        f32x4 cq[3];
#pragma unroll
        for (int t = 0; t < 3; t++) cq[t] = f32x4{0, 0, 0, 0};
        LAS char* wqp = sm + L_WQ + h * 3072 + (4 * g + q4) * 96 + 8 * p4;
#pragma unroll 1
        for (int k = 0; k < 10; k++)
#pragma unroll
          for (int t = 0; t < 3; t++) cq[t] = mfma32(ONES, trd2(wqp + k * 6144 + 32 * t, 1536), cq[t]);
#pragma unroll
        for (int t = 0; t < 3; t++) *(LAS float*)(sm + L_CSQ + (48 * h + 16 * t + i16) * 4) = cq[t][0];
        SIG_ADD(F_CSQ);
        SIG_ADD(F_P2);
      }
#pragma unroll
      for (int k = WO_FIRST; k < 30; k++)
        asm volatile("global_load_dwordx4 %0, %1, %2\n\ts_waitcnt vmcnt(" WTHR ")" : "=v"(wr_[k]) : "v"(ooff[k % 3]), "s"((const char*)wo + (10 * h + OROT(k)) * 40960));
      STAMP(9);
      SIG_WAIT(h ? F_KR : F_P2, h ? 8 : 10);
      asm volatile("s_waitcnt vmcnt(0)" ::: "memory");
      STAMP(10);
      __builtin_amdgcn_s_setprio(0);
#pragma unroll
      for (int k = 0; k < 30; k++) {
        asm volatile("" : "+v"(wr_[k]));
        *(LAS u32x4*)(sm + (h ? L_WO1 : L_WO0) + OROT(k) * 3072 + (k % 3) * 1024 + l * 16) = wr_[k];
      }
      SIG_ADD(F_WO);
      STAMP(12);
    } else {
      const int e = h - 2;
#define STATS_LD() u32x4 sx[5], sw[5]; { const u32 o = (4 * e + g) * 1280 + i16 * 16, o2 = i16 * 16; \
          _Pragma("unroll") for (int k = 0; k < 5; k++) { asm volatile("global_load_dwordx4 %0, %1, %2 offset:%3" : "=v"(sx[k]) : "v"(o), "s"(x + wgi * 8 * 640), "i"(k * 256)); \
            asm volatile("global_load_dwordx4 %0, %1, %2 offset:%3" : "=v"(sw[k]) : "v"(o2), "s"(wr), "i"(k * 256)); } }
#define STATS() do { _Pragma("unroll") for (int k = 0; k < 5; k++) asm volatile("" : "+v"(sx[k]), "+v"(sw[k])); \
          float s = 0.f, q = 0.f; \
          _Pragma("unroll") for (int k = 0; k < 20; k++) { s = dot2(sx[k >> 2][k & 3], 0x3F803F80u, s); q = dot2(sx[k >> 2][k & 3], sx[k >> 2][k & 3], q); } \
          const float mean = row_sum(s) * (1.f / 640.f); \
          const float var = fmaxf(row_sum(q) * (1.f / 640.f) - mean * mean, 0.f); \
          const float mb = rbf(mean), sb = rbf(__builtin_amdgcn_sqrtf(rbf(rbf(var) + 1e-5f))), rsb = rcp(sb); \
          const u32 ms = pk2(mb, sb); const int srow = wgi * 8 + 4 * e + g; \
          if (i16 == 0) ST4((u32x4{ms, tag_lo, ms, tag_hi}), WS_ST + srow * 16); \
          float r = 0.f; \
          _Pragma("unroll") for (int k = 0; k < 20; k++) { const u32 xd = sx[k >> 2][k & 3]; \
            u32 d = pk2(bflo(xd) - mb, bfhi(xd) - mb), n = pk2(bflo(d) * rsb, bfhi(d) * rsb); r = dot2(n, sw[k >> 2][k & 3], r); } \
          r = row_sum(r); \
          const u32 rb = pk2(r, 0.f) & 0xFFFFu; \
          const u32 ord = (rb & 0x8000u) ? (~rb & 0xFFFFu) : (rb | 0x8000u); \
          if (i16 == 0) *(LAS u32x2*)(sm + L_KEY + (4 * e + g) * 8) = u32x2{(ord << 16) | (u32)(0x7FFF - srow), ms}; \
          SIG_ADD(F_ST); } while (0)
      if (e == 1) {
        LOADEP();
        STATS_LD();
        BARRIER();
        if (l < 14) ST4((u32x4{SENT, SENT, SENT, SENT}), reg + R_KVR + l * 16);
        asm volatile("s_waitcnt vmcnt(1)" : "+v"(ep) :: "memory");
        GETTAG();
        STAMP(1);
        STATS();
        {
          SIG_WAIT(F_ST, 2);
          u32x2 kk = *(LAS u32x2*)(sm + L_KEY + (l & 7) * 8);
          u32 km = row_max(kk[0]);
          if (l < 8 && kk[0] == km) ST4((u32x4{kk[0], tag_lo, kk[1], tag_hi}), WS_REC + b * 16);
        }
        STAMP(2);
        u32x4 s0 = {0, 0, 0, 0}, s1 = {0, 0, 0, 0};
        const u32 sta = WS_ST + (rt * 128 + l) * 16;
#define LDST() asm volatile("global_load_dwordx4 %0, %2, %3 sc1\n\tglobal_load_dwordx4 %1, %2, %3 offset:1024 sc1\n\ts_waitcnt vmcnt(0)" : "=&v"(s0), "=&v"(s1) : "v"(sta), "s"(ws) : "memory")
        f32x4 cs[5];
#pragma unroll
        for (int t = 0; t < 5; t++) cs[t] = f32x4{0, 0, 0, 0};
        LAS char* w1p = sm + L_W1 + (4 * g + q4) * 160 + 8 * p4;
#pragma unroll 1
        for (int c = 0; c < 5; c++) {
          if (c == ST_AT) LDST();
          SIG_WAIT2(F_W0, c + 1, F_W1, c + 1);
          if (c == CLR_AT) {
#pragma unroll
            for (int k = 0; k < 7; k++) ST4((u32x4{SENT, SENT, SENT, SENT}), (k * 64 + l < 420) ? reg + R_KVP + (k * 64 + l) * 16 : WS_END);
          }
#pragma unroll
          for (int k = 0; k < 4; k++)
#pragma unroll
            for (int t = 3; t < 5; t++) cs[t] = mfma32(ONES, trd2(w1p + (4 * c + k) * 5120 + 32 * t, 2560), cs[t]);
          if (fast) {
#pragma unroll
            for (int k = 0; k < 4; k++)
#pragma unroll
              for (int t = 0; t < 3; t++) cs[t] = mfma32(ONES, trd2(w1p + (4 * c + k) * 5120 + 32 * t, 2560), cs[t]);
          }
          if (c == 2) { SIG_ADD(F_P1); if (fast) SIG_ADD(F_P1); }
        }
        STAMP(3);
#pragma unroll 1
        for (u32 spin = 0; spin < SPIN_CAP; spin++) {
          if (__all(s0[1] == tag_lo && s0[3] == tag_hi && s0[0] == s0[2] && s1[1] == tag_lo && s1[3] == tag_hi && s1[0] == s1[2])) break;
          __builtin_amdgcn_s_sleep(4);
          LDST();
        }
        *(LAS u32*)(sm + L_MS + l * 4) = s0[0];
        *(LAS u32*)(sm + L_MS + 256 + l * 4) = s1[0];
        *(LAS float*)(sm + L_CSP + (48 + i16) * 4) = cs[3][0]; *(LAS float*)(sm + L_CSP + (64 + i16) * 4) = cs[4][0];
        if (fast) {
#pragma unroll
          for (int t = 0; t < 3; t++) *(LAS float*)(sm + L_CSP + (16 * t + i16) * 4) = cs[t][0];
          SIG_ADD(F_CS);
        }
        SIG_ADD(F_CS);
        STAMP(4);
        asm volatile("s_waitcnt vmcnt(0)" ::: "memory");
        SIG_ADD(F_PHI);
        if (l < 16) ST2((u32x2{tag_lo, tag_hi}), WS_REG + (l * 16 + hg) * RS + R_I1 + rt * 8);
        STAMP(11);
        SIG_WAIT(F_PHI, 2);
        STAMP(5);
#define HINT(addr) do { _Pragma("unroll 1") for (u32 spin = 0; spin < SPIN_CAP; spin++) { u32x4 f_ = ld4w(ws, (addr) + (l & 7) * 16); \
          if (__all(f_[0] == tag_lo && f_[1] == tag_hi && f_[2] == tag_lo && f_[3] == tag_hi)) break; __builtin_amdgcn_s_sleep(4); } } while (0)
        HINT(reg + R_I1);
        SIG_WAIT(F_KR, 8);
        __builtin_amdgcn_s_sleep(H1D);
        STAMP(6);
        {
          const int he = l >= 28 ? l - 28 : l;
          const bool ok = (l < 56) && (rt < 15);
          const u32 h1a = ok ? WS_REG + ((l >= 28 ? 128 : 0) + hg) * RS + R_KVP + (28 * rt + he) * 16 : WS_END;
          u32x4 h1[8];
#pragma unroll 1
          for (u32 spin = 0; spin < SPIN_CAP; spin++) {
            MEMFENCE();
            u32 m = 0;
#pragma unroll
            for (int k = 0; k < 8; k++) h1[k] = __builtin_amdgcn_raw_buffer_load_b128(wsr, h1a, k * 16 * RS, 16);
#pragma unroll
            for (int k = 0; k < 8; k++) m = umax_(m, max4(h1[k]));
            if (__all(m != SENT)) break;
            __builtin_amdgcn_s_sleep(H1S);
          }
          f32x4 s = __builtin_bit_cast(f32x4, h1[0]);
#pragma unroll
          for (int k = 1; k < 8; k++) s += __builtin_bit_cast(f32x4, h1[k]);
#pragma unroll
          for (int r = 0; r < 4; r++) s[r] += __shfl(s[r], (l + 28) & 63);
          if (ok && l < 28) ST2((u32x2{san(pk2(s[0], s[1])), san(pk2(s[2], s[3]))}), reg + R_KVR + l * 8);
        }
        STAMP(7);
        {
          const int hpr = l / 14, e2 = l - 14 * hpr;
          const u32 h2a = WS_REG + (hpr * 16 + hg) * RS + R_KVR + e2 * 16;
          u32 ka[8];
#pragma unroll
          for (int k = 0; k < 8; k++) { int it = 112 * (k >> 1) + 2 * l + (k & 1), hf = it / 21, c = it - 21 * hf; ka[k] = L_KVT + c * 80 + hf * 8 + (hf >= 10 ? 1600 : 0); }
          u32x4 kv[4];
#pragma unroll 1
          for (u32 spin = 0; spin < SPIN_CAP; spin++) {
            MEMFENCE();
            u32 m = 0;
#pragma unroll
            for (int k = 0; k < 4; k++) kv[k] = LD4((l < 56 && 4 * k + hpr < 15) ? h2a + k * 64 * RS : WS_END);
#pragma unroll
            for (int k = 0; k < 4; k++) m = umax_(m, max4(kv[k]));
            if (__all(m != SENT)) break;
            __builtin_amdgcn_s_sleep(H2S);
          }
#pragma unroll
          for (int k = 0; k < 4; k++) if (l < 56 && 4 * k + hpr < 15) {
            *(LAS u32x2*)(sm + ka[2 * k]) = u32x2{kv[k][0], kv[k][1]};
            *(LAS u32x2*)(sm + ka[2 * k + 1]) = u32x2{kv[k][2], kv[k][3]};
          }
        }
        SIG_SET(F_KVT, 1);
        STAMP(8);
        SIG_WAIT(F_OS, 8);
        if (l < 16) ST2((u32x2{tag_lo, tag_hi}), WS_REG + (rt * 16 + l) * RS + R_I3 + hg * FS);
        STAMP(9);
#pragma unroll 1
        for (u32 spin = 0; spin < SPIN_CAP; spin++) {
          u32x2 f_ = ld2w(ws, reg + R_I3 + i16 * FS);
          const u64 okm = __ballot(f_[0] == tag_lo && f_[1] == tag_hi);
          if ((okm & 0xFF) == 0xFF) SIG_SET(F_O, 1);
          if ((okm & 0xFF00) == 0xFF00) SIG_SET(F_O1, 1);
          if ((okm & 0xFFFF) == 0xFFFF) break;
        }
        SIG_SET(F_O, 1); SIG_SET(F_O1, 1);
        STAMP(10);
      } else {
        LOADEP();
        STATS_LD();
        u32 wp[7];
#pragma unroll
        for (int k = 0; k < 7; k++) { const u32 o = hg * 1600 + (l + 64 * k < 400 ? l + 64 * k : 0) * 4; asm volatile("global_load_dword %0, %1, %2" : "=v"(wp[k]) : "v"(o), "s"(wphi)); }
        BARRIER();
        if (l < 4) *(LAS u32x4*)(sm + L_ZERO + l * 16) = u32x4{0, 0, 0, 0};
        if (l < 40) { *(LAS u32*)(sm + L_WPHI + l * 48 + 20) = 0u; *(LAS u32*)(sm + L_WPHI + l * 48 + 44) = 0u; }
        asm volatile("s_waitcnt vmcnt(0)" : "+v"(ep), "+v"(wp[0]), "+v"(wp[1]), "+v"(wp[2]), "+v"(wp[3]), "+v"(wp[4]), "+v"(wp[5]), "+v"(wp[6]) :: "memory");
        GETTAG();
        STATS();
#pragma unroll
        for (int k = 0; k < 7; k++) { const int u = l + 64 * k, row = u / 10, z = u - 10 * row; if (u < 400) *(LAS u32*)(sm + L_WPHI + row * 48 + z * 4 + (z >= 5 ? 4 : 0)) = wp[k]; }
        SIG_SET(F_WP, 1);
        STAMP(1);
        const int fi = wgi >> 1, cb = fi < 120 ? fi / 10 : 0, ksp = fi < 120 ? fi - 10 * cb : 0;
        if (fast) {
          SIG_WAIT(F_W0, REC_AT);
          const u32x4 S4 = {SENT, SENT, SENT, SENT};
          ST4(S4, WS_MG + fi * MGS + l * 16);
        }
        else {
          f32x4 cs[3];
#pragma unroll
          for (int t = 0; t < 3; t++) cs[t] = f32x4{0, 0, 0, 0};
          LAS char* w1p = sm + L_W1 + (4 * g + q4) * 160 + 8 * p4;
#pragma unroll 1
          for (int c = 0; c < 5; c++) {
            SIG_WAIT2(F_W0, c + 1, F_W1, c + 1);
#pragma unroll
            for (int k = 0; k < 4; k++)
#pragma unroll
              for (int t = 0; t < 3; t++) cs[t] = mfma32(ONES, trd2(w1p + (4 * c + k) * 5120 + 32 * t, 2560), cs[t]);
            if (c == 2) SIG_ADD(F_P1);
          }
#pragma unroll
          for (int t = 0; t < 3; t++) *(LAS float*)(sm + L_CSP + (16 * t + i16) * 4) = cs[t][0];
          SIG_ADD(F_CS);
#pragma unroll
          for (int zz = 0; zz < 3; zz++) { const int u = 64 * zz + l, sl = u / 10; ST4((u32x4{SENT, SENT, SENT, SENT}), u < 160 ? WS_YB + (sl * 128 + fi) * 160 + (u - 10 * sl) * 16 : WS_END); }
        }
        u32x2 rec;
        {
          u32 km = 0, ms = 0;
          u32x4 r_[4];
#pragma unroll 1
          for (u32 spin = 0; spin < SPIN_CAP; spin++) {
            const u32 ra = WS_REC + l * 16;
            asm volatile("global_load_dwordx4 %0, %4, %5 sc1\n\tglobal_load_dwordx4 %1, %4, %5 offset:1024 sc1\n\tglobal_load_dwordx4 %2, %4, %5 offset:2048 sc1\n\t"
                         "global_load_dwordx4 %3, %4, %5 offset:3072 sc1\n\ts_waitcnt vmcnt(0)"
                         : "=&v"(r_[0]), "=&v"(r_[1]), "=&v"(r_[2]), "=&v"(r_[3]) : "v"(ra), "s"(ws) : "memory");
            bool ok = true;
#pragma unroll
            for (int k = 0; k < 4; k++) ok = ok && r_[k][1] == tag_lo && r_[k][3] == tag_hi;
            if (__all(ok)) break;
            __builtin_amdgcn_s_sleep(16);
          }
#pragma unroll
          for (int k = 0; k < 4; k++) if (r_[k][0] > km) { km = r_[k][0]; ms = r_[k][2]; }
          u32 kx = row_max(km);
          { auto r = __builtin_amdgcn_permlane16_swap(kx, kx, false, false); kx = umax_(r[0], r[1]); }
          { auto r = __builtin_amdgcn_permlane32_swap(kx, kx, false, false); kx = umax_(r[0], r[1]); }
          const int src = __builtin_ctzll(__ballot(km == kx));
          rec = u32x2{(u32)__builtin_amdgcn_readfirstlane(kx), (u32)__builtin_amdgcn_readlane(ms, src)};
        }
        const int idx = (0x7FFF - (rec[0] & 0xFFFFu)) & 2047;
        if (b == 0 && l == 0) ST2((u32x2{tag_lo, tag_hi}), WS_EPOCH);
        SIG_SET(F_IDX, idx + 1);
        STAMP(4);
        const u32 mreg = WS_MG + fi * MGS;
        if (fast) {
          {
            const int c0 = (cb % 6) * 256, d = c0 / 12 + l;
            if (fi < 120 && 12 * d < c0 + 256) ST2((u32x2{tag_lo, tag_hi}), WS_MG + d * MGS + 1024 + ((12 * d < c0 ? 20 : 0) + (cb / 6) * 10 + ksp) * FS);
          }
          u32x2 xr;
          { const u32 xo = (64 * ksp) * 2 + i16 * 8; asm volatile("global_load_dwordx2 %0, %1, %2" : "=v"(xr) : "v"(xo), "s"(x + idx * 640)); }
          u32x4 gw[32];
          __builtin_amdgcn_s_setprio(0);
          SIG_WAIT(GW_SIG, GW_AT);
          STAMP(2);
          {
            const u16* gm = cb < 6 ? wg + cb * 256 : wu + (cb - 6) * 256;
            const u32 go = (64 * ksp + 2 * (l >> 5)) * 3072 + (l & 31) * 16;
#pragma unroll
            for (int k = 0; k < 32; k++) asm volatile("global_load_dwordx4 %0, %1, %2 offset:%3" : "=v"(gw[k]) : "v"(go), "s"(gm + (k >> 1) * 4 * 1536), "i"((k & 1) * 3072));
          }
          __builtin_amdgcn_s_setprio(MPRIO);
          STAMP(3);
          {
            asm volatile("s_waitcnt vmcnt(16)" : "+v"(xr) :: "memory");
            const float sel_m = bflo(rec[1]), sel_r = rcp(bfhi(rec[1]));
            u32 xp = (l >> 5) ? xr[1] : xr[0];
            { u32 d = pk2(bflo(xp) - sel_m, bfhi(xp) - sel_m); xp = pk2(bflo(d) * sel_r, bfhi(d) * sel_r); }
            float v[8];
#pragma unroll
            for (int k = 0; k < 8; k++) v[k] = 0.f;
#define M1ROW(r) do { asm volatile("" : "+v"(gw[2 * r]), "+v"(gw[2 * r + 1])); const u32 a = dppu<0x150 + r>(xp); \
              _Pragma("unroll") for (int k = 0; k < 4; k++) { v[2 * k] = dot2(__builtin_amdgcn_perm(gw[2 * r + 1][k], gw[2 * r][k], 0x05040100u), a, v[2 * k]); \
                v[2 * k + 1] = dot2(__builtin_amdgcn_perm(gw[2 * r + 1][k], gw[2 * r][k], 0x07060302u), a, v[2 * k + 1]); } } while (0)
            M1ROW(0); M1ROW(1); M1ROW(2); M1ROW(3); M1ROW(4); M1ROW(5); M1ROW(6); M1ROW(7);
            asm volatile("s_waitcnt vmcnt(0)" ::: "memory");
            STAMP(5);
            M1ROW(8); M1ROW(9); M1ROW(10); M1ROW(11); M1ROW(12); M1ROW(13); M1ROW(14); M1ROW(15);
            u32x4 u4;
#pragma unroll
            for (int k = 0; k < 4; k++) {
              auto r = __builtin_amdgcn_permlane32_swap(__float_as_uint(v[k]), __float_as_uint(v[k + 4]), false, false);
              u4[k] = __float_as_uint(__uint_as_float(r[0]) + __uint_as_float(r[1]));
            }
            ST4(san4(u4), fi < 120 ? mreg + (l & 31) * 32 + (l >> 5) * 16 : WS_END);
          }
          __builtin_amdgcn_s_setprio(3);
          STAMP(6);
        } else {
          if (l < 16) ST2((u32x2{tag_lo, tag_hi}), WS_IY + (l * 128 + fi) * FS);
          u32x2 dw[36];
          {
            const u16* dp = wd + (12 * fi) * 640;
            const u32 dof = l * 8, dof2 = (l < 32 ? l : 0) * 8;
#pragma unroll
            for (int k = 0; k < 12; k++) {
              asm volatile("global_load_dwordx2 %0, %1, %2" : "=v"(dw[3 * k]) : "v"(dof), "s"(dp + k * 640));
              asm volatile("global_load_dwordx2 %0, %1, %2 offset:512" : "=v"(dw[3 * k + 1]) : "v"(dof), "s"(dp + k * 640));
              asm volatile("global_load_dwordx2 %0, %1, %2 offset:1024" : "=v"(dw[3 * k + 2]) : "v"(dof2), "s"(dp + k * 640));
            }
          }
          STAMP(7);
          {
            const int nsl = ((12 * fi) >> 8) == ((12 * fi + 11) >> 8) ? 20 : 40;
#pragma unroll 1
            for (u32 spin = 0; spin < SPIN_CAP; spin++) {
              u32x2 f0 = ld2w(ws, mreg + 1024 + (l < nsl ? l : 0) * FS);
              if (__all(f0[0] == tag_lo && f0[1] == tag_hi)) break;
              __builtin_amdgcn_s_sleep(6);
            }
          }
          SIG_WAIT(M2_SIG, M2_AT);
          STAMP(8);
          {
            const int kw = l / 6, pr = l - 6 * kw, col = 12 * fi + 2 * pr;
            const u32 ga = l < 60 ? WS_MG + ((col >> 8) * 10 + kw) * MGS + (col & 255) * 4 : WS_END;
            u32x2 g2, u2;
#pragma unroll 1
            for (u32 spin = 0; spin < SPIN_CAP; spin++) {
              MEMFENCE();
              g2 = LD2(ga); u2 = __builtin_amdgcn_raw_buffer_load_b64(wsr, ga, 60 * MGS, 16);
              if (__all(umax_(umax_(g2[0], g2[1]), umax_(u2[0], u2[1])) != SENT)) break;
              __builtin_amdgcn_s_sleep(8);
            }
            STAMP(15);
            f32x2 gg2 = __builtin_bit_cast(f32x2, g2), uu2 = __builtin_bit_cast(f32x2, u2);
            float z[4] = {gg2[0], gg2[1], uu2[0], uu2[1]};
#pragma unroll
            for (int k = 0; k < 4; k++) {
              const float z0 = z[k] + __shfl(z[k], (l + 30) & 63);
              z[k] = rbf(z0 + __shfl(z0, (l + 6) & 63) + __shfl(z0, (l + 12) & 63) + __shfl(z0, (l + 18) & 63) + __shfl(z0, (l + 24) & 63));
            }
            const u32 hp2 = pk2(rbf(z[0] * rcp(1.f + __expf(-z[0]))) * z[2], rbf(z[1] * rcp(1.f + __expf(-z[1]))) * z[3]);
#pragma unroll
            for (int k = 0; k < 36; k++) asm volatile("" : "+v"(dw[k]));
#pragma unroll
            for (int zz = 0; zz < 3; zz++) {
              f32x4 y = {0, 0, 0, 0};
#pragma unroll
              for (int i = 0; i < 6; i++) {
                const u32 hh = __builtin_amdgcn_readlane(hp2, i);
                const u32x2 d0 = dw[6 * i + zz], d1 = dw[6 * i + 3 + zz];
                y[0] = dot2(__builtin_amdgcn_perm(d1[0], d0[0], 0x05040100u), hh, y[0]); y[1] = dot2(__builtin_amdgcn_perm(d1[0], d0[0], 0x07060302u), hh, y[1]);
                y[2] = dot2(__builtin_amdgcn_perm(d1[1], d0[1], 0x05040100u), hh, y[2]); y[3] = dot2(__builtin_amdgcn_perm(d1[1], d0[1], 0x07060302u), hh, y[3]);
              }
              { const int u = 64 * zz + l, sl = u / 10; ST4(san4(__builtin_bit_cast(u32x4, y)), u < 160 ? WS_YB + (sl * 128 + fi) * 160 + (u - 10 * sl) * 16 : WS_END); }
            }
          }
        }
        STAMP(9);
        if ((idx >> 7) == rt) {
          SIG_WAIT(M3_SIG, M3_AT);
          STAMP(11);
#pragma unroll 1
          for (u32 spin = 0; spin < SPIN_CAP; spin++) {
            __builtin_amdgcn_s_sleep(6);
            u32x2 f0, f1; const u32 fa_ = WS_IY + (hg * 128 + l) * FS, fb_ = fa_ + 64 * FS;
            asm volatile("global_load_dwordx2 %0, %2, %4 sc1\n\tglobal_load_dwordx2 %1, %3, %4 sc1\n\ts_waitcnt vmcnt(0)" : "=&v"(f0), "=&v"(f1) : "v"(fa_), "v"(fb_), "s"(ws) : "memory");
            if (__all(f0[0] == tag_lo && f0[1] == tag_hi && f1[0] == tag_lo && f1[1] == tag_hi)) break;
          }
          STAMP(12);
          f32x4 t;
          {
            const u32 ya = l < 60 ? WS_YB + og * 20480 + l * 16 : WS_END, yl = l < 20 ? ya : WS_END;
            f32x4 s_;
#pragma unroll 1
            for (u32 spin = 0; spin < SPIN_CAP; spin++) {
              MEMFENCE();
              u32x4 yv[22];
#pragma unroll
              for (int k = 0; k < 22; k++) yv[k] = __builtin_amdgcn_raw_buffer_load_b128(wsr, k < 21 ? ya : yl, k * 960, 16);
              u32 m = 0; s_ = f32x4{0, 0, 0, 0};
#pragma unroll
              for (int k = 0; k < 22; k++) { m = umax_(m, max4(yv[k])); s_ += __builtin_bit_cast(f32x4, yv[k]); }
              if (__all(m != SENT)) break;
              __builtin_amdgcn_s_sleep(6);
            }
            STAMP(13);
#pragma unroll
            for (int r = 0; r < 4; r++) { const float a_ = s_[r] + __shfl(s_[r], (l + 30) & 63); t[r] = a_ + __shfl(a_, (l + 10) & 63) + __shfl(a_, (l + 20) & 63); }
          }
          u32 rb = rec[0] >> 16;
          rb = (rb & 0x8000u) ? (rb & 0x7FFFu) : (~rb & 0xFFFFu);
          const float sig = rbf(rcp(1.f + __expf(-bflo(rb))));
          if (l < 10) *(LAS f32x4*)(sm + L_Y + l * 16) = f32x4{rbf(rbf(t[0]) * sig), rbf(rbf(t[1]) * sig), rbf(rbf(t[2]) * sig), rbf(rbf(t[3]) * sig)};
        }
        SIG_SET(F_Y, idx + 1);
        STAMP(10);
      }
    }
  } else {
    const int tg = w & 3, kh = w >> 2, tw = 2 * tg + kh;
    u32x4 xf[20];
    const char* xb = (const char*)x + rt * (128 * 1280) + kh * 640;
    const u32 xoff = (u32)((((g >> 1) ? (tw ^ 1) : tw) * 16 + 4 * q4) * 1280 + (g & 1) * 64 + p4 * 16);
#define XI(c, r) ((r) < 2 ? 2 * (c) + (r) : 8 + 2 * (c) + (r))
#define XL1(c, r) asm volatile("global_load_dwordx4 %0, %1, %2 offset:%3" : "=v"(xf[XI(c, r)]) : "v"(xoff), "s"(xb + CROT(c) * 128), "i"((r) * 1280))
#define XG(c) do { XL1(c, 0); XL1(c, 1); XL1(c, 2); XL1(c, 3); } while (0)
    __builtin_amdgcn_s_setprio(2);
    XG(0);
    BARRIER();
    XG(1); XG(2); XG(3); XG(4);
    __builtin_amdgcn_s_setprio(0);
    STAMP(1);
    f32x4 acc[2][5];
#pragma unroll
    for (int t = 0; t < 5; t++) { acc[0][t] = f32x4{0, 0, 0, 0}; acc[1][t] = f32x4{0, 0, 0, 0}; }
    LAS char* w1p = sm + L_W1 + kh * 5120 + (4 * g + q4) * 160 + 8 * p4;
    const u64 mE = 0x5555555555555555ull, mO = ~mE, mL = 0x3333333333333333ull, mH = ~mL;
    u32 ta[4], ua[4];
#define DPX1 " quad_perm:[1,0,3,2] row_mask:0xf bank_mask:0xf\n\t"
#define DPX2 " quad_perm:[2,3,0,1] row_mask:0xf bank_mask:0xf\n\t"
#define CD2(M, X, d0, s0, k0, d1, s1, k1) asm volatile("s_mov_b64 vcc, %6\n\tv_cndmask_b32_dpp %0, %2, %3, vcc" X "v_cndmask_b32_dpp %1, %4, %5, vcc" X \
      : "=&v"(d0), "=&v"(d1) : "v"(s0), "v"(k0), "v"(s1), "v"(k1), "s"(M) : "vcc")
#define CD2I(M, X, d0, s0, d1, s1) asm volatile("s_mov_b64 vcc, %4\n\tv_cndmask_b32_dpp %0, %2, %0, vcc" X "v_cndmask_b32_dpp %1, %3, %1, vcc" X \
      : "+v"(d0), "+v"(d1) : "v"(s0), "v"(s1), "s"(M) : "vcc")
#define A_(c, r) xf[XI(c, r)]
#define TP(c, k) do { const int d_ = (k) & 3; u32 n0, n1; \
      if ((k) < 4) { CD2(mE, DPX1, n0, A_(c, 1)[d_], A_(c, 0)[d_], n1, A_(c, 3)[d_], A_(c, 2)[d_]); ua[d_] = n0; ta[d_] = n1; } \
      else if ((k) < 8) { n0 = A_(c, 1)[d_]; n1 = A_(c, 3)[d_]; CD2I(mO, DPX1, n0, A_(c, 0)[d_], n1, A_(c, 2)[d_]); A_(c, 1)[d_] = n0; A_(c, 3)[d_] = n1; } \
      else if ((k) < 12) { CD2(mL, DPX2, n0, ta[d_], ua[d_], n1, A_(c, 3)[d_], A_(c, 1)[d_]); A_(c, 0)[d_] = n0; A_(c, 2)[d_] = n1; } \
      else if ((k) < 16) { n1 = A_(c, 3)[d_]; CD2I(mH, DPX2, ta[d_], ua[d_], n1, A_(c, 1)[d_]); A_(c, 3)[d_] = n1; } \
      else { auto r0 = __builtin_amdgcn_permlane32_swap(A_(c, 0)[d_], ta[d_], false, false); auto r1 = __builtin_amdgcn_permlane32_swap(A_(c, 2)[d_], A_(c, 3)[d_], false, false); \
        A_(c, 0)[d_] = r0[0]; A_(c, 2)[d_] = r0[1]; A_(c, 1)[d_] = r1[0]; A_(c, 3)[d_] = r1[1]; } } while (0)
#define XW(N, c) asm volatile("s_waitcnt vmcnt(" #N ")" : "+v"(A_(c, 0)), "+v"(A_(c, 1)), "+v"(A_(c, 2)), "+v"(A_(c, 3)) :: "memory")
#define RD(F, j, r) F[r] = trd(w1p + (j) * 10240 + 32 * ((r) >> 1) + 2560 * ((r) & 1))
#define MF(F, j, k) acc[(k) & 1][(k) >> 1] = mfma32(FR(F, (k) >> 1), xf[10 * ((k) & 1) + (j)], acc[(k) & 1][(k) >> 1])
#define STEPA(FA, FB, c, V) do { _Pragma("unroll") for (int k = 0; k < 10; k++) { MF(FA, 2 * (c), k); RD(FB, 2 * (c) + 1, k); if ((c) < 4) TP((c) + 1, k); V(k); SB(); } } while (0)
#define STEPB(FA, FB, c) do { _Pragma("unroll") for (int k = 0; k < 4; k++) { MF(FB, 2 * (c) + 1, k); TP((c) + 1, 10 + k); SB(); } \
      if ((c) == 2) SIG_ADD(F_P1); \
      if ((c) == 3) SIG_WAIT2(F_W0 + kh, (c) + 2, F_WP, 1); else SIG_WAIT(F_W0 + kh, (c) + 2); \
      _Pragma("unroll") for (int k = 4; k < 10; k++) { if (k < 9) { RD(FA, 2 * (c) + 2, 2 * (k - 4)); RD(FA, 2 * (c) + 2, 2 * (k - 4) + 1); } MF(FB, 2 * (c) + 1, k); TP((c) + 1, 10 + k); SB(); } } while (0)
    {
      u32x2 fa[10], fb[10];
      XW(16, 0);
      u32 fil = l;
#pragma unroll
      for (int k = 0; k < 20; k++) { TP(0, k); asm volatile("v_add_u32 %0, 1, %0" : "+v"(fil)); SB(); }
      SIG_WAIT(F_W0 + kh, 1);
      STAMP(2);
#pragma unroll
      for (int r = 0; r < 10; r++) RD(fa, 0, r);
      SB();
#define V3(k) do { if ((k) == 1) XL1(3, 0); if ((k) == 3) XL1(3, 1); if ((k) == 5) XL1(3, 2); if ((k) == 7) XL1(3, 3); } while (0)
#define V4(k) do { if ((k) == 1) XL1(4, 0); if ((k) == 3) XL1(4, 1); if ((k) == 5) XL1(4, 2); if ((k) == 7) XL1(4, 3); } while (0)
#define VN(k) do { } while (0)
      XW(12, 1);
      STEPA(fa, fb, 0, VN); STEPB(fa, fb, 0);
      STAMP(3);
      XW(8, 2);
      STEPA(fa, fb, 1, VN); STEPB(fa, fb, 1);
      STAMP(4);
      XW(4, 3);
      STEPA(fa, fb, 2, VN); STEPB(fa, fb, 2);
      STAMP(5);
      XW(0, 4);
      STEPA(fa, fb, 3, VN); STEPB(fa, fb, 3);
      STAMP(6);
      STEPA(fa, fb, 4, VN);
#pragma unroll
      for (int k = 0; k < 10; k++) MF(fb, 9, (k + 1 - 2 * (k & 1)));
    }
    STAMP(7);
    {
    int l_ = __builtin_amdgcn_mbcnt_hi(~0u, __builtin_amdgcn_mbcnt_lo(~0u, 0u));
    asm volatile("" : "+v"(l_));
    const int l = l_, tid = w * 64 + l, g = l >> 4, i16 = l & 15, q4 = i16 >> 2, p4 = l & 3;
    {
      const int tp = tw ^ 1;
      *(LAS f32x4*)(sm + L_PHI + tp * 2560 + l * 16) = acc[1][0];
      *(LAS f32x4*)(sm + L_PHI + tp * 2560 + 1024 + l * 16) = acc[1][1];
      *(LAS f32x4*)(sm + L_V + tp * 1536 + l * 16) = acc[1][2];
      *(LAS f32x4*)(sm + L_EXA + tp * 2048 + l * 16) = acc[1][3];
      *(LAS f32x4*)(sm + L_EXA + tp * 2048 + 1024 + l * 16) = acc[1][4];
      SIG_SET(F_EX + tp, 1);
    }
    u32x4 wa32[3]; u32x2 wa16[3];
    {
      const int hp = p4 >> 1;
      const int d0 = 4 * g + q4, d1 = d0 + 16, d2 = d0 + 32;
      LAS char* a0 = sm + (hp == 0 ? L_WPHI + d0 * 48 + 24 * (p4 & 1) : L_ZERO);
      LAS char* a1 = sm + ((d1 >= 20) == (hp == 1) ? L_WPHI + d1 * 48 + 24 * (p4 & 1) : L_ZERO);
      LAS char* a2 = sm + ((hp == 1 && d2 < 40) ? L_WPHI + d2 * 48 + 24 * (p4 & 1) : L_ZERO);
#pragma unroll
      for (int mt = 0; mt < 3; mt++) {
        u32x2 e0 = trd(a0 + 8 * mt), e1 = trd(a1 + 8 * mt);
        wa32[mt] = u32x4{e0[0], e0[1], e1[0], e1[1]};
        wa16[mt] = trd(a2 + 8 * mt);
      }
    }
    SIG_WAIT2(F_EX + tw, 1, F_CS, 2);
    STAMP(8);
    const u32 msv = *(LAS u32*)(sm + L_MS + (tw * 16 + i16) * 4);
    const float m_b = bflo(msv), rs = rcp(bfhi(msv));
    f32x4 cc[5], a[5];
#pragma unroll
    for (int t = 0; t < 3; t++) cc[t] = *(LAS f32x4*)(sm + L_CSP + (16 * t + 4 * g) * 4);
    a[0] = acc[0][0] + *(LAS f32x4*)(sm + L_PHI + tw * 2560 + l * 16);
    a[1] = acc[0][1] + *(LAS f32x4*)(sm + L_PHI + tw * 2560 + 1024 + l * 16);
    a[2] = acc[0][2] + *(LAS f32x4*)(sm + L_V + tw * 1536 + l * 16);
    {
      u32 kn[5][2];
#define NRM(A, t, C, O) do { f32x4 v_ = (A - m_b * C) * rs; O[t][0] = pk2(v_[0], v_[1]); O[t][1] = pk2(v_[2], v_[3]); } while (0)
      NRM(a[0], 0, cc[0], kn); NRM(a[1], 1, cc[1], kn); NRM(a[2], 2, cc[2], kn);
      SB();
      cc[3] = *(LAS f32x4*)(sm + L_CSP + (48 + 4 * g) * 4); cc[4] = *(LAS f32x4*)(sm + L_CSP + (64 + 4 * g) * 4);
      a[3] = acc[0][3] + *(LAS f32x4*)(sm + L_EXA + tw * 2048 + l * 16);
      a[4] = acc[0][4] + *(LAS f32x4*)(sm + L_EXA + tw * 2048 + 1024 + l * 16);
      u32x4 b32 = {kn[0][0], kn[0][1], kn[1][0], kn[1][1]};
      u32x2 b16 = {kn[2][0], kn[2][1]};
      f32x4 pp[3];
#pragma unroll
      for (int mt = 0; mt < 3; mt++) {
        pp[mt] = mfma32(wa32[mt], b32, f32x4{0, 0, 0, 0});
        pp[mt] = mfma16(wa16[mt], b16, pp[mt]);
      }
      NRM(a[3], 3, cc[3], kn); NRM(a[4], 4, cc[4], kn);
      __builtin_amdgcn_wave_barrier();
      LAS char* vr = sm + L_V + (16 * tw + i16) * 96;
      u32x2 v2 = (g >= 2) ? u32x2{kn[2][0], kn[2][1]} : u32x2{g == 0 ? 0x3F80u : 0u, 0u};
      *(LAS u32x2*)(vr + (g >= 2 ? 8 * (g - 2) : 80 + 8 * g)) = v2;
      *(LAS u32x2*)(vr + 16 + 8 * g) = u32x2{kn[3][0], kn[3][1]};
      *(LAS u32x2*)(vr + 48 + 8 * g) = u32x2{kn[4][0], kn[4][1]};
      u32 ph[10];
      softmax_pack(pp[0], pp[1], pp[2], ph);
      LAS char* pr = sm + L_PHI + (16 * tw + i16) * 160 + 40 * g;
#pragma unroll
      for (int k = 0; k < 5; k++) *(LAS u32x2*)(pr + 8 * k) = u32x2{ph[2 * k], ph[2 * k + 1]};
    }
    STAMP(9);
    SIG_ADD(F_PHI);
    __builtin_amdgcn_s_setprio(2);
    {
      const int mt0 = (0x22111000 >> (4 * w)) & 3, nt0 = (0x10210210 >> (4 * w)) & 3;
      const int mt1 = (0x22144332 >> (4 * w)) & 7, nt1 = (0x10221212 >> (4 * w)) & 3;
      u32 ap0 = L_PHI + (4 * g + q4) * 160 + 8 * p4 + 32 * mt0, ap1 = ap0 + 32 * (mt1 - mt0);
      u32 bp0 = L_V + (4 * g + q4) * 96 + 8 * p4 + 32 * nt0, bp1 = bp0 + 32 * (nt1 - nt0);
#define KVA(mt, nt, en) ({ const int fq = 4 * (mt) + g, col = 16 * (nt) + i16; int c = col - (fq >= 10 ? 20 : 0); bool ok = (c >= 0 && c < 20); \
        if (col == 40) { c = 20; ok = true; } (ok && (en)) ? reg + R_KVP + (fq * 21 + c) * 16 : WS_END; })
      u32 ks0 = KVA(mt0, nt0, true), ks1 = KVA(mt1, nt1, w < 5);
      asm volatile("" : "+v"(ap0), "+v"(ap1), "+v"(bp0), "+v"(bp1), "+v"(ks0), "+v"(ks1));
      SIG_WAIT(F_PHI, 9);
      STAMP(10);
      f32x4 d0 = {0, 0, 0, 0}, d1 = {0, 0, 0, 0};
#pragma unroll
      for (int u = 0; u < 4; u++) {
        d0 = mfma32(trd2(sm + ap0 + u * 5120, 2560), trd2(sm + bp0 + u * 3072, 1536), d0);
        d1 = mfma32(trd2(sm + ap1 + u * 5120, 2560), trd2(sm + bp1 + u * 3072, 1536), d1);
      }
      ST4(san4(__builtin_bit_cast(u32x4, d0)), ks0); ST4(san4(__builtin_bit_cast(u32x4, d1)), ks1);
    }
    SIG_ADD(F_KR);
    SIG_WAIT(F_Q0 + kh, 1);
    STAMP(11);
    f32x4 aq[2][3];
#pragma unroll
    for (int t = 0; t < 3; t++) { aq[0][t] = f32x4{0, 0, 0, 0}; aq[1][t] = f32x4{0, 0, 0, 0}; }
    {
      LAS char* wqp = sm + L_WQ + kh * 3072 + (4 * g + q4) * 96 + 8 * p4;
      u32x2 fa[6], fb[6];
#define RQ(F, P_, o, r) F[r] = trd(P_ + (o) + 32 * ((r) >> 1) + 1536 * ((r) & 1))
#define STEPQ(FA, FB, j) do { _Pragma("unroll") for (int k = 0; k < 6; k++) { aq[k & 1][k >> 1] = mfma32(FR(FA, k >> 1), xf[10 * (k & 1) + (j)], aq[k & 1][k >> 1]); \
          if ((j) < 9) RQ(FB, wqp, ((j) + 1) * 6144, k); SB(); } } while (0)
#pragma unroll
      for (int r = 0; r < 6; r++) RQ(fa, wqp, 0, r);
      SB();
#pragma unroll
      for (int j = 0; j < 10; j += 2) { STEPQ(fa, fb, j); STEPQ(fb, fa, j + 1);
      }
    }
#pragma unroll
    for (int t = 0; t < 3; t++) *(LAS f32x4*)(sm + L_EXQ + (tw ^ 1) * 3072 + t * 1024 + l * 16) = aq[1][t];
    SIG_SET(F_EX + (tw ^ 1), 2);
    SIG_ADD(F_P2);
    STAMP(12);
    SIG_WAIT2(F_EX + tw, 2, F_CSQ, 2);
    STAMP(13);
    const u32 slo0 = (rt * 128 + tw * 16 + l / 5) * 1280 + og * 80 + (l % 5) * 16;
    const u32 slo1 = (rt * 128 + tw * 16 + (64 + i16) / 5) * 1280 + og * 80 + ((64 + i16) % 5) * 16;
    SIG_WAIT(F_KR, 8);
    glds16(x, slo0, ldsb + L_V + tw * 1536);
    if (l < 16) glds16(x, slo1, ldsb + L_V + tw * 1536 + 1024);
    u32 pq[10];
    {
      f32x4 aqo[3], c2[3];
      u32 qn[3][2];
#pragma unroll
      for (int t = 0; t < 3; t++) {
        c2[t] = *(LAS f32x4*)(sm + L_CSQ + (16 * t + 4 * g) * 4) + *(LAS f32x4*)(sm + L_CSQ + (48 + 16 * t + 4 * g) * 4);
        aqo[t] = aq[0][t] + *(LAS f32x4*)(sm + L_EXQ + tw * 3072 + t * 1024 + l * 16);
        NRM(aqo[t], t, c2[t], qn);
      }
      u32x4 b32 = {qn[0][0], qn[0][1], qn[1][0], qn[1][1]};
      u32x2 b16 = {qn[2][0], qn[2][1]};
      f32x4 pp[3];
#pragma unroll
      for (int mt = 0; mt < 3; mt++) {
        pp[mt] = mfma32(wa32[mt], b32, f32x4{0, 0, 0, 0});
        pp[mt] = mfma16(wa16[mt], b16, pp[mt]);
      }
      softmax_pack(pp[0], pp[1], pp[2], pq);
#pragma unroll
      for (int k = 0; k < 10; k++) asm volatile("" : "+v"(pq[k]));
    }
    STAMP(14);
    u32 oad[3];
#pragma unroll
    for (int mt = 0; mt < 3; mt++) {
      const int col = mt < 2 ? 8 * q4 + 4 * mt + p4 : 32 + 2 * q4 + p4;
      const int hp = (mt == 2) ? (p4 == 2 ? 0 : 1) : (col >= 20 ? 1 : 0);
      const int row = (mt == 2 && p4 >= 2) ? hp * 21 + 20 : col + hp;
      oad[mt] = ((g >> 1) == hp) ? (u32)(L_KVT + row * 80 + 40 * (g & 1)) : L_ZERO;
      asm volatile("" : "+v"(oad[mt]));
    }
    STAMP(15);
    SIG_WAIT(F_KVT, 1);
    STAMP(16);
    {
      f32x4 oo[3];
#pragma unroll
      for (int mt = 0; mt < 3; mt++) {
        LAS char* ap = sm + oad[mt];
        u32x2 a0 = *(LAS u32x2*)(ap), a1 = *(LAS u32x2*)(ap + 8), a2 = *(LAS u32x2*)(ap + 16), a3 = *(LAS u32x2*)(ap + 24), a4 = *(LAS u32x2*)(ap + 32);
        oo[mt] = mfma32(u32x4{a0[0], a0[1], a1[0], a1[1]}, u32x4{pq[0], pq[1], pq[2], pq[3]}, f32x4{0, 0, 0, 0});
        oo[mt] = mfma32(u32x4{a2[0], a2[1], a3[0], a3[1]}, u32x4{pq[4], pq[5], pq[6], pq[7]}, oo[mt]);
        oo[mt] = mfma16(a4, u32x2{pq[8], pq[9]}, oo[mt]);
      }
      const float r0 = rcp(oo[2][2] + 1e-6f), r1 = rcp(oo[2][3] + 1e-6f);
      const float ra_ = g <= 2 ? r0 : r1, rb_ = g <= 1 ? r0 : r1;
      u32x4 o8 = {pk2(oo[0][0] * ra_, oo[0][1] * ra_), pk2(oo[0][2] * ra_, oo[0][3] * ra_), pk2(oo[1][0] * rb_, oo[1][1] * rb_), pk2(oo[1][2] * rb_, oo[1][3] * rb_)};
      u32 o2 = pk2(oo[2][0] * r1, oo[2][1] * r1);
      const u32 ob = reg + R_OB + tw * 1280 + l * 16;
      ST4(o8, ob);
      auto ra = __builtin_amdgcn_permlane32_swap(o2, o2, false, false);
      auto rb = __builtin_amdgcn_permlane16_swap(ra[0], ra[0], false, false);
      auto rc2 = __builtin_amdgcn_permlane16_swap(ra[1], ra[1], false, false);
      if (g == 0) ST4((u32x4{rb[0], rb[1], rc2[0], rc2[1]}), ob + 1024);
      asm volatile("s_waitcnt vmcnt(0)" ::: "memory");
      SIG_ADD(F_OS);
    }
    MEMFENCE();
    STAMP(17);
    u32x2 xv[3], xnp[3];
    {
      LAS char* xp = sm + L_V + tw * 1536 + i16 * 80 + 8 * g;
#pragma unroll
      for (int t = 0; t < 3; t++) {
        xv[t] = *(LAS u32x2*)(xp + 32 * t);
        u32 d0 = pk2(bflo(xv[t][0]) - m_b, bfhi(xv[t][0]) - m_b), d1 = pk2(bflo(xv[t][1]) - m_b, bfhi(xv[t][1]) - m_b);
        xnp[t] = u32x2{pk2(bflo(d0) * rs, bfhi(d0) * rs), pk2(bflo(d1) * rs, bfhi(d1) * rs)};
        asm volatile("" : "+v"(xnp[t]));
      }
    }
    const u32 obase = WS_REG + (rt * 16 + 8 * kh) * RS + R_OB + tg * 2560;
    u32 oa[5];
#pragma unroll
    for (int k = 0; k < 5; k++) { int C = 64 * k + l, pr = C / 80; oa[k] = obase + pr * (RS - 1280) + C * 16; asm volatile("" : "+v"(oa[k])); }
    f32x4 ao[2][3];
    int idx;
    {
      LAS char* wop = sm + (kh ? L_WO1 : L_WO0) + (4 * g + q4) * 96 + 8 * p4;
      SIG_WAIT2(F_WO, 2, F_IDX, 1);
      idx = __builtin_amdgcn_readfirstlane((int)*(volatile LAS u32*)(sm + L_SYN + F_IDX * 4) - 1);
      SIG_WAIT(kh ? F_O1 : F_O, 1);
      STAMP(19);
      {
        u32x4 of[2][10];
#pragma unroll
        for (int j = 0; j < 10; j++) {
          of[0][j] = __builtin_amdgcn_raw_buffer_load_b128(wsr, oa[j % 5], (j / 5) * 4 * RS + kh * 1280, 16);
          of[1][j] = __builtin_amdgcn_raw_buffer_load_b128(wsr, oa[j % 5], (j / 5) * 4 * RS + (1 - kh) * 1280, 16);
        }
#pragma unroll
        for (int t = 0; t < 3; t++) { ao[0][t] = f32x4{0, 0, 0, 0}; ao[1][t] = f32x4{0, 0, 0, 0}; }
        u32x2 fa[6], fb[6];
#define STEPO(FA, FB, j) do { _Pragma("unroll") for (int k = 0; k < 6; k++) { ao[k & 1][k >> 1] = mfma32(FR(FA, k >> 1), of[k & 1][j], ao[k & 1][k >> 1]); \
            if ((j) < 9) RQ(FB, wop, ((j) + 1) * 3072, k); SB(); } } while (0)
#pragma unroll
        for (int r = 0; r < 6; r++) RQ(fa, wop, 0, r);
        SB();
#pragma unroll
        for (int j = 0; j < 10; j += 2) { STEPO(fa, fb, j); STEPO(fb, fa, j + 1); }
      }
    }
#pragma unroll
    for (int t = 0; t < 3; t++) *(LAS f32x4*)(sm + L_EXQ + (tw ^ 1) * 3072 + t * 1024 + l * 16) = ao[1][t];
    SIG_SET(F_EX + (tw ^ 1), 3);
    STAMP(20);
    SIG_WAIT(F_EX + tw, 3);
    STAMP(21);
    {
      LAS char* xp = sm + L_V + tw * 1536 + i16 * 80 + 8 * g;
      if ((idx >> 4) == rt * 8 + tw) SIG_WAIT(F_Y, 1);
      if (idx == rt * 128 + tw * 16 + i16) {
#pragma unroll
        for (int t = 0; t < 3; t++) if (t < 2 || g < 2) {
          f32x4 yy = *(LAS f32x4*)(sm + L_Y + (16 * t + 4 * g) * 4);
          xnp[t] = u32x2{pk2(bflo(xnp[t][0]) + yy[0], bfhi(xnp[t][0]) + yy[1]), pk2(bflo(xnp[t][1]) + yy[2], bfhi(xnp[t][1]) + yy[3])};
        }
      }
#pragma unroll
      for (int t = 0; t < 3; t++) {
        f32x4 af = ao[0][t] + *(LAS f32x4*)(sm + L_EXQ + tw * 3072 + t * 1024 + l * 16);
        u32 p0 = pk2(af[0], af[1]), p1 = pk2(af[2], af[3]);
        u32 q0 = pk2(bflo(xv[t][0]) + bflo(p0), bfhi(xv[t][0]) + bfhi(p0)), q1 = pk2(bflo(xv[t][1]) + bflo(p1), bfhi(xv[t][1]) + bfhi(p1));
        u32x2 res = {pk2(bflo(q0) + bflo(xnp[t][0]), bfhi(q0) + bfhi(xnp[t][0])), pk2(bflo(q1) + bflo(xnp[t][1]), bfhi(q1) + bfhi(xnp[t][1]))};
        if (t < 2 || g < 2) *(LAS u32x2*)(xp + 32 * t) = res;
      }
      __builtin_amdgcn_wave_barrier();
      LAS char* op = sm + L_V + tw * 1536 + l * 16;
      *(u32x4*)((char*)out + slo0) = *(LAS u32x4*)(op);
      if (l < 16) *(u32x4*)((char*)out + slo1) = *(LAS u32x4*)(op + 1024);
    }
    STAMP(22);
    }
  }
}

extern "C" void kernel_launch(void* const* d_in, const int* in_sizes, int n_in, void* d_out, int out_size,
                              void* d_ws, size_t ws_size, hipStream_t stream) {
  fused_kernel<<<256, 768, 0, stream>>>((const u16*)d_in[0], (const u16*)d_in[1], (const u16*)d_in[2], (const u16*)d_in[3],
                                        (const u16*)d_in[5], (const u16*)d_in[6], (const u16*)d_in[7], (const u16*)d_in[8],
                                        (u16*)d_out, (char*)d_ws);
}
```

```cpp
#include <hip/hip_runtime.h>
#include <stdint.h>

typedef unsigned short u16;
typedef unsigned int u32;
typedef unsigned long long u64;
typedef __attribute__((ext_vector_type(8))) __bf16 bf16x8;
typedef __attribute__((ext_vector_type(4))) short s16x4;
typedef __attribute__((ext_vector_type(4))) float f32x4;
typedef __attribute__((ext_vector_type(2))) float f32x2;
typedef __attribute__((ext_vector_type(2))) __bf16 bf16x2;
typedef __attribute__((ext_vector_type(4))) u32 u32x4;
typedef __attribute__((ext_vector_type(2))) u32 u32x2;
#define LAS __attribute__((address_space(3)))
#define DI __device__ __forceinline__

#define FS 8
constexpr u32 WS_EPOCH = 0;
constexpr u32 WS_REC = 8192;
constexpr u32 WS_ST = 16384;
constexpr u32 WS_REG = 49152;
constexpr u32 RS = 23936;
constexpr u32 R_OB = 0;
constexpr u32 R_KVP = 10240;
constexpr u32 R_YP = 17024;
constexpr u32 R_GUP = 19584;
constexpr u32 R_KVR = 20608;
constexpr u32 R_I1 = 20864;
constexpr u32 R_I3 = 21760;
constexpr u32 R_IG = 21120;
constexpr u32 R_IY = 21760;
constexpr u32 WS_YB = WS_REG + 256 * RS;
constexpr u32 WS_MG = WS_YB + 327680;
constexpr u32 MGS = 1024 + 40 * 128;
constexpr u32 WS_IY = WS_MG + 128 * MGS;
constexpr u32 WS_END = WS_IY + 2048 * 128;
constexpr u32 WS_STAMP = 128u << 20;

constexpr u32 L_W1 = 0;
constexpr u32 L_WQ = 0;
constexpr u32 L_WO0 = 30720;
constexpr u32 L_WO1 = 92160;
constexpr u32 L_EXQ = 61440;
constexpr u32 L_CSP = 102400;
constexpr u32 L_EXA = 103424;
constexpr u32 L_PF = 162816;
constexpr u32 L_PHI = 122880;
constexpr u32 L_V = 143360;
constexpr u32 L_SM = 155648;
constexpr u32 L_WPHI = L_SM;
constexpr u32 L_ZERO = L_SM + 1920;
constexpr u32 L_KVT = L_SM + 1984;
constexpr u32 L_CSQ = L_KVT + 3360;
constexpr u32 L_KEY = L_CSQ + 768;
constexpr u32 L_IDX = L_KEY + 64;
constexpr u32 L_Y = L_IDX + 64;
constexpr u32 L_MS = L_Y + 160;
constexpr u32 L_SYN = L_MS + 512;
enum { F_W0 = 0, F_W1, F_ST, F_CS, F_P1, F_Q0, F_Q1, F_PHI, F_KV, F_CSQ, F_P2, F_KVT, F_WO, F_OS, F_O, F_Y, F_EX = 16, F_WP = 24, F_KR, F_IDX, F_YG, F_YP, F_O1 };
constexpr u32 L_YS = L_PHI;
constexpr u32 L_TOTAL = 163840;

constexpr u32 SENT = 0xFFFFFFFFu;
constexpr u32 SPIN_CAP = 1u << 15;
#define HOP1_AT 6

DI u32 pk2(float a, float b) { f32x2 v = {a, b}; return __builtin_bit_cast(u32, __builtin_convertvector(v, bf16x2)); }
DI float bflo(u32 u) { return __uint_as_float(u << 16); }
DI float bfhi(u32 u) { return __uint_as_float(u & 0xFFFF0000u); }
DI float rbf(float a) { return bflo(pk2(a, 0.f)); }
DI float rcp(float a) { return __builtin_amdgcn_rcpf(a); }
DI float dot2(u32 a, u32 b, float c) { return __builtin_amdgcn_fdot2_f32_bf16(__builtin_bit_cast(bf16x2, a), __builtin_bit_cast(bf16x2, b), c, false); }
DI f32x4 mfma32(u32x4 a, u32x4 b, f32x4 c) {
  return __builtin_amdgcn_mfma_f32_16x16x32_bf16(__builtin_bit_cast(bf16x8, a), __builtin_bit_cast(bf16x8, b), c, 0, 0, 0);
}
DI f32x4 mfma16(u32x2 a, u32x2 b, f32x4 c) {
  return mfma32(u32x4{a[0], a[1], 0u, 0u}, u32x4{b[0], b[1], 0u, 0u}, c);
}
DI u32x2 trd(LAS char* p) { return __builtin_bit_cast(u32x2, __builtin_amdgcn_ds_read_tr16_b64_v4i16((LAS s16x4*)p)); }
DI u32x4 trd2(LAS char* p, u32 d) { u32x2 a = trd(p), b = trd(p + d); return u32x4{a[0], a[1], b[0], b[1]}; }
DI void glds16(const void* sbase, u32 voff, u32 lds_dst) {
  asm volatile("s_mov_b32 m0, %2\n\ts_nop 0\n\tglobal_load_lds_dwordx4 %0, %1" :: "v"(voff), "s"(sbase), "s"(lds_dst) : "memory");
}
DI u32 umin_(u32 a, u32 b) { return a < b ? a : b; }
DI u32 umax_(u32 a, u32 b) { return a > b ? a : b; }
DI u32 san(u32 v) { return umin_(v, 0xFFFFFFFEu); }
DI u32x4 san4(u32x4 v) { return u32x4{san(v[0]), san(v[1]), san(v[2]), san(v[3])}; }
DI u32 max4(u32x4 v) { return umax_(umax_(v[0], v[1]), umax_(v[2], v[3])); }
DI float xchg16_max(float v) { auto r = __builtin_amdgcn_permlane16_swap(__float_as_uint(v), __float_as_uint(v), false, false); return fmaxf(__uint_as_float(r[0]), __uint_as_float(r[1])); }
DI float xchg16_min(float v) { auto r = __builtin_amdgcn_permlane16_swap(__float_as_uint(v), __float_as_uint(v), false, false); return fminf(__uint_as_float(r[0]), __uint_as_float(r[1])); }
DI float xchg16_add(float v) { auto r = __builtin_amdgcn_permlane16_swap(__float_as_uint(v), __float_as_uint(v), false, false); return __uint_as_float(r[0]) + __uint_as_float(r[1]); }
DI float xchg32_add(float v) { auto r = __builtin_amdgcn_permlane32_swap(__float_as_uint(v), __float_as_uint(v), false, false); return __uint_as_float(r[0]) + __uint_as_float(r[1]); }
template <int CTRL> DI float dpp(float v) { return __int_as_float(__builtin_amdgcn_update_dpp(0, __float_as_int(v), CTRL, 0xf, 0xf, true)); }
template <int CTRL> DI u32 dppu(u32 v) { return (u32)__builtin_amdgcn_update_dpp(0, (int)v, CTRL, 0xf, 0xf, true); }
DI float wave_sum(float v) {
  v += dpp<0xB1>(v); v += dpp<0x4E>(v); v += dpp<0x141>(v); v += dpp<0x140>(v);
  return xchg32_add(xchg16_add(v));
}
DI float row_sum(float v) { v += dpp<0xB1>(v); v += dpp<0x4E>(v); v += dpp<0x141>(v); return v + dpp<0x140>(v); }
DI u32 row_max(u32 v) { v = umax_(v, dppu<0xB1>(v)); v = umax_(v, dppu<0x4E>(v)); v = umax_(v, dppu<0x141>(v)); return umax_(v, dppu<0x140>(v)); }
DI u32x4 ld4w(const char* base, u32 off) { u32x4 v; asm volatile("global_load_dwordx4 %0, %1, %2 sc1\n\ts_waitcnt vmcnt(0)" : "=&v"(v) : "v"(off), "s"(base) : "memory"); return v; }
DI u32x2 ld2w(const char* base, u32 off) { u32x2 v; asm volatile("global_load_dwordx2 %0, %1, %2 sc1\n\ts_waitcnt vmcnt(0)" : "=&v"(v) : "v"(off), "s"(base) : "memory"); return v; }
#define MEMFENCE() asm volatile("" ::: "memory")
#define BARRIER() asm volatile("s_waitcnt lgkmcnt(0)\n\ts_barrier" ::: "memory")
#define SB() __builtin_amdgcn_sched_barrier(0)
#define LD4(off) __builtin_amdgcn_raw_buffer_load_b128(wsr, (off), 0, 16)
#define LD2(off) __builtin_amdgcn_raw_buffer_load_b64(wsr, (off), 0, 16)
#define ST4(v, off) __builtin_amdgcn_raw_buffer_store_b128((v), wsr, (off), 0, 16)
#define ST2(v, off) __builtin_amdgcn_raw_buffer_store_b64((v), wsr, (off), 0, 16)

#define STAMP(k) do { } while (0)

DI void softmax_pack(const f32x4& p0, const f32x4& p1, const f32x4& p2, u32 (&o)[10]) {
  float p[10] = {p0[0], p0[1], p0[2], p0[3], p1[0], p1[1], p1[2], p1[3], p2[0], p2[1]};
  float mx = p[0], mn = p[0];
#pragma unroll
  for (int i = 1; i < 10; i++) { mx = fmaxf(mx, p[i]); mn = fminf(mn, p[i]); }
  mx = xchg16_max(mx); mn = xchg16_min(mn);
  const float L2E = 1.4426950408889634f;
  const float a = -mx * L2E, b = mn * L2E;
  float e1[10], e2[10], s1 = 0.f, s2 = 0.f;
#pragma unroll
  for (int i = 0; i < 10; i++) {
    e1[i] = __builtin_amdgcn_exp2f(fmaf(p[i], L2E, a));
    e2[i] = __builtin_amdgcn_exp2f(fmaf(p[i], -L2E, b));
    s1 += e1[i]; s2 += e2[i];
  }
  s1 = xchg16_add(s1); s2 = xchg16_add(s2);
  const float r1 = rcp(s1), r2 = rcp(s2);
#pragma unroll
  for (int i = 0; i < 5; i++) {
    o[i] = pk2(e1[2 * i] * r1, e1[2 * i + 1] * r1);
    o[5 + i] = pk2(e2[2 * i] * r2, e2[2 * i + 1] * r2);
  }
}

#define THR "3"

#define SIG_SET(f, v) do { MEMFENCE(); *(volatile LAS u32*)(sm + L_SYN + (f) * 4) = (v); MEMFENCE(); } while (0)
DI void sig_add(u32 addr) {
  u64 keep; u32 one = 1;
  asm volatile("s_mov_b64 %0, exec\n\ts_mov_b64 exec, 1\n\tds_add_u32 %1, %2\n\ts_mov_b64 exec, %0" : "=&s"(keep) : "v"(addr), "v"(one) : "memory");
}
#define SIG_ADD(f) sig_add(ldsb + L_SYN + (f) * 4)
DI void sig_wait(u32 addr, u32 val) {
  u32 tv, ts, cnt;
  asm volatile(
      "s_mov_b32 %2, 0x400000\n"
      "LW%=:\n\t"
      "ds_read_b32 %0, %3\n\t"
      "s_waitcnt lgkmcnt(0)\n\t"
      "v_readfirstlane_b32 %1, %0\n\t"
      "s_nop 1\n\t"
      "s_cmp_ge_u32 %1, %4\n\t"
      "s_cbranch_scc1 LD%=\n\t"
      "s_sleep 1\n\t"
      "s_sub_u32 %2, %2, 1\n\t"
      "s_cmp_lg_u32 %2, 0\n\t"
      "s_cbranch_scc1 LW%=\n"
      "LD%=:"
      : "=&v"(tv), "=&s"(ts), "=&s"(cnt) : "v"(addr), "s"(val) : "memory", "scc");
}
DI void sig_wait2(u32 a0, u32 v0, u32 a1, u32 v1) {
  u32 t0, t1, s0, s1, cnt;
  asm volatile(
      "s_mov_b32 %4, 0x400000\n"
      "LW%=:\n\t"
      "ds_read_b32 %0, %5\n\t"
      "ds_read_b32 %1, %7\n\t"
      "s_waitcnt lgkmcnt(0)\n\t"
      "v_readfirstlane_b32 %2, %0\n\t"
      "v_readfirstlane_b32 %3, %1\n\t"
      "s_nop 1\n\t"
      "s_cmp_ge_u32 %2, %6\n\t"
      "s_cbranch_scc0 LS%=\n\t"
      "s_cmp_ge_u32 %3, %8\n\t"
      "s_cbranch_scc1 LD%=\n"
      "LS%=:\n\t"
      "s_sleep 1\n\t"
      "s_sub_u32 %4, %4, 1\n\t"
      "s_cmp_lg_u32 %4, 0\n\t"
      "s_cbranch_scc1 LW%=\n"
      "LD%=:"
      : "=&v"(t0), "=&v"(t1), "=&s"(s0), "=&s"(s1), "=&s"(cnt) : "v"(a0), "s"(v0), "v"(a1), "s"(v1) : "memory", "scc");
}
#define SIG_WAIT2(f0, v0, f1, v1) sig_wait2(ldsb + L_SYN + (f0) * 4, (v0), ldsb + L_SYN + (f1) * 4, (v1))
#define SIG_WAIT(f, v) sig_wait(ldsb + L_SYN + (f) * 4, (v))
#define WPRIO 0
#define CLR_AT 4
#define PF_AT 0
#define KVD_AT 2
#define WO_FIRST 0
#define GW_AT 4
#define GTHR "40"
#define REC_AT 3
#define ST_AT 2
#define GW_SIG F_W0
#define H1D 8
#define H1S 4
#define PFLATE 0
#define WTHR "40"
#define WORD 1
#define M3_SIG F_P2
#define M3_AT 10
#define H2S 0
#define OGX 1
#define MPRIO 3
#define M2_SIG F_KR
#define M2_AT 8
#define QW4 5
#define RAWBAR() asm volatile("s_barrier" ::: "memory")
__global__ __launch_bounds__(768, 3) void fused_kernel(
    const u16* __restrict__ x, const u16* __restrict__ wqkv, const u16* __restrict__ wphi, const u16* __restrict__ wo,
    const u16* __restrict__ wr, const u16* __restrict__ wg, const u16* __restrict__ wu, const u16* __restrict__ wd,
    u16* __restrict__ out, char* __restrict__ ws) {
  __shared__ __attribute__((aligned(1024))) char smem_[L_TOTAL];
  LAS char* sm = (LAS char*)smem_;
  const u32 ldsb = (u32)(size_t)smem_;
  STAMP(0);
  const int tid = threadIdx.x, l = tid & 63;
  const int w = __builtin_amdgcn_readfirstlane(tid >> 6);
  const int g = l >> 4, i16 = l & 15, q4 = i16 >> 2, p4 = l & 3;
  const int b = blockIdx.x, xcd = b & 7, slot = b >> 3;
  const int rt = (xcd >> 1) * 4 + (slot >> 3), hg = (xcd & 1) * 8 + (slot & 7);
  const int wgi = rt * 16 + hg;
  const bool fast = (0xA5 >> (hg & 7)) & 1;
  const int og = hg ^ OGX;
  const int rot = (hg + 2 * rt) % 5;
  const auto wsr = __builtin_amdgcn_make_buffer_rsrc((void*)ws, 0, WS_END, 0x00020000);
  const u32 reg = WS_REG + wgi * RS;
#define CROT(c) ((c) + rot >= 5 ? (c) + rot - 5 : (c) + rot)
#define FR(F, t) (u32x4{F[2 * (t)][0], F[2 * (t)][1], F[2 * (t) + 1][0], F[2 * (t) + 1][1]})
#define LOADEP() u32x2 ep; { const u32 z = 0; asm volatile("global_load_dwordx2 %0, %1, %2 sc1" : "=v"(ep) : "v"(z), "s"(ws) : "memory"); }
#define GETTAG() u32 tag_lo, tag_hi; { u64 e = (((u64)ep[1] << 32) | ep[0]) + 1; tag_lo = __builtin_amdgcn_readfirstlane((u32)e); tag_hi = __builtin_amdgcn_readfirstlane((u32)(e >> 32)); }

  if (tid < 32) *(LAS u32*)(sm + L_SYN + tid * 4) = 0u;
  if (w >= 8) {
    const int h = w - 8;
    __builtin_amdgcn_s_setprio(3);
    int l_ = __builtin_amdgcn_mbcnt_hi(~0u, __builtin_amdgcn_mbcnt_lo(~0u, 0u));
    asm volatile("" : "+v"(l_));
    const int l = l_, tid = w * 64 + l, g = l >> 4, i16 = l & 15, q4 = i16 >> 2, p4 = l & 3;
    const u32x4 ONES = {0x3F803F80u, 0x3F803F80u, 0x3F803F80u, 0x3F803F80u};
    if (h < 2) {
      u32 woff[5];
#pragma unroll
      for (int k = 0; k < 5; k++) {
        int P = 64 * k + l, rho = P / 10, s = P - 10 * rho;
        woff[k] = (32 * ((rho >> 2) & 1) + 16 * ((rho >> 3) & 1) + 4 * ((rho >> 4) & 1) + (rho & 3)) * 3840 + 1280 + (s >= 5 ? 1200 : 0) + hg * 80 + s * 16;
      }
      const char* wbase = (const char*)wqkv + h * (320 * 3840);
#define WL(c) do { const char* b_ = wbase + CROT(c) * (64 * 3840); _Pragma("unroll") for (int k = 0; k < 10; k++) \
        glds16(b_ + (k / 5) * (8 * 3840), woff[k % 5], ldsb + L_W1 + (4 * (c) + 2 * (k / 5) + h) * 5120 + (k % 5) * 1024); } while (0)
#define WDONE2(N, c) do { asm volatile("s_waitcnt vmcnt(%0)" :: "i"(N) : "memory"); SIG_SET(F_W0 + h, (c) + 1); } while (0)
#define WDONE(N, c) do { asm volatile("s_waitcnt vmcnt(" #N ")" ::: "memory"); SIG_SET(F_W0 + h, (c) + 1); } while (0)
      WL(0);
      BARRIER();
      WDONE(0, 0);
      WL(1);
      STAMP(1);
      WDONE(0, 1);
      __builtin_amdgcn_s_setprio(0);
      WL(2);
      __builtin_amdgcn_s_setprio(3);
      STAMP(2);
      WL(3);
      STAMP(3);
      WDONE(10, 2); WL(4);
      STAMP(4);
      u32 qoff[3], ooff[3], po[7];
#pragma unroll
      for (int k = 0; k < 3; k++) {
        int P = 64 * k + l, rho = P / 6, s = P - 6 * rho; if (s > 4) s = 4;
        qoff[k] = (32 * ((rho >> 2) & 1) + 16 * ((rho >> 3) & 1) + 4 * ((rho >> 4) & 1) + (rho & 3)) * 3840 + hg * 80 + s * 16;
        ooff[k] = (8 * ((rho >> 2) & 3) + 4 * ((rho >> 4) & 1) + (rho & 3)) * 1280 + og * 80 + s * 16;
      }
#pragma unroll
      for (int k = 0; k < 7; k++) { const int qq = slot * 100 + h * 50 + (l >> 3) + (k < 6 ? 8 * k : 42), row = qq / 5, cc_ = qq - 5 * row; po[k] = row * 1280 + (xcd & 1) * 640 + cc_ * 128 + (l & 7) * 16; }
      WDONE(10, 3);
      u32x4 wr_[30];
#define OROT(k) (2 * CROT((k) / 6) + ((k) / 3 & 1))
#pragma unroll
      for (int k = 0; k < 30; k++) {
        asm volatile("global_load_dwordx4 %0, %1, %2" : "=v"(wr_[k]) : "v"(qoff[k % 3]), "s"(wbase + CROT(k / 6) * (64 * 3840) + ((k / 3) & 1) * (8 * 3840)));
        if (k == QW4) { WDONE2(QW4 + 1, 4); STAMP(5); }
      }
#pragma unroll
      for (int k = 0; k < 7; k++) glds16(wo, po[k], ldsb + L_PF);
      STAMP(6);
      SIG_WAIT(F_P1, 10);
      asm volatile("s_waitcnt vmcnt(7)" ::: "memory");
      STAMP(7);
#pragma unroll
      for (int k = 0; k < 30; k++) {
        asm volatile("" : "+v"(wr_[k]));
        *(LAS u32x4*)(sm + L_WQ + (2 * (k / 3) + h) * 3072 + (k % 3) * 1024 + l * 16) = wr_[k];
      }
      SIG_SET(F_Q0 + h, 1);
      STAMP(8);
      __builtin_amdgcn_s_setprio(0);
#pragma unroll
      for (int k = 0; k < 5; k++) ST4((u32x4{SENT, SENT, SENT, SENT}), reg + R_OB + (h * 5 + k) * 1024 + l * 16);
#pragma unroll
      for (int k = 0; k < WO_FIRST; k++)
        asm volatile("global_load_dwordx4 %0, %1, %2" : "=v"(wr_[k]) : "v"(ooff[k % 3]), "s"((const char*)wo + (10 * h + OROT(k)) * 40960));
      {
        f32x4 cq[3];
#pragma unroll
        for (int t = 0; t < 3; t++) cq[t] = f32x4{0, 0, 0, 0};
        LAS char* wqp = sm + L_WQ + h * 3072 + (4 * g + q4) * 96 + 8 * p4;
#pragma unroll 1
        for (int k = 0; k < 10; k++)
#pragma unroll
          for (int t = 0; t < 3; t++) cq[t] = mfma32(ONES, trd2(wqp + k * 6144 + 32 * t, 1536), cq[t]);
#pragma unroll
        for (int t = 0; t < 3; t++) *(LAS float*)(sm + L_CSQ + (48 * h + 16 * t + i16) * 4) = cq[t][0];
        asm volatile("s_waitcnt vmcnt(0)" ::: "memory");
        SIG_ADD(F_CSQ);
        SIG_ADD(F_P2);
      }
#pragma unroll
      for (int k = WO_FIRST; k < 30; k++)
        asm volatile("global_load_dwordx4 %0, %1, %2\n\ts_waitcnt vmcnt(" WTHR ")" : "=v"(wr_[k]) : "v"(ooff[k % 3]), "s"((const char*)wo + (10 * h + OROT(k)) * 40960));
      STAMP(9);
      SIG_WAIT(h ? F_KR : F_P2, h ? 8 : 10);
      asm volatile("s_waitcnt vmcnt(0)" ::: "memory");
      STAMP(10);
      __builtin_amdgcn_s_setprio(0);
#pragma unroll
      for (int k = 0; k < 30; k++) {
        asm volatile("" : "+v"(wr_[k]));
        *(LAS u32x4*)(sm + (h ? L_WO1 : L_WO0) + OROT(k) * 3072 + (k % 3) * 1024 + l * 16) = wr_[k];
      }
      SIG_ADD(F_WO);
      STAMP(12);
    } else {
      const int e = h - 2;
#define STATS_LD() u32x4 sx[5], sw[5]; { const u32 o = (4 * e + g) * 1280 + i16 * 16, o2 = i16 * 16; \
          _Pragma("unroll") for (int k = 0; k < 5; k++) { asm volatile("global_load_dwordx4 %0, %1, %2 offset:%3" : "=v"(sx[k]) : "v"(o), "s"(x + wgi * 8 * 640), "i"(k * 256)); \
            asm volatile("global_load_dwordx4 %0, %1, %2 offset:%3" : "=v"(sw[k]) : "v"(o2), "s"(wr), "i"(k * 256)); } }
#define STATS() do { _Pragma("unroll") for (int k = 0; k < 5; k++) asm volatile("" : "+v"(sx[k]), "+v"(sw[k])); \
          float s = 0.f, q = 0.f; \
          _Pragma("unroll") for (int k = 0; k < 20; k++) { s = dot2(sx[k >> 2][k & 3], 0x3F803F80u, s); q = dot2(sx[k >> 2][k & 3], sx[k >> 2][k & 3], q); } \
          const float mean = row_sum(s) * (1.f / 640.f); \
          const float var = fmaxf(row_sum(q) * (1.f / 640.f) - mean * mean, 0.f); \
          const float mb = rbf(mean), sb = rbf(__builtin_amdgcn_sqrtf(rbf(rbf(var) + 1e-5f))), rsb = rcp(sb); \
          const u32 ms = pk2(mb, sb); const int srow = wgi * 8 + 4 * e + g; \
          if (i16 == 0) ST4((u32x4{ms, tag_lo, ms, tag_hi}), WS_ST + srow * 16); \
          float r = 0.f; \
          _Pragma("unroll") for (int k = 0; k < 20; k++) { const u32 xd = sx[k >> 2][k & 3]; \
            u32 d = pk2(bflo(xd) - mb, bfhi(xd) - mb), n = pk2(bflo(d) * rsb, bfhi(d) * rsb); r = dot2(n, sw[k >> 2][k & 3], r); } \
          r = row_sum(r); \
          const u32 rb = pk2(r, 0.f) & 0xFFFFu; \
          const u32 ord = (rb & 0x8000u) ? (~rb & 0xFFFFu) : (rb | 0x8000u); \
          if (i16 == 0) *(LAS u32x2*)(sm + L_KEY + (4 * e + g) * 8) = u32x2{(ord << 16) | (u32)(0x7FFF - srow), ms}; \
          SIG_ADD(F_ST); } while (0)
      if (e == 1) {
        LOADEP();
        STATS_LD();
        BARRIER();
        if (l < 14) ST4((u32x4{SENT, SENT, SENT, SENT}), reg + R_KVR + l * 16);
        asm volatile("s_waitcnt vmcnt(1)" : "+v"(ep) :: "memory");
        GETTAG();
        STAMP(1);
        STATS();
        {
          SIG_WAIT(F_ST, 2);
          u32x2 kk = *(LAS u32x2*)(sm + L_KEY + (l & 7) * 8);
          u32 km = row_max(kk[0]);
          if (l < 8 && kk[0] == km) ST4((u32x4{kk[0], tag_lo, kk[1], tag_hi}), WS_REC + b * 16);
        }
        STAMP(2);
        u32x4 s0 = {0, 0, 0, 0}, s1 = {0, 0, 0, 0};
        const u32 sta = WS_ST + (rt * 128 + l) * 16;
#define LDST() asm volatile("global_load_dwordx4 %0, %2, %3 sc1\n\tglobal_load_dwordx4 %1, %2, %3 offset:1024 sc1\n\ts_waitcnt vmcnt(0)" : "=&v"(s0), "=&v"(s1) : "v"(sta), "s"(ws) : "memory")
        f32x4 cs[5];
#pragma unroll
        for (int t = 0; t < 5; t++) cs[t] = f32x4{0, 0, 0, 0};
        LAS char* w1p = sm + L_W1 + (4 * g + q4) * 160 + 8 * p4;
#pragma unroll 1
        for (int c = 0; c < 5; c++) {
          if (c == ST_AT) LDST();
          SIG_WAIT2(F_W0, c + 1, F_W1, c + 1);
          if (c == CLR_AT) {
#pragma unroll
            for (int k = 0; k < 7; k++) ST4((u32x4{SENT, SENT, SENT, SENT}), (k * 64 + l < 420) ? reg + R_KVP + (k * 64 + l) * 16 : WS_END);
          }
#pragma unroll
          for (int k = 0; k < 4; k++)
#pragma unroll
            for (int t = 3; t < 5; t++) cs[t] = mfma32(ONES, trd2(w1p + (4 * c + k) * 5120 + 32 * t, 2560), cs[t]);
          if (fast) {
#pragma unroll
            for (int k = 0; k < 4; k++)
#pragma unroll
              for (int t = 0; t < 3; t++) cs[t] = mfma32(ONES, trd2(w1p + (4 * c + k) * 5120 + 32 * t, 2560), cs[t]);
          }
          if (c == 2) { SIG_ADD(F_P1); if (fast) SIG_ADD(F_P1); }
        }
        STAMP(3);
#pragma unroll 1
        for (u32 spin = 0; spin < SPIN_CAP; spin++) {
          if (__all(s0[1] == tag_lo && s0[3] == tag_hi && s0[0] == s0[2] && s1[1] == tag_lo && s1[3] == tag_hi && s1[0] == s1[2])) break;
          __builtin_amdgcn_s_sleep(4);
          LDST();
        }
        *(LAS u32*)(sm + L_MS + l * 4) = s0[0];
        *(LAS u32*)(sm + L_MS + 256 + l * 4) = s1[0];
        *(LAS float*)(sm + L_CSP + (48 + i16) * 4) = cs[3][0]; *(LAS float*)(sm + L_CSP + (64 + i16) * 4) = cs[4][0];
        if (fast) {
#pragma unroll
          for (int t = 0; t < 3; t++) *(LAS float*)(sm + L_CSP + (16 * t + i16) * 4) = cs[t][0];
          SIG_ADD(F_CS);
        }
        SIG_ADD(F_CS);
        STAMP(4);
        asm volatile("s_waitcnt vmcnt(0)" ::: "memory");
        SIG_ADD(F_PHI);
        if (l < 16) ST2((u32x2{tag_lo, tag_hi}), WS_REG + (l * 16 + hg) * RS + R_I1 + rt * 8);
        STAMP(11);
        SIG_WAIT(F_PHI, 2);
        STAMP(5);
#define HINT(addr) do { _Pragma("unroll 1") for (u32 spin = 0; spin < SPIN_CAP; spin++) { u32x4 f_ = ld4w(ws, (addr) + (l & 7) * 16); \
          if (__all(f_[0] == tag_lo && f_[1] == tag_hi && f_[2] == tag_lo && f_[3] == tag_hi)) break; __builtin_amdgcn_s_sleep(4); } } while (0)
        HINT(reg + R_I1);
        SIG_WAIT(F_KR, 8);
        __builtin_amdgcn_s_sleep(H1D);
        STAMP(6);
        {
          const int he = l >= 28 ? l - 28 : l;
          const bool ok = (l < 56) && (rt < 15);
          const u32 h1a = ok ? WS_REG + ((l >= 28 ? 128 : 0) + hg) * RS + R_KVP + (28 * rt + he) * 16 : WS_END;
          u32x4 h1[8];
#pragma unroll 1
          for (u32 spin = 0; spin < SPIN_CAP; spin++) {
            MEMFENCE();
            u32 m = 0;
#pragma unroll
            for (int k = 0; k < 8; k++) h1[k] = __builtin_amdgcn_raw_buffer_load_b128(wsr, h1a, k * 16 * RS, 16);
#pragma unroll
            for (int k = 0; k < 8; k++) m = umax_(m, max4(h1[k]));
            if (__all(m != SENT)) break;
            __builtin_amdgcn_s_sleep(H1S);
          }
          f32x4 s = __builtin_bit_cast(f32x4, h1[0]);
#pragma unroll
          for (int k = 1; k < 8; k++) s += __builtin_bit_cast(f32x4, h1[k]);
#pragma unroll
          for (int r = 0; r < 4; r++) s[r] += __shfl(s[r], (l + 28) & 63);
          if (ok && l < 28) ST2((u32x2{san(pk2(s[0], s[1])), san(pk2(s[2], s[3]))}), reg + R_KVR + l * 8);
        }
        STAMP(7);
        {
          const int hpr = l / 14, e2 = l - 14 * hpr;
          const u32 h2a = WS_REG + (hpr * 16 + hg) * RS + R_KVR + e2 * 16;
          u32 ka[8];
#pragma unroll
          for (int k = 0; k < 8; k++) { int it = 112 * (k >> 1) + 2 * l + (k & 1), hf = it / 21, c = it - 21 * hf; ka[k] = L_KVT + c * 80 + hf * 8 + (hf >= 10 ? 1600 : 0); }
          u32x4 kv[4];
#pragma unroll 1
          for (u32 spin = 0; spin < SPIN_CAP; spin++) {
            MEMFENCE();
            u32 m = 0;
#pragma unroll
            for (int k = 0; k < 4; k++) kv[k] = LD4((l < 56 && 4 * k + hpr < 15) ? h2a + k * 64 * RS : WS_END);
#pragma unroll
            for (int k = 0; k < 4; k++) m = umax_(m, max4(kv[k]));
            if (__all(m != SENT)) break;
            __builtin_amdgcn_s_sleep(H2S);
          }
#pragma unroll
          for (int k = 0; k < 4; k++) if (l < 56 && 4 * k + hpr < 15) {
            *(LAS u32x2*)(sm + ka[2 * k]) = u32x2{kv[k][0], kv[k][1]};
            *(LAS u32x2*)(sm + ka[2 * k + 1]) = u32x2{kv[k][2], kv[k][3]};
          }
        }
        SIG_SET(F_KVT, 1);
        STAMP(8);
        SIG_WAIT(F_OS, 8);
        if (l < 16) ST2((u32x2{tag_lo, tag_hi}), WS_REG + (rt * 16 + l) * RS + R_I3 + hg * FS);
        STAMP(9);
#pragma unroll 1
        for (u32 spin = 0; spin < SPIN_CAP; spin++) {
          u32x2 f_ = ld2w(ws, reg + R_I3 + i16 * FS);
          const u64 okm = __ballot(f_[0] == tag_lo && f_[1] == tag_hi);
          if ((okm & 0xFF) == 0xFF) SIG_SET(F_O, 1);
          if ((okm & 0xFF00) == 0xFF00) SIG_SET(F_O1, 1);
          if ((okm & 0xFFFF) == 0xFFFF) break;
        }
        SIG_SET(F_O, 1); SIG_SET(F_O1, 1);
        STAMP(10);
      } else {
        LOADEP();
        STATS_LD();
        u32 wp[7];
#pragma unroll
        for (int k = 0; k < 7; k++) { const u32 o = hg * 1600 + (l + 64 * k < 400 ? l + 64 * k : 0) * 4; asm volatile("global_load_dword %0, %1, %2" : "=v"(wp[k]) : "v"(o), "s"(wphi)); }
        BARRIER();
        if (l < 4) *(LAS u32x4*)(sm + L_ZERO + l * 16) = u32x4{0, 0, 0, 0};
        if (l < 40) { *(LAS u32*)(sm + L_WPHI + l * 48 + 20) = 0u; *(LAS u32*)(sm + L_WPHI + l * 48 + 44) = 0u; }
        asm volatile("s_waitcnt vmcnt(0)" : "+v"(ep), "+v"(wp[0]), "+v"(wp[1]), "+v"(wp[2]), "+v"(wp[3]), "+v"(wp[4]), "+v"(wp[5]), "+v"(wp[6]) :: "memory");
        GETTAG();
        STATS();
#pragma unroll
        for (int k = 0; k < 7; k++) { const int u = l + 64 * k, row = u / 10, z = u - 10 * row; if (u < 400) *(LAS u32*)(sm + L_WPHI + row * 48 + z * 4 + (z >= 5 ? 4 : 0)) = wp[k]; }
        SIG_SET(F_WP, 1);
        STAMP(1);
        const int fi = wgi >> 1, cb = fi < 120 ? fi / 10 : 0, ksp = fi < 120 ? fi - 10 * cb : 0;
        if (fast) {
          SIG_WAIT(F_W0, REC_AT);
          const u32x4 S4 = {SENT, SENT, SENT, SENT};
          ST4(S4, WS_MG + fi * MGS + l * 16);
        }
        else {
          f32x4 cs[3];
#pragma unroll
          for (int t = 0; t < 3; t++) cs[t] = f32x4{0, 0, 0, 0};
          LAS char* w1p = sm + L_W1 + (4 * g + q4) * 160 + 8 * p4;
#pragma unroll 1
          for (int c = 0; c < 5; c++) {
            SIG_WAIT2(F_W0, c + 1, F_W1, c + 1);
#pragma unroll
            for (int k = 0; k < 4; k++)
#pragma unroll
              for (int t = 0; t < 3; t++) cs[t] = mfma32(ONES, trd2(w1p + (4 * c + k) * 5120 + 32 * t, 2560), cs[t]);
            if (c == 2) SIG_ADD(F_P1);
          }
#pragma unroll
          for (int t = 0; t < 3; t++) *(LAS float*)(sm + L_CSP + (16 * t + i16) * 4) = cs[t][0];
          SIG_ADD(F_CS);
#pragma unroll
          for (int zz = 0; zz < 3; zz++) { const int u = 64 * zz + l, sl = u / 10; ST4((u32x4{SENT, SENT, SENT, SENT}), u < 160 ? WS_YB + (sl * 128 + fi) * 160 + (u - 10 * sl) * 16 : WS_END); }
        }
        u32x2 rec;
        {
          u32 km = 0, ms = 0;
          u32x4 r_[4];
#pragma unroll 1
          for (u32 spin = 0; spin < SPIN_CAP; spin++) {
            const u32 ra = WS_REC + l * 16;
            asm volatile("global_load_dwordx4 %0, %4, %5 sc1\n\tglobal_load_dwordx4 %1, %4, %5 offset:1024 sc1\n\tglobal_load_dwordx4 %2, %4, %5 offset:2048 sc1\n\t"
                         "global_load_dwordx4 %3, %4, %5 offset:3072 sc1\n\ts_waitcnt vmcnt(0)"
                         : "=&v"(r_[0]), "=&v"(r_[1]), "=&v"(r_[2]), "=&v"(r_[3]) : "v"(ra), "s"(ws) : "memory");
            bool ok = true;
#pragma unroll
            for (int k = 0; k < 4; k++) ok = ok && r_[k][1] == tag_lo && r_[k][3] == tag_hi;
            if (__all(ok)) break;
            __builtin_amdgcn_s_sleep(16);
          }
#pragma unroll
          for (int k = 0; k < 4; k++) if (r_[k][0] > km) { km = r_[k][0]; ms = r_[k][2]; }
          u32 kx = row_max(km);
          { auto r = __builtin_amdgcn_permlane16_swap(kx, kx, false, false); kx = umax_(r[0], r[1]); }
          { auto r = __builtin_amdgcn_permlane32_swap(kx, kx, false, false); kx = umax_(r[0], r[1]); }
          const int src = __builtin_ctzll(__ballot(km == kx));
          rec = u32x2{(u32)__builtin_amdgcn_readfirstlane(kx), (u32)__builtin_amdgcn_readlane(ms, src)};
        }
        const int idx = (0x7FFF - (rec[0] & 0xFFFFu)) & 2047;
        if (b == 0 && l == 0) ST2((u32x2{tag_lo, tag_hi}), WS_EPOCH);
        SIG_SET(F_IDX, idx + 1);
        STAMP(4);
        const u32 mreg = WS_MG + fi * MGS;
        if (fast) {
          {
            const int c0 = (cb % 6) * 256, d = c0 / 12 + l;
            if (fi < 120 && 12 * d < c0 + 256) ST2((u32x2{tag_lo, tag_hi}), WS_MG + d * MGS + 1024 + ((12 * d < c0 ? 20 : 0) + (cb / 6) * 10 + ksp) * FS);
          }
          u32x2 xr;
          { const u32 xo = (64 * ksp) * 2 + i16 * 8; asm volatile("global_load_dwordx2 %0, %1, %2" : "=v"(xr) : "v"(xo), "s"(x + idx * 640)); }
          u32x4 gw[32];
          __builtin_amdgcn_s_setprio(0);
          SIG_WAIT(GW_SIG, GW_AT);
          STAMP(2);
          {
            const u16* gm = cb < 6 ? wg + cb * 256 : wu + (cb - 6) * 256;
            const u32 go = (64 * ksp + 2 * (l >> 5)) * 3072 + (l & 31) * 16;
#pragma unroll
            for (int k = 0; k < 32; k++) asm volatile("global_load_dwordx4 %0, %1, %2 offset:%3" : "=v"(gw[k]) : "v"(go), "s"(gm + (k >> 1) * 4 * 1536), "i"((k & 1) * 3072));
          }
          __builtin_amdgcn_s_setprio(MPRIO);
          STAMP(3);
          {
            asm volatile("s_waitcnt vmcnt(16)" : "+v"(xr) :: "memory");
            const float sel_m = bflo(rec[1]), sel_r = rcp(bfhi(rec[1]));
            u32 xp = (l >> 5) ? xr[1] : xr[0];
            { u32 d = pk2(bflo(xp) - sel_m, bfhi(xp) - sel_m); xp = pk2(bflo(d) * sel_r, bfhi(d) * sel_r); }
            float v[8];
#pragma unroll
            for (int k = 0; k < 8; k++) v[k] = 0.f;
#define M1ROW(r) do { asm volatile("" : "+v"(gw[2 * r]), "+v"(gw[2 * r + 1])); const u32 a = dppu<0x150 + r>(xp); \
              _Pragma("unroll") for (int k = 0; k < 4; k++) { v[2 * k] = dot2(__builtin_amdgcn_perm(gw[2 * r + 1][k], gw[2 * r][k], 0x05040100u), a, v[2 * k]); \
                v[2 * k + 1] = dot2(__builtin_amdgcn_perm(gw[2 * r + 1][k], gw[2 * r][k], 0x07060302u), a, v[2 * k + 1]); } } while (0)
            M1ROW(0); M1ROW(1); M1ROW(2); M1ROW(3); M1ROW(4); M1ROW(5); M1ROW(6); M1ROW(7);
            asm volatile("s_waitcnt vmcnt(0)" ::: "memory");
            STAMP(5);
            M1ROW(8); M1ROW(9); M1ROW(10); M1ROW(11); M1ROW(12); M1ROW(13); M1ROW(14); M1ROW(15);
            u32x4 u4;
#pragma unroll
            for (int k = 0; k < 4; k++) {
              auto r = __builtin_amdgcn_permlane32_swap(__float_as_uint(v[k]), __float_as_uint(v[k + 4]), false, false);
              u4[k] = __float_as_uint(__uint_as_float(r[0]) + __uint_as_float(r[1]));
            }
            ST4(san4(u4), fi < 120 ? mreg + (l & 31) * 32 + (l >> 5) * 16 : WS_END);
          }
          __builtin_amdgcn_s_setprio(3);
          STAMP(6);
        } else {
          if (l < 16) ST2((u32x2{tag_lo, tag_hi}), WS_IY + (l * 128 + fi) * FS);
          u32x2 dw[36];
          {
            const u16* dp = wd + (12 * fi) * 640;
            const u32 dof = l * 8, dof2 = (l < 32 ? l : 0) * 8;
#pragma unroll
            for (int k = 0; k < 12; k++) {
              asm volatile("global_load_dwordx2 %0, %1, %2" : "=v"(dw[3 * k]) : "v"(dof), "s"(dp + k * 640));
              asm volatile("global_load_dwordx2 %0, %1, %2 offset:512" : "=v"(dw[3 * k + 1]) : "v"(dof), "s"(dp + k * 640));
              asm volatile("global_load_dwordx2 %0, %1, %2 offset:1024" : "=v"(dw[3 * k + 2]) : "v"(dof2), "s"(dp + k * 640));
            }
          }
          STAMP(7);
          {
            const int nsl = ((12 * fi) >> 8) == ((12 * fi + 11) >> 8) ? 20 : 40;
#pragma unroll 1
            for (u32 spin = 0; spin < SPIN_CAP; spin++) {
              u32x2 f0 = ld2w(ws, mreg + 1024 + (l < nsl ? l : 0) * FS);
              if (__all(f0[0] == tag_lo && f0[1] == tag_hi)) break;
              __builtin_amdgcn_s_sleep(6);
            }
          }
          SIG_WAIT(M2_SIG, M2_AT);
          STAMP(8);
          {
            const int kw = l / 6, pr = l - 6 * kw, col = 12 * fi + 2 * pr;
            const u32 ga = l < 60 ? WS_MG + ((col >> 8) * 10 + kw) * MGS + (col & 255) * 4 : WS_END;
            u32x2 g2, u2;
#pragma unroll 1
            for (u32 spin = 0; spin < SPIN_CAP; spin++) {
              MEMFENCE();
              g2 = LD2(ga); u2 = __builtin_amdgcn_raw_buffer_load_b64(wsr, ga, 60 * MGS, 16);
              if (__all(umax_(umax_(g2[0], g2[1]), umax_(u2[0], u2[1])) != SENT)) break;
              __builtin_amdgcn_s_sleep(8);
            }
            STAMP(15);
            f32x2 gg2 = __builtin_bit_cast(f32x2, g2), uu2 = __builtin_bit_cast(f32x2, u2);
            float z[4] = {gg2[0], gg2[1], uu2[0], uu2[1]};
#pragma unroll
            for (int k = 0; k < 4; k++) {
              const float z0 = z[k] + __shfl(z[k], (l + 30) & 63);
              z[k] = rbf(z0 + __shfl(z0, (l + 6) & 63) + __shfl(z0, (l + 12) & 63) + __shfl(z0, (l + 18) & 63) + __shfl(z0, (l + 24) & 63));
            }
            const u32 hp2 = pk2(rbf(z[0] * rcp(1.f + __expf(-z[0]))) * z[2], rbf(z[1] * rcp(1.f + __expf(-z[1]))) * z[3]);
#pragma unroll
            for (int k = 0; k < 36; k++) asm volatile("" : "+v"(dw[k]));
#pragma unroll
            for (int zz = 0; zz < 3; zz++) {
              f32x4 y = {0, 0, 0, 0};
#pragma unroll
              for (int i = 0; i < 6; i++) {
                const u32 hh = __builtin_amdgcn_readlane(hp2, i);
                const u32x2 d0 = dw[6 * i + zz], d1 = dw[6 * i + 3 + zz];
                y[0] = dot2(__builtin_amdgcn_perm(d1[0], d0[0], 0x05040100u), hh, y[0]); y[1] = dot2(__builtin_amdgcn_perm(d1[0], d0[0], 0x07060302u), hh, y[1]);
                y[2] = dot2(__builtin_amdgcn_perm(d1[1], d0[1], 0x05040100u), hh, y[2]); y[3] = dot2(__builtin_amdgcn_perm(d1[1], d0[1], 0x07060302u), hh, y[3]);
              }
              { const int u = 64 * zz + l, sl = u / 10; ST4(san4(__builtin_bit_cast(u32x4, y)), u < 160 ? WS_YB + (sl * 128 + fi) * 160 + (u - 10 * sl) * 16 : WS_END); }
            }
          }
        }
        STAMP(9);
        if ((idx >> 7) == rt) {
          SIG_WAIT(M3_SIG, M3_AT);
          STAMP(11);
#pragma unroll 1
          for (u32 spin = 0; spin < SPIN_CAP; spin++) {
            __builtin_amdgcn_s_sleep(6);
            u32x2 f0, f1; const u32 fa_ = WS_IY + (hg * 128 + l) * FS, fb_ = fa_ + 64 * FS;
            asm volatile("global_load_dwordx2 %0, %2, %4 sc1\n\tglobal_load_dwordx2 %1, %3, %4 sc1\n\ts_waitcnt vmcnt(0)" : "=&v"(f0), "=&v"(f1) : "v"(fa_), "v"(fb_), "s"(ws) : "memory");
            if (__all(f0[0] == tag_lo && f0[1] == tag_hi && f1[0] == tag_lo && f1[1] == tag_hi)) break;
          }
          STAMP(12);
          f32x4 t;
          {
            const u32 ya = l < 60 ? WS_YB + og * 20480 + l * 16 : WS_END, yl = l < 20 ? ya : WS_END;
            f32x4 s_;
#pragma unroll 1
            for (u32 spin = 0; spin < SPIN_CAP; spin++) {
              MEMFENCE();
              u32x4 yv[22];
#pragma unroll
              for (int k = 0; k < 22; k++) yv[k] = __builtin_amdgcn_raw_buffer_load_b128(wsr, k < 21 ? ya : yl, k * 960, 16);
              u32 m = 0; s_ = f32x4{0, 0, 0, 0};
#pragma unroll
              for (int k = 0; k < 22; k++) { m = umax_(m, max4(yv[k])); s_ += __builtin_bit_cast(f32x4, yv[k]); }
              if (__all(m != SENT)) break;
              __builtin_amdgcn_s_sleep(6);
            }
            STAMP(13);
#pragma unroll
            for (int r = 0; r < 4; r++) { const float a_ = s_[r] + __shfl(s_[r], (l + 30) & 63); t[r] = a_ + __shfl(a_, (l + 10) & 63) + __shfl(a_, (l + 20) & 63); }
          }
          u32 rb = rec[0] >> 16;
          rb = (rb & 0x8000u) ? (rb & 0x7FFFu) : (~rb & 0xFFFFu);
          const float sig = rbf(rcp(1.f + __expf(-bflo(rb))));
          if (l < 10) *(LAS f32x4*)(sm + L_Y + l * 16) = f32x4{rbf(rbf(t[0]) * sig), rbf(rbf(t[1]) * sig), rbf(rbf(t[2]) * sig), rbf(rbf(t[3]) * sig)};
        }
        SIG_SET(F_Y, idx + 1);
        STAMP(10);
      }
    }
  } else {
    const int tg = w & 3, kh = w >> 2, tw = 2 * tg + kh;
    u32x4 xf[20];
    const char* xb = (const char*)x + rt * (128 * 1280) + kh * 640;
    const u32 xoff = (u32)((((g >> 1) ? (tw ^ 1) : tw) * 16 + 4 * q4) * 1280 + (g & 1) * 64 + p4 * 16);
#define XI(c, r) ((r) < 2 ? 2 * (c) + (r) : 8 + 2 * (c) + (r))
#define XL1(c, r) asm volatile("global_load_dwordx4 %0, %1, %2 offset:%3" : "=v"(xf[XI(c, r)]) : "v"(xoff), "s"(xb + CROT(c) * 128), "i"((r) * 1280))
#define XG(c) do { XL1(c, 0); XL1(c, 1); XL1(c, 2); XL1(c, 3); } while (0)
    __builtin_amdgcn_s_setprio(2);
    XG(0);
    BARRIER();
    XG(1); XG(2); XG(3); XG(4);
    __builtin_amdgcn_s_setprio(0);
    STAMP(1);
    f32x4 acc[2][5];
#pragma unroll
    for (int t = 0; t < 5; t++) { acc[0][t] = f32x4{0, 0, 0, 0}; acc[1][t] = f32x4{0, 0, 0, 0}; }
    LAS char* w1p = sm + L_W1 + kh * 5120 + (4 * g + q4) * 160 + 8 * p4;
    const u64 mE = 0x5555555555555555ull, mO = ~mE, mL = 0x3333333333333333ull, mH = ~mL;
    u32 ta[4], ua[4];
#define DPX1 " quad_perm:[1,0,3,2] row_mask:0xf bank_mask:0xf\n\t"
#define DPX2 " quad_perm:[2,3,0,1] row_mask:0xf bank_mask:0xf\n\t"
#define CD2(M, X, d0, s0, k0, d1, s1, k1) asm volatile("s_mov_b64 vcc, %6\n\tv_cndmask_b32_dpp %0, %2, %3, vcc" X "v_cndmask_b32_dpp %1, %4, %5, vcc" X \
      : "=&v"(d0), "=&v"(d1) : "v"(s0), "v"(k0), "v"(s1), "v"(k1), "s"(M) : "vcc")
#define CD2I(M, X, d0, s0, d1, s1) asm volatile("s_mov_b64 vcc, %4\n\tv_cndmask_b32_dpp %0, %2, %0, vcc" X "v_cndmask_b32_dpp %1, %3, %1, vcc" X \
      : "+v"(d0), "+v"(d1) : "v"(s0), "v"(s1), "s"(M) : "vcc")
#define A_(c, r) xf[XI(c, r)]
#define TP(c, k) do { const int d_ = (k) & 3; u32 n0, n1; \
      if ((k) < 4) { CD2(mE, DPX1, n0, A_(c, 1)[d_], A_(c, 0)[d_], n1, A_(c, 3)[d_], A_(c, 2)[d_]); ua[d_] = n0; ta[d_] = n1; } \
      else if ((k) < 8) { n0 = A_(c, 1)[d_]; n1 = A_(c, 3)[d_]; CD2I(mO, DPX1, n0, A_(c, 0)[d_], n1, A_(c, 2)[d_]); A_(c, 1)[d_] = n0; A_(c, 3)[d_] = n1; } \
      else if ((k) < 12) { CD2(mL, DPX2, n0, ta[d_], ua[d_], n1, A_(c, 3)[d_], A_(c, 1)[d_]); A_(c, 0)[d_] = n0; A_(c, 2)[d_] = n1; } \
      else if ((k) < 16) { n1 = A_(c, 3)[d_]; CD2I(mH, DPX2, ta[d_], ua[d_], n1, A_(c, 1)[d_]); A_(c, 3)[d_] = n1; } \
      else { auto r0 = __builtin_amdgcn_permlane32_swap(A_(c, 0)[d_], ta[d_], false, false); auto r1 = __builtin_amdgcn_permlane32_swap(A_(c, 2)[d_], A_(c, 3)[d_], false, false); \
        A_(c, 0)[d_] = r0[0]; A_(c, 2)[d_] = r0[1]; A_(c, 1)[d_] = r1[0]; A_(c, 3)[d_] = r1[1]; } } while (0)
#define XW(N, c) asm volatile("s_waitcnt vmcnt(" #N ")" : "+v"(A_(c, 0)), "+v"(A_(c, 1)), "+v"(A_(c, 2)), "+v"(A_(c, 3)) :: "memory")
#define RD(F, j, r) F[r] = trd(w1p + (j) * 10240 + 32 * ((r) >> 1) + 2560 * ((r) & 1))
#define MF(F, j, k) acc[(k) & 1][(k) >> 1] = mfma32(FR(F, (k) >> 1), xf[10 * ((k) & 1) + (j)], acc[(k) & 1][(k) >> 1])
#define STEPA(FA, FB, c, V) do { _Pragma("unroll") for (int k = 0; k < 10; k++) { MF(FA, 2 * (c), k); RD(FB, 2 * (c) + 1, k); if ((c) < 4) TP((c) + 1, k); V(k); SB(); } } while (0)
#define STEPB(FA, FB, c) do { _Pragma("unroll") for (int k = 0; k < 4; k++) { MF(FB, 2 * (c) + 1, k); TP((c) + 1, 10 + k); SB(); } \
      if ((c) == 2) SIG_ADD(F_P1); \
      if ((c) == 3) SIG_WAIT2(F_W0 + kh, (c) + 2, F_WP, 1); else SIG_WAIT(F_W0 + kh, (c) + 2); \
      _Pragma("unroll") for (int k = 4; k < 10; k++) { if (k < 9) { RD(FA, 2 * (c) + 2, 2 * (k - 4)); RD(FA, 2 * (c) + 2, 2 * (k - 4) + 1); } MF(FB, 2 * (c) + 1, k); TP((c) + 1, 10 + k); SB(); } } while (0)
    {
      u32x2 fa[10], fb[10];
      XW(16, 0);
      u32 fil = l;
#pragma unroll
      for (int k = 0; k < 20; k++) { TP(0, k); asm volatile("v_add_u32 %0, 1, %0" : "+v"(fil)); SB(); }
      SIG_WAIT(F_W0 + kh, 1);
      STAMP(2);
#pragma unroll
      for (int r = 0; r < 10; r++) RD(fa, 0, r);
      SB();
#define V3(k) do { if ((k) == 1) XL1(3, 0); if ((k) == 3) XL1(3, 1); if ((k) == 5) XL1(3, 2); if ((k) == 7) XL1(3, 3); } while (0)
#define V4(k) do { if ((k) == 1) XL1(4, 0); if ((k) == 3) XL1(4, 1); if ((k) == 5) XL1(4, 2); if ((k) == 7) XL1(4, 3); } while (0)
#define VN(k) do { } while (0)
      XW(12, 1);
      STEPA(fa, fb, 0, VN); STEPB(fa, fb, 0);
      STAMP(3);
      XW(8, 2);
      STEPA(fa, fb, 1, VN); STEPB(fa, fb, 1);
      STAMP(4);
      XW(4, 3);
      STEPA(fa, fb, 2, VN); STEPB(fa, fb, 2);
      STAMP(5);
      XW(0, 4);
      STEPA(fa, fb, 3, VN); STEPB(fa, fb, 3);
      STAMP(6);
      STEPA(fa, fb, 4, VN);
#pragma unroll
      for (int k = 0; k < 10; k++) MF(fb, 9, (k + 1 - 2 * (k & 1)));
    }
    STAMP(7);
    {
    int l_ = __builtin_amdgcn_mbcnt_hi(~0u, __builtin_amdgcn_mbcnt_lo(~0u, 0u));
    asm volatile("" : "+v"(l_));
    const int l = l_, tid = w * 64 + l, g = l >> 4, i16 = l & 15, q4 = i16 >> 2, p4 = l & 3;
    {
      const int tp = tw ^ 1;
      *(LAS f32x4*)(sm + L_PHI + tp * 2560 + l * 16) = acc[1][0];
      *(LAS f32x4*)(sm + L_PHI + tp * 2560 + 1024 + l * 16) = acc[1][1];
      *(LAS f32x4*)(sm + L_V + tp * 1536 + l * 16) = acc[1][2];
      *(LAS f32x4*)(sm + L_EXA + tp * 2048 + l * 16) = acc[1][3];
      *(LAS f32x4*)(sm + L_EXA + tp * 2048 + 1024 + l * 16) = acc[1][4];
      SIG_SET(F_EX + tp, 1);
    }
    u32x4 wa32[3]; u32x2 wa16[3];
    {
      const int hp = p4 >> 1;
      const int d0 = 4 * g + q4, d1 = d0 + 16, d2 = d0 + 32;
      LAS char* a0 = sm + (hp == 0 ? L_WPHI + d0 * 48 + 24 * (p4 & 1) : L_ZERO);
      LAS char* a1 = sm + ((d1 >= 20) == (hp == 1) ? L_WPHI + d1 * 48 + 24 * (p4 & 1) : L_ZERO);
      LAS char* a2 = sm + ((hp == 1 && d2 < 40) ? L_WPHI + d2 * 48 + 24 * (p4 & 1) : L_ZERO);
#pragma unroll
      for (int mt = 0; mt < 3; mt++) {
        u32x2 e0 = trd(a0 + 8 * mt), e1 = trd(a1 + 8 * mt);
        wa32[mt] = u32x4{e0[0], e0[1], e1[0], e1[1]};
        wa16[mt] = trd(a2 + 8 * mt);
      }
    }
    SIG_WAIT2(F_EX + tw, 1, F_CS, 2);
    STAMP(8);
    const u32 msv = *(LAS u32*)(sm + L_MS + (tw * 16 + i16) * 4);
    const float m_b = bflo(msv), rs = rcp(bfhi(msv));
    f32x4 cc[5], a[5];
#pragma unroll
    for (int t = 0; t < 3; t++) cc[t] = *(LAS f32x4*)(sm + L_CSP + (16 * t + 4 * g) * 4);
    a[0] = acc[0][0] + *(LAS f32x4*)(sm + L_PHI + tw * 2560 + l * 16);
    a[1] = acc[0][1] + *(LAS f32x4*)(sm + L_PHI + tw * 2560 + 1024 + l * 16);
    a[2] = acc[0][2] + *(LAS f32x4*)(sm + L_V + tw * 1536 + l * 16);
    {
      u32 kn[5][2];
#define NRM(A, t, C, O) do { f32x4 v_ = (A - m_b * C) * rs; O[t][0] = pk2(v_[0], v_[1]); O[t][1] = pk2(v_[2], v_[3]); } while (0)
      NRM(a[0], 0, cc[0], kn); NRM(a[1], 1, cc[1], kn); NRM(a[2], 2, cc[2], kn);
      SB();
      cc[3] = *(LAS f32x4*)(sm + L_CSP + (48 + 4 * g) * 4); cc[4] = *(LAS f32x4*)(sm + L_CSP + (64 + 4 * g) * 4);
      a[3] = acc[0][3] + *(LAS f32x4*)(sm + L_EXA + tw * 2048 + l * 16);
      a[4] = acc[0][4] + *(LAS f32x4*)(sm + L_EXA + tw * 2048 + 1024 + l * 16);
      u32x4 b32 = {kn[0][0], kn[0][1], kn[1][0], kn[1][1]};
      u32x2 b16 = {kn[2][0], kn[2][1]};
      f32x4 pp[3];
#pragma unroll
      for (int mt = 0; mt < 3; mt++) {
        pp[mt] = mfma32(wa32[mt], b32, f32x4{0, 0, 0, 0});
        pp[mt] = mfma16(wa16[mt], b16, pp[mt]);
      }
      NRM(a[3], 3, cc[3], kn); NRM(a[4], 4, cc[4], kn);
      __builtin_amdgcn_wave_barrier();
      LAS char* vr = sm + L_V + (16 * tw + i16) * 96;
      u32x2 v2 = (g >= 2) ? u32x2{kn[2][0], kn[2][1]} : u32x2{g == 0 ? 0x3F80u : 0u, 0u};
      *(LAS u32x2*)(vr + (g >= 2 ? 8 * (g - 2) : 80 + 8 * g)) = v2;
      *(LAS u32x2*)(vr + 16 + 8 * g) = u32x2{kn[3][0], kn[3][1]};
      *(LAS u32x2*)(vr + 48 + 8 * g) = u32x2{kn[4][0], kn[4][1]};
      u32 ph[10];
      softmax_pack(pp[0], pp[1], pp[2], ph);
      LAS char* pr = sm + L_PHI + (16 * tw + i16) * 160 + 40 * g;
#pragma unroll
      for (int k = 0; k < 5; k++) *(LAS u32x2*)(pr + 8 * k) = u32x2{ph[2 * k], ph[2 * k + 1]};
    }
    STAMP(9);
    SIG_ADD(F_PHI);
    __builtin_amdgcn_s_setprio(2);
    {
      const int mt0 = (0x22111000 >> (4 * w)) & 3, nt0 = (0x10210210 >> (4 * w)) & 3;
      const int mt1 = (0x22144332 >> (4 * w)) & 7, nt1 = (0x10221212 >> (4 * w)) & 3;
      u32 ap0 = L_PHI + (4 * g + q4) * 160 + 8 * p4 + 32 * mt0, ap1 = ap0 + 32 * (mt1 - mt0);
      u32 bp0 = L_V + (4 * g + q4) * 96 + 8 * p4 + 32 * nt0, bp1 = bp0 + 32 * (nt1 - nt0);
#define KVA(mt, nt, en) ({ const int fq = 4 * (mt) + g, col = 16 * (nt) + i16; int c = col - (fq >= 10 ? 20 : 0); bool ok = (c >= 0 && c < 20); \
        if (col == 40) { c = 20; ok = true; } (ok && (en)) ? reg + R_KVP + (fq * 21 + c) * 16 : WS_END; })
      u32 ks0 = KVA(mt0, nt0, true), ks1 = KVA(mt1, nt1, w < 5);
      asm volatile("" : "+v"(ap0), "+v"(ap1), "+v"(bp0), "+v"(bp1), "+v"(ks0), "+v"(ks1));
      SIG_WAIT(F_PHI, 9);
      STAMP(10);
      f32x4 d0 = {0, 0, 0, 0}, d1 = {0, 0, 0, 0};
#pragma unroll
      for (int u = 0; u < 4; u++) {
        d0 = mfma32(trd2(sm + ap0 + u * 5120, 2560), trd2(sm + bp0 + u * 3072, 1536), d0);
        d1 = mfma32(trd2(sm + ap1 + u * 5120, 2560), trd2(sm + bp1 + u * 3072, 1536), d1);
      }
      ST4(san4(__builtin_bit_cast(u32x4, d0)), ks0); ST4(san4(__builtin_bit_cast(u32x4, d1)), ks1);
    }
    SIG_ADD(F_KR);
    SIG_WAIT(F_Q0 + kh, 1);
    STAMP(11);
    f32x4 aq[2][3];
#pragma unroll
    for (int t = 0; t < 3; t++) { aq[0][t] = f32x4{0, 0, 0, 0}; aq[1][t] = f32x4{0, 0, 0, 0}; }
    {
      LAS char* wqp = sm + L_WQ + kh * 3072 + (4 * g + q4) * 96 + 8 * p4;
      u32x2 fa[6], fb[6];
#define RQ(F, P_, o, r) F[r] = trd(P_ + (o) + 32 * ((r) >> 1) + 1536 * ((r) & 1))
#define STEPQ(FA, FB, j) do { _Pragma("unroll") for (int k = 0; k < 6; k++) { aq[k & 1][k >> 1] = mfma32(FR(FA, k >> 1), xf[10 * (k & 1) + (j)], aq[k & 1][k >> 1]); \
          if ((j) < 9) RQ(FB, wqp, ((j) + 1) * 6144, k); SB(); } } while (0)
#pragma unroll
      for (int r = 0; r < 6; r++) RQ(fa, wqp, 0, r);
      SB();
#pragma unroll
      for (int j = 0; j < 10; j += 2) { STEPQ(fa, fb, j); STEPQ(fb, fa, j + 1);
      }
    }
#pragma unroll
    for (int t = 0; t < 3; t++) *(LAS f32x4*)(sm + L_EXQ + (tw ^ 1) * 3072 + t * 1024 + l * 16) = aq[1][t];
    SIG_SET(F_EX + (tw ^ 1), 2);
    SIG_ADD(F_P2);
    STAMP(12);
    SIG_WAIT2(F_EX + tw, 2, F_CSQ, 2);
    STAMP(13);
    const u32 slo0 = (rt * 128 + tw * 16 + l / 5) * 1280 + og * 80 + (l % 5) * 16;
    const u32 slo1 = (rt * 128 + tw * 16 + (64 + i16) / 5) * 1280 + og * 80 + ((64 + i16) % 5) * 16;
    SIG_WAIT(F_KR, 8);
    glds16(x, slo0, ldsb + L_V + tw * 1536);
    if (l < 16) glds16(x, slo1, ldsb + L_V + tw * 1536 + 1024);
    u32 pq[10];
    {
      f32x4 aqo[3], c2[3];
      u32 qn[3][2];
#pragma unroll
      for (int t = 0; t < 3; t++) {
        c2[t] = *(LAS f32x4*)(sm + L_CSQ + (16 * t + 4 * g) * 4) + *(LAS f32x4*)(sm + L_CSQ + (48 + 16 * t + 4 * g) * 4);
        aqo[t] = aq[0][t] + *(LAS f32x4*)(sm + L_EXQ + tw * 3072 + t * 1024 + l * 16);
        NRM(aqo[t], t, c2[t], qn);
      }
      u32x4 b32 = {qn[0][0], qn[0][1], qn[1][0], qn[1][1]};
      u32x2 b16 = {qn[2][0], qn[2][1]};
      f32x4 pp[3];
#pragma unroll
      for (int mt = 0; mt < 3; mt++) {
        pp[mt] = mfma32(wa32[mt], b32, f32x4{0, 0, 0, 0});
        pp[mt] = mfma16(wa16[mt], b16, pp[mt]);
      }
      softmax_pack(pp[0], pp[1], pp[2], pq);
#pragma unroll
      for (int k = 0; k < 10; k++) asm volatile("" : "+v"(pq[k]));
    }
    STAMP(14);
    u32 oad[3];
#pragma unroll
    for (int mt = 0; mt < 3; mt++) {
      const int col = mt < 2 ? 8 * q4 + 4 * mt + p4 : 32 + 2 * q4 + p4;
      const int hp = (mt == 2) ? (p4 == 2 ? 0 : 1) : (col >= 20 ? 1 : 0);
      const int row = (mt == 2 && p4 >= 2) ? hp * 21 + 20 : col + hp;
      oad[mt] = ((g >> 1) == hp) ? (u32)(L_KVT + row * 80 + 40 * (g & 1)) : L_ZERO;
      asm volatile("" : "+v"(oad[mt]));
    }
    STAMP(15);
    SIG_WAIT(F_KVT, 1);
    STAMP(16);
    {
      f32x4 oo[3];
#pragma unroll
      for (int mt = 0; mt < 3; mt++) {
        LAS char* ap = sm + oad[mt];
        u32x2 a0 = *(LAS u32x2*)(ap), a1 = *(LAS u32x2*)(ap + 8), a2 = *(LAS u32x2*)(ap + 16), a3 = *(LAS u32x2*)(ap + 24), a4 = *(LAS u32x2*)(ap + 32);
        oo[mt] = mfma32(u32x4{a0[0], a0[1], a1[0], a1[1]}, u32x4{pq[0], pq[1], pq[2], pq[3]}, f32x4{0, 0, 0, 0});
        oo[mt] = mfma32(u32x4{a2[0], a2[1], a3[0], a3[1]}, u32x4{pq[4], pq[5], pq[6], pq[7]}, oo[mt]);
        oo[mt] = mfma16(a4, u32x2{pq[8], pq[9]}, oo[mt]);
      }
      const float r0 = rcp(oo[2][2] + 1e-6f), r1 = rcp(oo[2][3] + 1e-6f);
      const float ra_ = g <= 2 ? r0 : r1, rb_ = g <= 1 ? r0 : r1;
      u32x4 o8 = {pk2(oo[0][0] * ra_, oo[0][1] * ra_), pk2(oo[0][2] * ra_, oo[0][3] * ra_), pk2(oo[1][0] * rb_, oo[1][1] * rb_), pk2(oo[1][2] * rb_, oo[1][3] * rb_)};
      u32 o2 = san(pk2(oo[2][0] * r1, oo[2][1] * r1));
      const u32 ob = reg + R_OB + tw * 1280 + l * 16;
      ST4(san4(o8), ob);
      auto ra = __builtin_amdgcn_permlane32_swap(o2, o2, false, false);
      auto rb = __builtin_amdgcn_permlane16_swap(ra[0], ra[0], false, false);
      auto rc2 = __builtin_amdgcn_permlane16_swap(ra[1], ra[1], false, false);
      if (g == 0) ST4((u32x4{rb[0], rb[1], rc2[0], rc2[1]}), ob + 1024);
      SIG_ADD(F_OS);
      asm volatile("s_waitcnt vmcnt(2)" ::: "memory");
    }
    MEMFENCE();
    STAMP(17);
    u32x2 xv[3], xnp[3];
    {
      LAS char* xp = sm + L_V + tw * 1536 + i16 * 80 + 8 * g;
#pragma unroll
      for (int t = 0; t < 3; t++) {
        xv[t] = *(LAS u32x2*)(xp + 32 * t);
        u32 d0 = pk2(bflo(xv[t][0]) - m_b, bfhi(xv[t][0]) - m_b), d1 = pk2(bflo(xv[t][1]) - m_b, bfhi(xv[t][1]) - m_b);
        xnp[t] = u32x2{pk2(bflo(d0) * rs, bfhi(d0) * rs), pk2(bflo(d1) * rs, bfhi(d1) * rs)};
        asm volatile("" : "+v"(xnp[t]));
      }
    }
    const u32 obase = WS_REG + (rt * 16 + 8 * kh) * RS + R_OB + tg * 2560;
    u32 oa[5];
#pragma unroll
    for (int k = 0; k < 5; k++) { int C = 64 * k + l, pr = C / 80; oa[k] = obase + pr * (RS - 1280) + C * 16; asm volatile("" : "+v"(oa[k])); }
    f32x4 ao[2][3];
    int idx;
    {
      LAS char* wop = sm + (kh ? L_WO1 : L_WO0) + (4 * g + q4) * 96 + 8 * p4;
      SIG_WAIT2(F_WO, 2, F_IDX, 1);
      idx = __builtin_amdgcn_readfirstlane((int)*(volatile LAS u32*)(sm + L_SYN + F_IDX * 4) - 1);
      SIG_WAIT(kh ? F_O1 : F_O, 1);
      STAMP(19);
#pragma unroll 1
      for (u32 spin = 0; spin < SPIN_CAP; spin++) {
        MEMFENCE();
        u32x4 of[2][10];
#pragma unroll
        for (int j = 0; j < 10; j++) {
          of[0][j] = __builtin_amdgcn_raw_buffer_load_b128(wsr, oa[j % 5], (j / 5) * 4 * RS + kh * 1280, 16);
          of[1][j] = __builtin_amdgcn_raw_buffer_load_b128(wsr, oa[j % 5], (j / 5) * 4 * RS + (1 - kh) * 1280, 16);
        }
#pragma unroll
        for (int t = 0; t < 3; t++) { ao[0][t] = f32x4{0, 0, 0, 0}; ao[1][t] = f32x4{0, 0, 0, 0}; }
        u32 m = 0;
        u32x2 fa[6], fb[6];
#define STEPO(FA, FB, j) do { _Pragma("unroll") for (int k = 0; k < 6; k++) { ao[k & 1][k >> 1] = mfma32(FR(FA, k >> 1), of[k & 1][j], ao[k & 1][k >> 1]); \
            if ((j) < 9) RQ(FB, wop, ((j) + 1) * 3072, k); if (k == 1) m = umax_(m, max4(of[0][j])); if (k == 3) m = umax_(m, max4(of[1][j])); SB(); } } while (0)
#pragma unroll
        for (int r = 0; r < 6; r++) RQ(fa, wop, 0, r);
        SB();
#pragma unroll
        for (int j = 0; j < 10; j += 2) { STEPO(fa, fb, j); STEPO(fb, fa, j + 1); }
        if (__all(m != SENT)) break;
      }
    }
#pragma unroll
    for (int t = 0; t < 3; t++) *(LAS f32x4*)(sm + L_EXQ + (tw ^ 1) * 3072 + t * 1024 + l * 16) = ao[1][t];
    SIG_SET(F_EX + (tw ^ 1), 3);
    STAMP(20);
    SIG_WAIT(F_EX + tw, 3);
    STAMP(21);
    {
      LAS char* xp = sm + L_V + tw * 1536 + i16 * 80 + 8 * g;
      if ((idx >> 4) == rt * 8 + tw) SIG_WAIT(F_Y, 1);
      if (idx == rt * 128 + tw * 16 + i16) {
#pragma unroll
        for (int t = 0; t < 3; t++) if (t < 2 || g < 2) {
          f32x4 yy = *(LAS f32x4*)(sm + L_Y + (16 * t + 4 * g) * 4);
          xnp[t] = u32x2{pk2(bflo(xnp[t][0]) + yy[0], bfhi(xnp[t][0]) + yy[1]), pk2(bflo(xnp[t][1]) + yy[2], bfhi(xnp[t][1]) + yy[3])};
        }
      }
#pragma unroll
      for (int t = 0; t < 3; t++) {
        f32x4 af = ao[0][t] + *(LAS f32x4*)(sm + L_EXQ + tw * 3072 + t * 1024 + l * 16);
        u32 p0 = pk2(af[0], af[1]), p1 = pk2(af[2], af[3]);
        u32 q0 = pk2(bflo(xv[t][0]) + bflo(p0), bfhi(xv[t][0]) + bfhi(p0)), q1 = pk2(bflo(xv[t][1]) + bflo(p1), bfhi(xv[t][1]) + bfhi(p1));
        u32x2 res = {pk2(bflo(q0) + bflo(xnp[t][0]), bfhi(q0) + bfhi(xnp[t][0])), pk2(bflo(q1) + bflo(xnp[t][1]), bfhi(q1) + bfhi(xnp[t][1]))};
        if (t < 2 || g < 2) *(LAS u32x2*)(xp + 32 * t) = res;
      }
      __builtin_amdgcn_wave_barrier();
      LAS char* op = sm + L_V + tw * 1536 + l * 16;
      *(u32x4*)((char*)out + slo0) = *(LAS u32x4*)(op);
      if (l < 16) *(u32x4*)((char*)out + slo1) = *(LAS u32x4*)(op + 1024);
    }
    STAMP(22);
    }
  }
}

extern "C" void kernel_launch(void* const* d_in, const int* in_sizes, int n_in, void* d_out, int out_size,
                              void* d_ws, size_t ws_size, hipStream_t stream) {
  fused_kernel<<<256, 768, 0, stream>>>((const u16*)d_in[0], (const u16*)d_in[1], (const u16*)d_in[2], (const u16*)d_in[3],
                                        (const u16*)d_in[5], (const u16*)d_in[6], (const u16*)d_in[7], (const u16*)d_in[8],
                                        (u16*)d_out, (char*)d_ws);
}
```

```cpp
#include <hip/hip_runtime.h>
#include <stdint.h>

typedef unsigned short u16;
typedef unsigned int u32;
typedef unsigned long long u64;
typedef __attribute__((ext_vector_type(8))) __bf16 bf16x8;
typedef __attribute__((ext_vector_type(4))) short s16x4;
typedef __attribute__((ext_vector_type(4))) float f32x4;
typedef __attribute__((ext_vector_type(2))) float f32x2;
typedef __attribute__((ext_vector_type(2))) __bf16 bf16x2;
typedef __attribute__((ext_vector_type(4))) u32 u32x4;
typedef __attribute__((ext_vector_type(2))) u32 u32x2;
#define LAS __attribute__((address_space(3)))
#define DI __device__ __forceinline__

#define FS 8
constexpr u32 WS_EPOCH = 0;
constexpr u32 WS_REC = 8192;
constexpr u32 WS_ST = 16384;
constexpr u32 WS_REG = 49152;
constexpr u32 RS = 23936;
constexpr u32 R_OB = 0;
constexpr u32 R_KVP = 10240;
constexpr u32 R_YP = 17024;
constexpr u32 R_CS = 19584;
constexpr u32 R_KVR = 20608;
constexpr u32 R_I1 = 20864;
constexpr u32 R_I3 = 21760;
constexpr u32 R_IG = 21120;
constexpr u32 R_IY = 21760;
constexpr u32 WS_YB = WS_REG + 256 * RS;
constexpr u32 WS_MG = WS_YB + 327680;
constexpr u32 MGS = 1024 + 40 * 128;
constexpr u32 WS_IY = WS_MG + 128 * MGS;
constexpr u32 WS_END = WS_IY + 2048 * 128;
constexpr u32 WS_STAMP = 128u << 20;

constexpr u32 L_W1 = 0;
constexpr u32 L_WQ = 0;
constexpr u32 L_WO0 = 30720;
constexpr u32 L_WO1 = 92160;
constexpr u32 L_EXQ = 61440;
constexpr u32 L_CSP = 102400;
constexpr u32 L_EXA = 103424;
constexpr u32 L_PF = 162816;
constexpr u32 L_PHI = 122880;
constexpr u32 L_V = 143360;
constexpr u32 L_SM = 155648;
constexpr u32 L_WPHI = L_SM;
constexpr u32 L_ZERO = L_SM + 1920;
constexpr u32 L_KVT = L_SM + 1984;
constexpr u32 L_CSQ = L_KVT + 3360;
constexpr u32 L_KEY = L_CSQ + 768;
constexpr u32 L_IDX = L_KEY + 64;
constexpr u32 L_Y = L_IDX + 64;
constexpr u32 L_MS = L_Y + 160;
constexpr u32 L_SYN = L_MS + 512;
enum { F_W0 = 0, F_W1, F_ST, F_CS, F_P1, F_Q0, F_Q1, F_PHI, F_KV, F_CSQ, F_P2, F_KVT, F_WO, F_OS, F_O, F_Y, F_EX = 16, F_WP = 24, F_KR, F_IDX, F_YG, F_YP, F_O1 };
constexpr u32 L_YS = L_PHI;
constexpr u32 L_TOTAL = 163840;

constexpr u32 SENT = 0xFFFFFFFFu;
constexpr u32 SPIN_CAP = 1u << 15;
#define HOP1_AT 6

DI u32 pk2(float a, float b) { f32x2 v = {a, b}; return __builtin_bit_cast(u32, __builtin_convertvector(v, bf16x2)); }
DI float bflo(u32 u) { return __uint_as_float(u << 16); }
DI float bfhi(u32 u) { return __uint_as_float(u & 0xFFFF0000u); }
DI float rbf(float a) { return bflo(pk2(a, 0.f)); }
DI float rcp(float a) { return __builtin_amdgcn_rcpf(a); }
DI float dot2(u32 a, u32 b, float c) { return __builtin_amdgcn_fdot2_f32_bf16(__builtin_bit_cast(bf16x2, a), __builtin_bit_cast(bf16x2, b), c, false); }
DI f32x4 mfma32(u32x4 a, u32x4 b, f32x4 c) {
  return __builtin_amdgcn_mfma_f32_16x16x32_bf16(__builtin_bit_cast(bf16x8, a), __builtin_bit_cast(bf16x8, b), c, 0, 0, 0);
}
DI f32x4 mfma16(u32x2 a, u32x2 b, f32x4 c) {
  return mfma32(u32x4{a[0], a[1], 0u, 0u}, u32x4{b[0], b[1], 0u, 0u}, c);
}
DI u32x2 trd(LAS char* p) { return __builtin_bit_cast(u32x2, __builtin_amdgcn_ds_read_tr16_b64_v4i16((LAS s16x4*)p)); }
DI u32x4 trd2(LAS char* p, u32 d) { u32x2 a = trd(p), b = trd(p + d); return u32x4{a[0], a[1], b[0], b[1]}; }
DI void glds16(const void* sbase, u32 voff, u32 lds_dst) {
  asm volatile("s_mov_b32 m0, %2\n\ts_nop 0\n\tglobal_load_lds_dwordx4 %0, %1" :: "v"(voff), "s"(sbase), "s"(lds_dst) : "memory");
}
DI u32 umin_(u32 a, u32 b) { return a < b ? a : b; }
DI u32 umax_(u32 a, u32 b) { return a > b ? a : b; }
DI u32 san(u32 v) { return umin_(v, 0xFFFFFFFEu); }
DI u32x4 san4(u32x4 v) { return u32x4{san(v[0]), san(v[1]), san(v[2]), san(v[3])}; }
DI u32 max4(u32x4 v) { return umax_(umax_(v[0], v[1]), umax_(v[2], v[3])); }
DI float xchg16_max(float v) { auto r = __builtin_amdgcn_permlane16_swap(__float_as_uint(v), __float_as_uint(v), false, false); return fmaxf(__uint_as_float(r[0]), __uint_as_float(r[1])); }
DI float xchg16_min(float v) { auto r = __builtin_amdgcn_permlane16_swap(__float_as_uint(v), __float_as_uint(v), false, false); return fminf(__uint_as_float(r[0]), __uint_as_float(r[1])); }
DI float xchg16_add(float v) { auto r = __builtin_amdgcn_permlane16_swap(__float_as_uint(v), __float_as_uint(v), false, false); return __uint_as_float(r[0]) + __uint_as_float(r[1]); }
DI float xchg32_add(float v) { auto r = __builtin_amdgcn_permlane32_swap(__float_as_uint(v), __float_as_uint(v), false, false); return __uint_as_float(r[0]) + __uint_as_float(r[1]); }
template <int CTRL> DI float dpp(float v) { return __int_as_float(__builtin_amdgcn_update_dpp(0, __float_as_int(v), CTRL, 0xf, 0xf, true)); }
template <int CTRL> DI u32 dppu(u32 v) { return (u32)__builtin_amdgcn_update_dpp(0, (int)v, CTRL, 0xf, 0xf, true); }
DI float wave_sum(float v) {
  v += dpp<0xB1>(v); v += dpp<0x4E>(v); v += dpp<0x141>(v); v += dpp<0x140>(v);
  return xchg32_add(xchg16_add(v));
}
DI float row_sum(float v) { v += dpp<0xB1>(v); v += dpp<0x4E>(v); v += dpp<0x141>(v); return v + dpp<0x140>(v); }
DI u32 row_max(u32 v) { v = umax_(v, dppu<0xB1>(v)); v = umax_(v, dppu<0x4E>(v)); v = umax_(v, dppu<0x141>(v)); return umax_(v, dppu<0x140>(v)); }
DI u32x4 ld4w(const char* base, u32 off) { u32x4 v; asm volatile("global_load_dwordx4 %0, %1, %2 sc1\n\ts_waitcnt vmcnt(0)" : "=&v"(v) : "v"(off), "s"(base) : "memory"); return v; }
DI u32x2 ld2w(const char* base, u32 off) { u32x2 v; asm volatile("global_load_dwordx2 %0, %1, %2 sc1\n\ts_waitcnt vmcnt(0)" : "=&v"(v) : "v"(off), "s"(base) : "memory"); return v; }
#define MEMFENCE() asm volatile("" ::: "memory")
#define BARRIER() asm volatile("s_waitcnt lgkmcnt(0)\n\ts_barrier" ::: "memory")
#define SB() __builtin_amdgcn_sched_barrier(0)
#define LD4(off) __builtin_amdgcn_raw_buffer_load_b128(wsr, (off), 0, 16)
#define LD2(off) __builtin_amdgcn_raw_buffer_load_b64(wsr, (off), 0, 16)
#define ST4(v, off) __builtin_amdgcn_raw_buffer_store_b128((v), wsr, (off), 0, 16)
#define ST2(v, off) __builtin_amdgcn_raw_buffer_store_b64((v), wsr, (off), 0, 16)

#define STAMP(k) do { } while (0)

DI void softmax_pack(const f32x4& p0, const f32x4& p1, const f32x4& p2, u32 (&o)[10]) {
  float p[10] = {p0[0], p0[1], p0[2], p0[3], p1[0], p1[1], p1[2], p1[3], p2[0], p2[1]};
  float mx = p[0], mn = p[0];
#pragma unroll
  for (int i = 1; i < 10; i++) { mx = fmaxf(mx, p[i]); mn = fminf(mn, p[i]); }
  mx = xchg16_max(mx); mn = xchg16_min(mn);
  const float L2E = 1.4426950408889634f;
  const float a = -mx * L2E, b = mn * L2E;
  float e1[10], e2[10], s1 = 0.f, s2 = 0.f;
#pragma unroll
  for (int i = 0; i < 10; i++) {
    e1[i] = __builtin_amdgcn_exp2f(fmaf(p[i], L2E, a));
    e2[i] = __builtin_amdgcn_exp2f(fmaf(p[i], -L2E, b));
    s1 += e1[i]; s2 += e2[i];
  }
  s1 = xchg16_add(s1); s2 = xchg16_add(s2);
  const float r1 = rcp(s1), r2 = rcp(s2);
#pragma unroll
  for (int i = 0; i < 5; i++) {
    o[i] = pk2(e1[2 * i] * r1, e1[2 * i + 1] * r1);
    o[5 + i] = pk2(e2[2 * i] * r2, e2[2 * i + 1] * r2);
  }
}

#define THR "3"

#define SIG_SET(f, v) do { MEMFENCE(); *(volatile LAS u32*)(sm + L_SYN + (f) * 4) = (v); MEMFENCE(); } while (0)
DI void sig_add(u32 addr) {
  u64 keep; u32 one = 1;
  asm volatile("s_mov_b64 %0, exec\n\ts_mov_b64 exec, 1\n\tds_add_u32 %1, %2\n\ts_mov_b64 exec, %0" : "=&s"(keep) : "v"(addr), "v"(one) : "memory");
}
#define SIG_ADD(f) sig_add(ldsb + L_SYN + (f) * 4)
DI void sig_wait(u32 addr, u32 val) {
  u32 tv, ts, cnt;
  asm volatile(
      "s_mov_b32 %2, 0x400000\n"
      "LW%=:\n\t"
      "ds_read_b32 %0, %3\n\t"
      "s_waitcnt lgkmcnt(0)\n\t"
      "v_readfirstlane_b32 %1, %0\n\t"
      "s_nop 1\n\t"
      "s_cmp_ge_u32 %1, %4\n\t"
      "s_cbranch_scc1 LD%=\n\t"
      "s_sleep 1\n\t"
      "s_sub_u32 %2, %2, 1\n\t"
      "s_cmp_lg_u32 %2, 0\n\t"
      "s_cbranch_scc1 LW%=\n"
      "LD%=:"
      : "=&v"(tv), "=&s"(ts), "=&s"(cnt) : "v"(addr), "s"(val) : "memory", "scc");
}
DI void sig_wait2(u32 a0, u32 v0, u32 a1, u32 v1) {
  u32 t0, t1, s0, s1, cnt;
  asm volatile(
      "s_mov_b32 %4, 0x400000\n"
      "LW%=:\n\t"
      "ds_read_b32 %0, %5\n\t"
      "ds_read_b32 %1, %7\n\t"
      "s_waitcnt lgkmcnt(0)\n\t"
      "v_readfirstlane_b32 %2, %0\n\t"
      "v_readfirstlane_b32 %3, %1\n\t"
      "s_nop 1\n\t"
      "s_cmp_ge_u32 %2, %6\n\t"
      "s_cbranch_scc0 LS%=\n\t"
      "s_cmp_ge_u32 %3, %8\n\t"
      "s_cbranch_scc1 LD%=\n"
      "LS%=:\n\t"
      "s_sleep 1\n\t"
      "s_sub_u32 %4, %4, 1\n\t"
      "s_cmp_lg_u32 %4, 0\n\t"
      "s_cbranch_scc1 LW%=\n"
      "LD%=:"
      : "=&v"(t0), "=&v"(t1), "=&s"(s0), "=&s"(s1), "=&s"(cnt) : "v"(a0), "s"(v0), "v"(a1), "s"(v1) : "memory", "scc");
}
#define SIG_WAIT2(f0, v0, f1, v1) sig_wait2(ldsb + L_SYN + (f0) * 4, (v0), ldsb + L_SYN + (f1) * 4, (v1))
#define SIG_WAIT(f, v) sig_wait(ldsb + L_SYN + (f) * 4, (v))
#define WPRIO 0
#define CLR_AT 2
#define PF_AT 0
#define KVD_AT 2
#define WO_FIRST 0
#define GW_AT 4
#define GTHR "40"
#define REC_AT 3
#define ST_AT 2
#define GW_SIG F_W0
#define H1D 8
#define H1S 4
#define PFLATE 0
#define WTHR "40"
#define WORD 1
#define M3_SIG F_P2
#define M3_AT 10
#define H2S 0
#define OGX 1
#define MPRIO 3
#define M2_SIG F_KR
#define M2_AT 8
#define QW4 5
#define MPF 3
#define RAWBAR() asm volatile("s_barrier" ::: "memory")
__global__ __launch_bounds__(768, 3) void fused_kernel(
    const u16* __restrict__ x, const u16* __restrict__ wqkv, const u16* __restrict__ wphi, const u16* __restrict__ wo,
    const u16* __restrict__ wr, const u16* __restrict__ wg, const u16* __restrict__ wu, const u16* __restrict__ wd,
    u16* __restrict__ out, char* __restrict__ ws) {
  __shared__ __attribute__((aligned(1024))) char smem_[L_TOTAL];
  LAS char* sm = (LAS char*)smem_;
  const u32 ldsb = (u32)(size_t)smem_;
  STAMP(0);
  const int tid = threadIdx.x, l = tid & 63;
  const int w = __builtin_amdgcn_readfirstlane(tid >> 6);
  const int g = l >> 4, i16 = l & 15, q4 = i16 >> 2, p4 = l & 3;
  const int b = blockIdx.x, xcd = b & 7, slot = b >> 3;
  const int rt = (xcd >> 1) * 4 + (slot >> 3), hg = (xcd & 1) * 8 + (slot & 7);
  const int wgi = rt * 16 + hg;
  const bool fast = (0xA5 >> (hg & 7)) & 1;
  const int og = hg ^ OGX;
  const int rot = (hg + 2 * rt) % 5;
  const auto wsr = __builtin_amdgcn_make_buffer_rsrc((void*)ws, 0, WS_END, 0x00020000);
  const u32 reg = WS_REG + wgi * RS;
#define CROT(c) ((c) + rot >= 5 ? (c) + rot - 5 : (c) + rot)
#define FR(F, t) (u32x4{F[2 * (t)][0], F[2 * (t)][1], F[2 * (t) + 1][0], F[2 * (t) + 1][1]})
#define LOADEP() u32x2 ep; { const u32 z = 0; asm volatile("global_load_dwordx2 %0, %1, %2 sc1" : "=v"(ep) : "v"(z), "s"(ws) : "memory"); }
#define GETTAG() u32 tag_lo, tag_hi; { u64 e = (((u64)ep[1] << 32) | ep[0]) + 1; tag_lo = __builtin_amdgcn_readfirstlane((u32)e); tag_hi = __builtin_amdgcn_readfirstlane((u32)(e >> 32)); }

  if (tid < 32) *(LAS u32*)(sm + L_SYN + tid * 4) = 0u;
  if (w >= 8) {
    const int h = w - 8;
    __builtin_amdgcn_s_setprio(3);
    int l_ = __builtin_amdgcn_mbcnt_hi(~0u, __builtin_amdgcn_mbcnt_lo(~0u, 0u));
    asm volatile("" : "+v"(l_));
    const int l = l_, tid = w * 64 + l, g = l >> 4, i16 = l & 15, q4 = i16 >> 2, p4 = l & 3;
    const u32x4 ONES = {0x3F803F80u, 0x3F803F80u, 0x3F803F80u, 0x3F803F80u};
    if (h < 2) {
      u32 woff[5];
#pragma unroll
      for (int k = 0; k < 5; k++) {
        int P = 64 * k + l, rho = P / 10, s = P - 10 * rho;
        woff[k] = (32 * ((rho >> 2) & 1) + 16 * ((rho >> 3) & 1) + 4 * ((rho >> 4) & 1) + (rho & 3)) * 3840 + 1280 + (s >= 5 ? 1200 : 0) + hg * 80 + s * 16;
      }
      const char* wbase = (const char*)wqkv + h * (320 * 3840);
#define WL(c) do { const char* b_ = wbase + CROT(c) * (64 * 3840); _Pragma("unroll") for (int k = 0; k < 10; k++) \
        glds16(b_ + (k / 5) * (8 * 3840), woff[k % 5], ldsb + L_W1 + (4 * (c) + 2 * (k / 5) + h) * 5120 + (k % 5) * 1024); } while (0)
#define WDONE2(N, c) do { asm volatile("s_waitcnt vmcnt(%0)" :: "i"(N) : "memory"); SIG_SET(F_W0 + h, (c) + 1); } while (0)
#define WDONE(N, c) do { asm volatile("s_waitcnt vmcnt(" #N ")" ::: "memory"); SIG_SET(F_W0 + h, (c) + 1); } while (0)
      WL(0);
      BARRIER();
      WDONE(0, 0);
      WL(1);
      STAMP(1);
      WDONE(0, 1);
      __builtin_amdgcn_s_setprio(0);
      WL(2);
      __builtin_amdgcn_s_setprio(3);
      STAMP(2);
      WL(3);
      STAMP(3);
      WDONE(10, 2); WL(4);
      STAMP(4);
      u32 qoff[3], ooff[3], po[7];
#pragma unroll
      for (int k = 0; k < 3; k++) {
        int P = 64 * k + l, rho = P / 6, s = P - 6 * rho; if (s > 4) s = 4;
        qoff[k] = (32 * ((rho >> 2) & 1) + 16 * ((rho >> 3) & 1) + 4 * ((rho >> 4) & 1) + (rho & 3)) * 3840 + hg * 80 + s * 16;
        ooff[k] = (8 * ((rho >> 2) & 3) + 4 * ((rho >> 4) & 1) + (rho & 3)) * 1280 + og * 80 + s * 16;
      }
#pragma unroll
      for (int k = 0; k < 7; k++) { const int qq = slot * 100 + h * 50 + (l >> 3) + (k < 6 ? 8 * k : 42), row = qq / 5, cc_ = qq - 5 * row; po[k] = row * 1280 + (xcd & 1) * 640 + cc_ * 128 + (l & 7) * 16; }
      WDONE(10, 3);
      u32x4 wr_[30];
#define OROT(k) (2 * CROT((k) / 6) + ((k) / 3 & 1))
#pragma unroll
      for (int k = 0; k < 30; k++) {
        asm volatile("global_load_dwordx4 %0, %1, %2" : "=v"(wr_[k]) : "v"(qoff[k % 3]), "s"(wbase + CROT(k / 6) * (64 * 3840) + ((k / 3) & 1) * (8 * 3840)));
        if (k == QW4) { WDONE2(QW4 + 1, 4); STAMP(5); }
      }
#pragma unroll
      for (int k = 0; k < 7; k++) glds16(wo, po[k], ldsb + L_PF);
      STAMP(6);
      SIG_WAIT(F_P1, 9);
      asm volatile("s_waitcnt vmcnt(7)" ::: "memory");
      STAMP(7);
#pragma unroll
      for (int k = 0; k < 30; k++) {
        asm volatile("" : "+v"(wr_[k]));
        *(LAS u32x4*)(sm + L_WQ + (2 * (k / 3) + h) * 3072 + (k % 3) * 1024 + l * 16) = wr_[k];
      }
      SIG_SET(F_Q0 + h, 1);
      STAMP(8);
      __builtin_amdgcn_s_setprio(0);
#pragma unroll
      for (int k = 0; k < WO_FIRST; k++)
        asm volatile("global_load_dwordx4 %0, %1, %2" : "=v"(wr_[k]) : "v"(ooff[k % 3]), "s"((const char*)wo + (10 * h + OROT(k)) * 40960));
      {
        f32x4 cq[3];
#pragma unroll
        for (int t = 0; t < 3; t++) cq[t] = f32x4{0, 0, 0, 0};
        LAS char* wqp = sm + L_WQ + h * 3072 + (4 * g + q4) * 96 + 8 * p4;
#pragma unroll 1
        for (int k = 0; k < 10; k++)
#pragma unroll
          for (int t = 0; t < 3; t++) cq[t] = mfma32(ONES, trd2(wqp + k * 6144 + 32 * t, 1536), cq[t]);
#pragma unroll
        for (int t = 0; t < 3; t++) *(LAS float*)(sm + L_CSQ + (48 * h + 16 * t + i16) * 4) = cq[t][0];
        SIG_ADD(F_CSQ);
        SIG_ADD(F_P2);
      }
#pragma unroll
      for (int k = WO_FIRST; k < 30; k++)
        asm volatile("global_load_dwordx4 %0, %1, %2\n\ts_waitcnt vmcnt(" WTHR ")" : "=v"(wr_[k]) : "v"(ooff[k % 3]), "s"((const char*)wo + (10 * h + OROT(k)) * 40960));
      STAMP(9);
      SIG_WAIT(h ? F_KR : F_P2, h ? 8 : 10);
      asm volatile("s_waitcnt vmcnt(0)" ::: "memory");
      STAMP(10);
      __builtin_amdgcn_s_setprio(0);
#pragma unroll
      for (int k = 0; k < 30; k++) {
        asm volatile("" : "+v"(wr_[k]));
        *(LAS u32x4*)(sm + (h ? L_WO1 : L_WO0) + OROT(k) * 3072 + (k % 3) * 1024 + l * 16) = wr_[k];
      }
      SIG_ADD(F_WO);
      STAMP(12);
    } else {
      const int e = h - 2;
#define STATS_LD() u32x4 sx[5], sw[5]; { const u32 o = (4 * e + g) * 1280 + i16 * 16, o2 = i16 * 16; \
          _Pragma("unroll") for (int k = 0; k < 5; k++) { asm volatile("global_load_dwordx4 %0, %1, %2 offset:%3" : "=v"(sx[k]) : "v"(o), "s"(x + wgi * 8 * 640), "i"(k * 256)); \
            asm volatile("global_load_dwordx4 %0, %1, %2 offset:%3" : "=v"(sw[k]) : "v"(o2), "s"(wr), "i"(k * 256)); } }
#define STATS() do { _Pragma("unroll") for (int k = 0; k < 5; k++) asm volatile("" : "+v"(sx[k]), "+v"(sw[k])); \
          float s = 0.f, q = 0.f; \
          _Pragma("unroll") for (int k = 0; k < 20; k++) { s = dot2(sx[k >> 2][k & 3], 0x3F803F80u, s); q = dot2(sx[k >> 2][k & 3], sx[k >> 2][k & 3], q); } \
          const float mean = row_sum(s) * (1.f / 640.f); \
          const float var = fmaxf(row_sum(q) * (1.f / 640.f) - mean * mean, 0.f); \
          const float mb = rbf(mean), sb = rbf(__builtin_amdgcn_sqrtf(rbf(rbf(var) + 1e-5f))), rsb = rcp(sb); \
          const u32 ms = pk2(mb, sb); const int srow = wgi * 8 + 4 * e + g; \
          if (i16 == 0) ST4((u32x4{ms, tag_lo, ms, tag_hi}), WS_ST + srow * 16); \
          float r = 0.f; \
          _Pragma("unroll") for (int k = 0; k < 20; k++) { const u32 xd = sx[k >> 2][k & 3]; \
            u32 d = pk2(bflo(xd) - mb, bfhi(xd) - mb), n = pk2(bflo(d) * rsb, bfhi(d) * rsb); r = dot2(n, sw[k >> 2][k & 3], r); } \
          r = row_sum(r); \
          const u32 rb = pk2(r, 0.f) & 0xFFFFu; \
          const u32 ord = (rb & 0x8000u) ? (~rb & 0xFFFFu) : (rb | 0x8000u); \
          if (i16 == 0) *(LAS u32x2*)(sm + L_KEY + (4 * e + g) * 8) = u32x2{(ord << 16) | (u32)(0x7FFF - srow), ms}; \
          SIG_ADD(F_ST); } while (0)
      if (e == 1) {
        LOADEP();
        STATS_LD();
        BARRIER();
        if (l < 14) ST4((u32x4{SENT, SENT, SENT, SENT}), reg + R_KVR + l * 16);
        asm volatile("s_waitcnt vmcnt(1)" : "+v"(ep) :: "memory");
        GETTAG();
        STAMP(1);
        STATS();
        {
          SIG_WAIT(F_ST, 2);
          u32x2 kk = *(LAS u32x2*)(sm + L_KEY + (l & 7) * 8);
          u32 km = row_max(kk[0]);
          if (l < 8 && kk[0] == km) ST4((u32x4{kk[0], tag_lo, kk[1], tag_hi}), WS_REC + b * 16);
        }
        STAMP(2);
        u32x4 s0 = {0, 0, 0, 0}, s1 = {0, 0, 0, 0};
        const u32 sta = WS_ST + (rt * 128 + l) * 16;
#define LDST() asm volatile("global_load_dwordx4 %0, %2, %3 sc1\n\tglobal_load_dwordx4 %1, %2, %3 offset:1024 sc1\n\ts_waitcnt vmcnt(0)" : "=&v"(s0), "=&v"(s1) : "v"(sta), "s"(ws) : "memory")
        {
          f32x4 cs[5];
#pragma unroll
          for (int t = 0; t < 5; t++) cs[t] = f32x4{0, 0, 0, 0};
          LAS char* w1p = sm + L_W1 + (4 * g + q4) * 160 + 8 * p4;
          SIG_WAIT2(F_W0, 1, F_W1, 1);
#pragma unroll
          for (int k = 0; k < 4; k++)
#pragma unroll
            for (int t = 0; t < 5; t++) cs[t] = mfma32(ONES, trd2(w1p + k * 5120 + 32 * t, 2560), cs[t]);
          SIG_ADD(F_P1);
#pragma unroll
          for (int t = 0; t < 5; t++) *(LAS float*)(sm + L_CSP + (16 * t + i16) * 4) = cs[t][0];
          __builtin_amdgcn_wave_barrier();
          const u32x2 pv = *(LAS u32x2*)(sm + L_CSP + (l < 40 ? l : 0) * 8);
          if (l < 40) ST4((u32x4{pv[0], tag_lo, pv[1], tag_hi}), reg + R_CS + l * 16);
        }
        STAMP(3);
        SIG_WAIT2(F_W0, CLR_AT + 1, F_W1, CLR_AT + 1);
#pragma unroll
        for (int k = 0; k < 7; k++) ST4((u32x4{SENT, SENT, SENT, SENT}), (k * 64 + l < 420) ? reg + R_KVP + (k * 64 + l) * 16 : WS_END);
        SIG_WAIT2(F_W0, ST_AT + 1, F_W1, ST_AT + 1);
        {
          u32x4 cp[5];
          const u32 ca = WS_REG + ((rt < 15 ? rt / 5 : 2) * 80 + hg) * RS + R_CS + (l < 40 ? l : 0) * 16;
#pragma unroll 1
          for (u32 spin = 0; spin < SPIN_CAP; spin++) {
            MEMFENCE();
#pragma unroll
            for (int k = 0; k < 5; k++) cp[k] = __builtin_amdgcn_raw_buffer_load_b128(wsr, ca, k * 16 * RS, 16);
            LDST();
            bool ok = s0[1] == tag_lo && s0[3] == tag_hi && s0[0] == s0[2] && s1[1] == tag_lo && s1[3] == tag_hi && s1[0] == s1[2];
#pragma unroll
            for (int k = 0; k < 5; k++) ok = ok && cp[k][1] == tag_lo && cp[k][3] == tag_hi;
            if (__all(ok)) break;
            __builtin_amdgcn_s_sleep(6);
          }
          float c0 = 0.f, c1 = 0.f;
#pragma unroll
          for (int k = 0; k < 5; k++) { c0 += __uint_as_float(cp[k][0]); c1 += __uint_as_float(cp[k][2]); }
          if (l < 40) *(LAS f32x2*)(sm + L_CSP + l * 8) = f32x2{c0, c1};
        }
        *(LAS u32*)(sm + L_MS + l * 4) = s0[0];
        *(LAS u32*)(sm + L_MS + 256 + l * 4) = s1[0];
        SIG_SET(F_CS, 2);
        STAMP(4);
        asm volatile("s_waitcnt vmcnt(0)" ::: "memory");
        SIG_ADD(F_PHI);
        if (l < 16) ST2((u32x2{tag_lo, tag_hi}), WS_REG + (l * 16 + hg) * RS + R_I1 + rt * 8);
        STAMP(11);
        SIG_WAIT(F_PHI, 2);
        STAMP(5);
#define HINT(addr) do { _Pragma("unroll 1") for (u32 spin = 0; spin < SPIN_CAP; spin++) { u32x4 f_ = ld4w(ws, (addr) + (l & 7) * 16); \
          if (__all(f_[0] == tag_lo && f_[1] == tag_hi && f_[2] == tag_lo && f_[3] == tag_hi)) break; __builtin_amdgcn_s_sleep(4); } } while (0)
        HINT(reg + R_I1);
        SIG_WAIT(F_KR, 8);
        __builtin_amdgcn_s_sleep(H1D);
        STAMP(6);
        {
          const int he = l >= 28 ? l - 28 : l;
          const bool ok = (l < 56) && (rt < 15);
          const u32 h1a = ok ? WS_REG + ((l >= 28 ? 128 : 0) + hg) * RS + R_KVP + (28 * rt + he) * 16 : WS_END;
          u32x4 h1[8];
#pragma unroll 1
          for (u32 spin = 0; spin < SPIN_CAP; spin++) {
            MEMFENCE();
            u32 m = 0;
#pragma unroll
            for (int k = 0; k < 8; k++) h1[k] = __builtin_amdgcn_raw_buffer_load_b128(wsr, h1a, k * 16 * RS, 16);
#pragma unroll
            for (int k = 0; k < 8; k++) m = umax_(m, max4(h1[k]));
            if (__all(m != SENT)) break;
            __builtin_amdgcn_s_sleep(H1S);
          }
          f32x4 s = __builtin_bit_cast(f32x4, h1[0]);
#pragma unroll
          for (int k = 1; k < 8; k++) s += __builtin_bit_cast(f32x4, h1[k]);
#pragma unroll
          for (int r = 0; r < 4; r++) s[r] += __shfl(s[r], (l + 28) & 63);
          if (ok && l < 28) ST2((u32x2{san(pk2(s[0], s[1])), san(pk2(s[2], s[3]))}), reg + R_KVR + l * 8);
        }
        STAMP(7);
        {
          const int hpr = l / 14, e2 = l - 14 * hpr;
          const u32 h2a = WS_REG + (hpr * 16 + hg) * RS + R_KVR + e2 * 16;
          u32 ka[8];
#pragma unroll
          for (int k = 0; k < 8; k++) { int it = 112 * (k >> 1) + 2 * l + (k & 1), hf = it / 21, c = it - 21 * hf; ka[k] = L_KVT + c * 80 + hf * 8 + (hf >= 10 ? 1600 : 0); }
          u32x4 kv[4];
#pragma unroll 1
          for (u32 spin = 0; spin < SPIN_CAP; spin++) {
            MEMFENCE();
            u32 m = 0;
#pragma unroll
            for (int k = 0; k < 4; k++) kv[k] = LD4((l < 56 && 4 * k + hpr < 15) ? h2a + k * 64 * RS : WS_END);
#pragma unroll
            for (int k = 0; k < 4; k++) m = umax_(m, max4(kv[k]));
            if (__all(m != SENT)) break;
            __builtin_amdgcn_s_sleep(H2S);
          }
#pragma unroll
          for (int k = 0; k < 4; k++) if (l < 56 && 4 * k + hpr < 15) {
            *(LAS u32x2*)(sm + ka[2 * k]) = u32x2{kv[k][0], kv[k][1]};
            *(LAS u32x2*)(sm + ka[2 * k + 1]) = u32x2{kv[k][2], kv[k][3]};
          }
        }
        SIG_SET(F_KVT, 1);
        STAMP(8);
        SIG_WAIT(F_OS, 8);
        if (l < 16) ST2((u32x2{tag_lo, tag_hi}), WS_REG + (rt * 16 + l) * RS + R_I3 + hg * FS);
        STAMP(9);
#pragma unroll 1
        for (u32 spin = 0; spin < SPIN_CAP; spin++) {
          u32x2 f_ = ld2w(ws, reg + R_I3 + i16 * FS);
          const u64 okm = __ballot(f_[0] == tag_lo && f_[1] == tag_hi);
          if ((okm & 0xFF) == 0xFF) SIG_SET(F_O, 1);
          if ((okm & 0xFF00) == 0xFF00) SIG_SET(F_O1, 1);
          if ((okm & 0xFFFF) == 0xFFFF) break;
        }
        SIG_SET(F_O, 1); SIG_SET(F_O1, 1);
        STAMP(10);
      } else {
        LOADEP();
        STATS_LD();
        if (fast) {
          const u32 o = ((slot >> 1) * 40 + (l >> 3)) * 3840 + (xcd & 1) * 640 + 256 + (l & 7) * 16;
#pragma unroll
          for (int k = 0; k < 10; k++) glds16((const char*)wqkv + (k % 5) * (8 * 3840) + 1280 * (1 + k / 5), o, ldsb + L_PF);
        }
        u32 wp[7];
#pragma unroll
        for (int k = 0; k < 7; k++) { const u32 o = hg * 1600 + (l + 64 * k < 400 ? l + 64 * k : 0) * 4; asm volatile("global_load_dword %0, %1, %2" : "=v"(wp[k]) : "v"(o), "s"(wphi)); }
        BARRIER();
        if (l < 4) *(LAS u32x4*)(sm + L_ZERO + l * 16) = u32x4{0, 0, 0, 0};
        if (l < 40) { *(LAS u32*)(sm + L_WPHI + l * 48 + 20) = 0u; *(LAS u32*)(sm + L_WPHI + l * 48 + 44) = 0u; }
        asm volatile("s_waitcnt vmcnt(0)" : "+v"(ep), "+v"(wp[0]), "+v"(wp[1]), "+v"(wp[2]), "+v"(wp[3]), "+v"(wp[4]), "+v"(wp[5]), "+v"(wp[6]) :: "memory");
        GETTAG();
        STATS();
#pragma unroll
        for (int k = 0; k < 7; k++) { const int u = l + 64 * k, row = u / 10, z = u - 10 * row; if (u < 400) *(LAS u32*)(sm + L_WPHI + row * 48 + z * 4 + (z >= 5 ? 4 : 0)) = wp[k]; }
        SIG_SET(F_WP, 1);
        STAMP(1);
        if (fast) {
          const u32 o = ((slot >> 1) * 40 + (l >> 3)) * 3840 + (xcd & 1) * 640 + 256 + (l & 7) * 16;
#pragma unroll
          for (int k = 0; k < 5; k++) glds16((const char*)wqkv + k * (8 * 3840), o, ldsb + L_PF);
        }
        const int fi = wgi >> 1, cb = fi < 120 ? fi / 10 : 0, ksp = fi < 120 ? fi - 10 * cb : 0;
        if (fast) {
          SIG_WAIT(F_W0, REC_AT);
          const u32x4 S4 = {SENT, SENT, SENT, SENT};
          ST4(S4, WS_MG + fi * MGS + l * 16);
        }
        else {
          SIG_WAIT(F_W0, 5);
#pragma unroll
          for (int zz = 0; zz < 3; zz++) { const int u = 64 * zz + l, sl = u / 10; ST4((u32x4{SENT, SENT, SENT, SENT}), u < 160 ? WS_YB + (sl * 128 + fi) * 160 + (u - 10 * sl) * 16 : WS_END); }
        }
        u32x2 rec;
        {
          u32 km = 0, ms = 0;
          u32x4 r_[4];
#pragma unroll 1
          for (u32 spin = 0; spin < SPIN_CAP; spin++) {
            const u32 ra = WS_REC + l * 16;
            asm volatile("global_load_dwordx4 %0, %4, %5 sc1\n\tglobal_load_dwordx4 %1, %4, %5 offset:1024 sc1\n\tglobal_load_dwordx4 %2, %4, %5 offset:2048 sc1\n\t"
                         "global_load_dwordx4 %3, %4, %5 offset:3072 sc1\n\ts_waitcnt vmcnt(0)"
                         : "=&v"(r_[0]), "=&v"(r_[1]), "=&v"(r_[2]), "=&v"(r_[3]) : "v"(ra), "s"(ws) : "memory");
            bool ok = true;
#pragma unroll
            for (int k = 0; k < 4; k++) ok = ok && r_[k][1] == tag_lo && r_[k][3] == tag_hi;
            if (__all(ok)) break;
            __builtin_amdgcn_s_sleep(16);
          }
#pragma unroll
          for (int k = 0; k < 4; k++) if (r_[k][0] > km) { km = r_[k][0]; ms = r_[k][2]; }
          u32 kx = row_max(km);
          { auto r = __builtin_amdgcn_permlane16_swap(kx, kx, false, false); kx = umax_(r[0], r[1]); }
          { auto r = __builtin_amdgcn_permlane32_swap(kx, kx, false, false); kx = umax_(r[0], r[1]); }
          const int src = __builtin_ctzll(__ballot(km == kx));
          rec = u32x2{(u32)__builtin_amdgcn_readfirstlane(kx), (u32)__builtin_amdgcn_readlane(ms, src)};
        }
        const int idx = (0x7FFF - (rec[0] & 0xFFFFu)) & 2047;
        if (b == 0 && l == 0) ST2((u32x2{tag_lo, tag_hi}), WS_EPOCH);
        SIG_SET(F_IDX, idx + 1);
        STAMP(4);
        const u32 mreg = WS_MG + fi * MGS;
        if (fast) {
          {
            const int c0 = (cb % 6) * 256, d = c0 / 12 + l;
            if (fi < 120 && 12 * d < c0 + 256) ST2((u32x2{tag_lo, tag_hi}), WS_MG + d * MGS + 1024 + ((12 * d < c0 ? 20 : 0) + (cb / 6) * 10 + ksp) * FS);
          }
          u32x2 xr;
          { const u32 xo = (64 * ksp) * 2 + i16 * 8; asm volatile("global_load_dwordx2 %0, %1, %2" : "=v"(xr) : "v"(xo), "s"(x + idx * 640)); }
          u32x4 gw[32];
          __builtin_amdgcn_s_setprio(0);
          SIG_WAIT(GW_SIG, GW_AT);
          STAMP(2);
          {
            const u16* gm = cb < 6 ? wg + cb * 256 : wu + (cb - 6) * 256;
            const u32 go = (64 * ksp + 2 * (l >> 5)) * 3072 + (l & 31) * 16;
#pragma unroll
            for (int k = 0; k < 32; k++) asm volatile("global_load_dwordx4 %0, %1, %2 offset:%3" : "=v"(gw[k]) : "v"(go), "s"(gm + (k >> 1) * 4 * 1536), "i"((k & 1) * 3072));
          }
          __builtin_amdgcn_s_setprio(MPRIO);
          STAMP(3);
          {
            asm volatile("s_waitcnt vmcnt(16)" : "+v"(xr) :: "memory");
            const float sel_m = bflo(rec[1]), sel_r = rcp(bfhi(rec[1]));
            u32 xp = (l >> 5) ? xr[1] : xr[0];
            { u32 d = pk2(bflo(xp) - sel_m, bfhi(xp) - sel_m); xp = pk2(bflo(d) * sel_r, bfhi(d) * sel_r); }
            float v[8];
#pragma unroll
            for (int k = 0; k < 8; k++) v[k] = 0.f;
#define M1ROW(r) do { asm volatile("" : "+v"(gw[2 * r]), "+v"(gw[2 * r + 1])); const u32 a = dppu<0x150 + r>(xp); \
              _Pragma("unroll") for (int k = 0; k < 4; k++) { v[2 * k] = dot2(__builtin_amdgcn_perm(gw[2 * r + 1][k], gw[2 * r][k], 0x05040100u), a, v[2 * k]); \
                v[2 * k + 1] = dot2(__builtin_amdgcn_perm(gw[2 * r + 1][k], gw[2 * r][k], 0x07060302u), a, v[2 * k + 1]); } } while (0)
            M1ROW(0); M1ROW(1); M1ROW(2); M1ROW(3); M1ROW(4); M1ROW(5); M1ROW(6); M1ROW(7);
            asm volatile("s_waitcnt vmcnt(0)" ::: "memory");
            STAMP(5);
            M1ROW(8); M1ROW(9); M1ROW(10); M1ROW(11); M1ROW(12); M1ROW(13); M1ROW(14); M1ROW(15);
            u32x4 u4;
#pragma unroll
            for (int k = 0; k < 4; k++) {
              auto r = __builtin_amdgcn_permlane32_swap(__float_as_uint(v[k]), __float_as_uint(v[k + 4]), false, false);
              u4[k] = __float_as_uint(__uint_as_float(r[0]) + __uint_as_float(r[1]));
            }
            ST4(san4(u4), fi < 120 ? mreg + (l & 31) * 32 + (l >> 5) * 16 : WS_END);
          }
          __builtin_amdgcn_s_setprio(3);
          STAMP(6);
        } else {
          if (l < 16) ST2((u32x2{tag_lo, tag_hi}), WS_IY + (l * 128 + fi) * FS);
          u32x2 dw[36];
          {
            const u16* dp = wd + (12 * fi) * 640;
            const u32 dof = l * 8, dof2 = (l < 32 ? l : 0) * 8;
#pragma unroll
            for (int k = 0; k < 12; k++) {
              asm volatile("global_load_dwordx2 %0, %1, %2" : "=v"(dw[3 * k]) : "v"(dof), "s"(dp + k * 640));
              asm volatile("global_load_dwordx2 %0, %1, %2 offset:512" : "=v"(dw[3 * k + 1]) : "v"(dof), "s"(dp + k * 640));
              asm volatile("global_load_dwordx2 %0, %1, %2 offset:1024" : "=v"(dw[3 * k + 2]) : "v"(dof2), "s"(dp + k * 640));
            }
          }
          STAMP(7);
          {
            const int nsl = ((12 * fi) >> 8) == ((12 * fi + 11) >> 8) ? 20 : 40;
#pragma unroll 1
            for (u32 spin = 0; spin < SPIN_CAP; spin++) {
              u32x2 f0 = ld2w(ws, mreg + 1024 + (l < nsl ? l : 0) * FS);
              if (__all(f0[0] == tag_lo && f0[1] == tag_hi)) break;
              __builtin_amdgcn_s_sleep(6);
            }
          }
          SIG_WAIT(M2_SIG, M2_AT);
          STAMP(8);
          {
            const int kw = l / 6, pr = l - 6 * kw, col = 12 * fi + 2 * pr;
            const u32 ga = l < 60 ? WS_MG + ((col >> 8) * 10 + kw) * MGS + (col & 255) * 4 : WS_END;
            u32x2 g2, u2;
#pragma unroll 1
            for (u32 spin = 0; spin < SPIN_CAP; spin++) {
              MEMFENCE();
              g2 = LD2(ga); u2 = __builtin_amdgcn_raw_buffer_load_b64(wsr, ga, 60 * MGS, 16);
              if (__all(umax_(umax_(g2[0], g2[1]), umax_(u2[0], u2[1])) != SENT)) break;
              __builtin_amdgcn_s_sleep(8);
            }
            STAMP(15);
            f32x2 gg2 = __builtin_bit_cast(f32x2, g2), uu2 = __builtin_bit_cast(f32x2, u2);
            float z[4] = {gg2[0], gg2[1], uu2[0], uu2[1]};
#pragma unroll
            for (int k = 0; k < 4; k++) {
              const float z0 = z[k] + __shfl(z[k], (l + 30) & 63);
              z[k] = rbf(z0 + __shfl(z0, (l + 6) & 63) + __shfl(z0, (l + 12) & 63) + __shfl(z0, (l + 18) & 63) + __shfl(z0, (l + 24) & 63));
            }
            const u32 hp2 = pk2(rbf(z[0] * rcp(1.f + __expf(-z[0]))) * z[2], rbf(z[1] * rcp(1.f + __expf(-z[1]))) * z[3]);
#pragma unroll
            for (int k = 0; k < 36; k++) asm volatile("" : "+v"(dw[k]));
#pragma unroll
            for (int zz = 0; zz < 3; zz++) {
              f32x4 y = {0, 0, 0, 0};
#pragma unroll
              for (int i = 0; i < 6; i++) {
                const u32 hh = __builtin_amdgcn_readlane(hp2, i);
                const u32x2 d0 = dw[6 * i + zz], d1 = dw[6 * i + 3 + zz];
                y[0] = dot2(__builtin_amdgcn_perm(d1[0], d0[0], 0x05040100u), hh, y[0]); y[1] = dot2(__builtin_amdgcn_perm(d1[0], d0[0], 0x07060302u), hh, y[1]);
                y[2] = dot2(__builtin_amdgcn_perm(d1[1], d0[1], 0x05040100u), hh, y[2]); y[3] = dot2(__builtin_amdgcn_perm(d1[1], d0[1], 0x07060302u), hh, y[3]);
              }
              { const int u = 64 * zz + l, sl = u / 10; ST4(san4(__builtin_bit_cast(u32x4, y)), u < 160 ? WS_YB + (sl * 128 + fi) * 160 + (u - 10 * sl) * 16 : WS_END); }
            }
          }
        }
        STAMP(9);
        if ((idx >> 7) == rt) {
          SIG_WAIT(M3_SIG, M3_AT);
          STAMP(11);
#pragma unroll 1
          for (u32 spin = 0; spin < SPIN_CAP; spin++) {
            __builtin_amdgcn_s_sleep(6);
            u32x2 f0, f1; const u32 fa_ = WS_IY + (hg * 128 + l) * FS, fb_ = fa_ + 64 * FS;
            asm volatile("global_load_dwordx2 %0, %2, %4 sc1\n\tglobal_load_dwordx2 %1, %3, %4 sc1\n\ts_waitcnt vmcnt(0)" : "=&v"(f0), "=&v"(f1) : "v"(fa_), "v"(fb_), "s"(ws) : "memory");
            if (__all(f0[0] == tag_lo && f0[1] == tag_hi && f1[0] == tag_lo && f1[1] == tag_hi)) break;
          }
          STAMP(12);
          f32x4 t;
          {
            const u32 ya = l < 60 ? WS_YB + og * 20480 + l * 16 : WS_END, yl = l < 20 ? ya : WS_END;
            f32x4 s_;
#pragma unroll 1
            for (u32 spin = 0; spin < SPIN_CAP; spin++) {
              MEMFENCE();
              u32x4 yv[22];
#pragma unroll
              for (int k = 0; k < 22; k++) yv[k] = __builtin_amdgcn_raw_buffer_load_b128(wsr, k < 21 ? ya : yl, k * 960, 16);
              u32 m = 0; s_ = f32x4{0, 0, 0, 0};
#pragma unroll
              for (int k = 0; k < 22; k++) { m = umax_(m, max4(yv[k])); s_ += __builtin_bit_cast(f32x4, yv[k]); }
              if (__all(m != SENT)) break;
              __builtin_amdgcn_s_sleep(6);
            }
            STAMP(13);
#pragma unroll
            for (int r = 0; r < 4; r++) { const float a_ = s_[r] + __shfl(s_[r], (l + 30) & 63); t[r] = a_ + __shfl(a_, (l + 10) & 63) + __shfl(a_, (l + 20) & 63); }
          }
          u32 rb = rec[0] >> 16;
          rb = (rb & 0x8000u) ? (rb & 0x7FFFu) : (~rb & 0xFFFFu);
          const float sig = rbf(rcp(1.f + __expf(-bflo(rb))));
          if (l < 10) *(LAS f32x4*)(sm + L_Y + l * 16) = f32x4{rbf(rbf(t[0]) * sig), rbf(rbf(t[1]) * sig), rbf(rbf(t[2]) * sig), rbf(rbf(t[3]) * sig)};
        }
        SIG_SET(F_Y, idx + 1);
        STAMP(10);
      }
    }
  } else {
    const int tg = w >> 1, kh = w & 1, tw = w;
    u32x4 xf[20];
    const char* xb = (const char*)x + rt * (128 * 1280) + kh * 640;
    const u32 xoff = (u32)((((g >> 1) ? (tw ^ 1) : tw) * 16 + 4 * q4) * 1280 + (g & 1) * 64 + p4 * 16);
#define XI(c, r) ((r) < 2 ? 2 * (c) + (r) : 8 + 2 * (c) + (r))
#define XL1(c, r) asm volatile("global_load_dwordx4 %0, %1, %2 offset:%3" : "=v"(xf[XI(c, r)]) : "v"(xoff), "s"(xb + CROT(c) * 128), "i"((r) * 1280))
#define XG(c) do { XL1(c, 0); XL1(c, 1); XL1(c, 2); XL1(c, 3); } while (0)
    __builtin_amdgcn_s_setprio(2);
    XG(0);
    BARRIER();
    XG(1); XG(2); XG(3); XG(4);
    __builtin_amdgcn_s_setprio(0);
    STAMP(1);
    f32x4 acc[2][5];
#pragma unroll
    for (int t = 0; t < 5; t++) { acc[0][t] = f32x4{0, 0, 0, 0}; acc[1][t] = f32x4{0, 0, 0, 0}; }
    LAS char* w1p = sm + L_W1 + kh * 5120 + (4 * g + q4) * 160 + 8 * p4;
    const u64 mE = 0x5555555555555555ull, mO = ~mE, mL = 0x3333333333333333ull, mH = ~mL;
    u32 ta[4], ua[4];
#define DPX1 " quad_perm:[1,0,3,2] row_mask:0xf bank_mask:0xf\n\t"
#define DPX2 " quad_perm:[2,3,0,1] row_mask:0xf bank_mask:0xf\n\t"
#define CD2(M, X, d0, s0, k0, d1, s1, k1) asm volatile("s_mov_b64 vcc, %6\n\tv_cndmask_b32_dpp %0, %2, %3, vcc" X "v_cndmask_b32_dpp %1, %4, %5, vcc" X \
      : "=&v"(d0), "=&v"(d1) : "v"(s0), "v"(k0), "v"(s1), "v"(k1), "s"(M) : "vcc")
#define CD2I(M, X, d0, s0, d1, s1) asm volatile("s_mov_b64 vcc, %4\n\tv_cndmask_b32_dpp %0, %2, %0, vcc" X "v_cndmask_b32_dpp %1, %3, %1, vcc" X \
      : "+v"(d0), "+v"(d1) : "v"(s0), "v"(s1), "s"(M) : "vcc")
#define A_(c, r) xf[XI(c, r)]
#define TP(c, k) do { const int d_ = (k) & 3; u32 n0, n1; \
      if ((k) < 4) { CD2(mE, DPX1, n0, A_(c, 1)[d_], A_(c, 0)[d_], n1, A_(c, 3)[d_], A_(c, 2)[d_]); ua[d_] = n0; ta[d_] = n1; } \
      else if ((k) < 8) { n0 = A_(c, 1)[d_]; n1 = A_(c, 3)[d_]; CD2I(mO, DPX1, n0, A_(c, 0)[d_], n1, A_(c, 2)[d_]); A_(c, 1)[d_] = n0; A_(c, 3)[d_] = n1; } \
      else if ((k) < 12) { CD2(mL, DPX2, n0, ta[d_], ua[d_], n1, A_(c, 3)[d_], A_(c, 1)[d_]); A_(c, 0)[d_] = n0; A_(c, 2)[d_] = n1; } \
      else if ((k) < 16) { n1 = A_(c, 3)[d_]; CD2I(mH, DPX2, ta[d_], ua[d_], n1, A_(c, 1)[d_]); A_(c, 3)[d_] = n1; } \
      else { auto r0 = __builtin_amdgcn_permlane32_swap(A_(c, 0)[d_], ta[d_], false, false); auto r1 = __builtin_amdgcn_permlane32_swap(A_(c, 2)[d_], A_(c, 3)[d_], false, false); \
        A_(c, 0)[d_] = r0[0]; A_(c, 2)[d_] = r0[1]; A_(c, 1)[d_] = r1[0]; A_(c, 3)[d_] = r1[1]; } } while (0)
#define XW(N, c) asm volatile("s_waitcnt vmcnt(" #N ")" : "+v"(A_(c, 0)), "+v"(A_(c, 1)), "+v"(A_(c, 2)), "+v"(A_(c, 3)) :: "memory")
#define RD(F, j, r) F[r] = trd(w1p + (j) * 10240 + 32 * ((r) >> 1) + 2560 * ((r) & 1))
#define MF(F, j, k) acc[(k) & 1][(k) >> 1] = mfma32(FR(F, (k) >> 1), xf[10 * ((k) & 1) + (j)], acc[(k) & 1][(k) >> 1])
#define STEPA(FA, FB, c, V) do { _Pragma("unroll") for (int k = 0; k < 10; k++) { MF(FA, 2 * (c), k); RD(FB, 2 * (c) + 1, k); if ((c) < 4) TP((c) + 1, k); V(k); SB(); } } while (0)
#define STEPB(FA, FB, c) do { _Pragma("unroll") for (int k = 0; k < 4; k++) { MF(FB, 2 * (c) + 1, k); TP((c) + 1, 10 + k); SB(); } \
      if ((c) == 2) SIG_ADD(F_P1); \
      if ((c) == 3) SIG_WAIT2(F_W0 + kh, (c) + 2, F_WP, 1); else SIG_WAIT(F_W0 + kh, (c) + 2); \
      _Pragma("unroll") for (int k = 4; k < 10; k++) { if (k < 9) { RD(FA, 2 * (c) + 2, 2 * (k - 4)); RD(FA, 2 * (c) + 2, 2 * (k - 4) + 1); } MF(FB, 2 * (c) + 1, k); TP((c) + 1, 10 + k); SB(); } } while (0)
    {
      u32x2 fa[10], fb[10];
      XW(16, 0);
      u32 fil = l;
#pragma unroll
      for (int k = 0; k < 20; k++) { TP(0, k); asm volatile("v_add_u32 %0, 1, %0" : "+v"(fil)); SB(); }
      SIG_WAIT(F_W0 + kh, 1);
      STAMP(2);
#pragma unroll
      for (int r = 0; r < 10; r++) RD(fa, 0, r);
      SB();
#define V3(k) do { if ((k) == 1) XL1(3, 0); if ((k) == 3) XL1(3, 1); if ((k) == 5) XL1(3, 2); if ((k) == 7) XL1(3, 3); } while (0)
#define V4(k) do { if ((k) == 1) XL1(4, 0); if ((k) == 3) XL1(4, 1); if ((k) == 5) XL1(4, 2); if ((k) == 7) XL1(4, 3); } while (0)
#define VN(k) do { } while (0)
      XW(12, 1);
      STEPA(fa, fb, 0, VN); STEPB(fa, fb, 0);
      STAMP(3);
      XW(8, 2);
      STEPA(fa, fb, 1, VN); STEPB(fa, fb, 1);
      STAMP(4);
      XW(4, 3);
      STEPA(fa, fb, 2, VN); STEPB(fa, fb, 2);
      STAMP(5);
      XW(0, 4);
      STEPA(fa, fb, 3, VN); STEPB(fa, fb, 3);
      STAMP(6);
      STEPA(fa, fb, 4, VN);
#pragma unroll
      for (int k = 0; k < 10; k++) MF(fb, 9, (k + 1 - 2 * (k & 1)));
    }
    STAMP(7);
    {
    int l_ = __builtin_amdgcn_mbcnt_hi(~0u, __builtin_amdgcn_mbcnt_lo(~0u, 0u));
    asm volatile("" : "+v"(l_));
    const int l = l_, tid = w * 64 + l, g = l >> 4, i16 = l & 15, q4 = i16 >> 2, p4 = l & 3;
    {
      const int tp = tw ^ 1;
      *(LAS f32x4*)(sm + L_PHI + tp * 2560 + l * 16) = acc[1][0];
      *(LAS f32x4*)(sm + L_PHI + tp * 2560 + 1024 + l * 16) = acc[1][1];
      *(LAS f32x4*)(sm + L_V + tp * 1536 + l * 16) = acc[1][2];
      *(LAS f32x4*)(sm + L_EXA + tp * 2048 + l * 16) = acc[1][3];
      *(LAS f32x4*)(sm + L_EXA + tp * 2048 + 1024 + l * 16) = acc[1][4];
      SIG_SET(F_EX + tp, 1);
    }
    u32x4 wa32[3]; u32x2 wa16[3];
    {
      const int hp = p4 >> 1;
      const int d0 = 4 * g + q4, d1 = d0 + 16, d2 = d0 + 32;
      LAS char* a0 = sm + (hp == 0 ? L_WPHI + d0 * 48 + 24 * (p4 & 1) : L_ZERO);
      LAS char* a1 = sm + ((d1 >= 20) == (hp == 1) ? L_WPHI + d1 * 48 + 24 * (p4 & 1) : L_ZERO);
      LAS char* a2 = sm + ((hp == 1 && d2 < 40) ? L_WPHI + d2 * 48 + 24 * (p4 & 1) : L_ZERO);
#pragma unroll
      for (int mt = 0; mt < 3; mt++) {
        u32x2 e0 = trd(a0 + 8 * mt), e1 = trd(a1 + 8 * mt);
        wa32[mt] = u32x4{e0[0], e0[1], e1[0], e1[1]};
        wa16[mt] = trd(a2 + 8 * mt);
      }
    }
    SIG_WAIT2(F_EX + tw, 1, F_CS, 2);
    STAMP(8);
    const u32 msv = *(LAS u32*)(sm + L_MS + (tw * 16 + i16) * 4);
    const float m_b = bflo(msv), rs = rcp(bfhi(msv));
    f32x4 cc[5], a[5];
#pragma unroll
    for (int t = 0; t < 3; t++) cc[t] = *(LAS f32x4*)(sm + L_CSP + (16 * t + 4 * g) * 4);
    a[0] = acc[0][0] + *(LAS f32x4*)(sm + L_PHI + tw * 2560 + l * 16);
    a[1] = acc[0][1] + *(LAS f32x4*)(sm + L_PHI + tw * 2560 + 1024 + l * 16);
    a[2] = acc[0][2] + *(LAS f32x4*)(sm + L_V + tw * 1536 + l * 16);
    {
      u32 kn[5][2];
#define NRM(A, t, C, O) do { f32x4 v_ = (A - m_b * C) * rs; O[t][0] = pk2(v_[0], v_[1]); O[t][1] = pk2(v_[2], v_[3]); } while (0)
      NRM(a[0], 0, cc[0], kn); NRM(a[1], 1, cc[1], kn); NRM(a[2], 2, cc[2], kn);
      SB();
      cc[3] = *(LAS f32x4*)(sm + L_CSP + (48 + 4 * g) * 4); cc[4] = *(LAS f32x4*)(sm + L_CSP + (64 + 4 * g) * 4);
      a[3] = acc[0][3] + *(LAS f32x4*)(sm + L_EXA + tw * 2048 + l * 16);
      a[4] = acc[0][4] + *(LAS f32x4*)(sm + L_EXA + tw * 2048 + 1024 + l * 16);
      u32x4 b32 = {kn[0][0], kn[0][1], kn[1][0], kn[1][1]};
      u32x2 b16 = {kn[2][0], kn[2][1]};
      f32x4 pp[3];
#pragma unroll
      for (int mt = 0; mt < 3; mt++) {
        pp[mt] = mfma32(wa32[mt], b32, f32x4{0, 0, 0, 0});
        pp[mt] = mfma16(wa16[mt], b16, pp[mt]);
      }
      NRM(a[3], 3, cc[3], kn); NRM(a[4], 4, cc[4], kn);
      __builtin_amdgcn_wave_barrier();
      LAS char* vr = sm + L_V + (16 * tw + i16) * 96;
      u32x2 v2 = (g >= 2) ? u32x2{kn[2][0], kn[2][1]} : u32x2{g == 0 ? 0x3F80u : 0u, 0u};
      *(LAS u32x2*)(vr + (g >= 2 ? 8 * (g - 2) : 80 + 8 * g)) = v2;
      *(LAS u32x2*)(vr + 16 + 8 * g) = u32x2{kn[3][0], kn[3][1]};
      *(LAS u32x2*)(vr + 48 + 8 * g) = u32x2{kn[4][0], kn[4][1]};
      u32 ph[10];
      softmax_pack(pp[0], pp[1], pp[2], ph);
      LAS char* pr = sm + L_PHI + (16 * tw + i16) * 160 + 40 * g;
#pragma unroll
      for (int k = 0; k < 5; k++) *(LAS u32x2*)(pr + 8 * k) = u32x2{ph[2 * k], ph[2 * k + 1]};
    }
    STAMP(9);
    SIG_ADD(F_PHI);
    __builtin_amdgcn_s_setprio(2);
    {
      const int mt0 = (0x22111000 >> (4 * w)) & 3, nt0 = (0x10210210 >> (4 * w)) & 3;
      const int mt1 = (0x22144332 >> (4 * w)) & 7, nt1 = (0x10221212 >> (4 * w)) & 3;
      u32 ap0 = L_PHI + (4 * g + q4) * 160 + 8 * p4 + 32 * mt0, ap1 = ap0 + 32 * (mt1 - mt0);
      u32 bp0 = L_V + (4 * g + q4) * 96 + 8 * p4 + 32 * nt0, bp1 = bp0 + 32 * (nt1 - nt0);
#define KVA(mt, nt, en) ({ const int fq = 4 * (mt) + g, col = 16 * (nt) + i16; int c = col - (fq >= 10 ? 20 : 0); bool ok = (c >= 0 && c < 20); \
        if (col == 40) { c = 20; ok = true; } (ok && (en)) ? reg + R_KVP + (fq * 21 + c) * 16 : WS_END; })
      u32 ks0 = KVA(mt0, nt0, true), ks1 = KVA(mt1, nt1, w < 5);
      asm volatile("" : "+v"(ap0), "+v"(ap1), "+v"(bp0), "+v"(bp1), "+v"(ks0), "+v"(ks1));
      SIG_WAIT(F_PHI, 9);
      STAMP(10);
      f32x4 d0 = {0, 0, 0, 0}, d1 = {0, 0, 0, 0};
#pragma unroll
      for (int u = 0; u < 4; u++) {
        d0 = mfma32(trd2(sm + ap0 + u * 5120, 2560), trd2(sm + bp0 + u * 3072, 1536), d0);
        d1 = mfma32(trd2(sm + ap1 + u * 5120, 2560), trd2(sm + bp1 + u * 3072, 1536), d1);
      }
      ST4(san4(__builtin_bit_cast(u32x4, d0)), ks0); ST4(san4(__builtin_bit_cast(u32x4, d1)), ks1);
    }
    SIG_ADD(F_KR);
    SIG_WAIT(F_Q0 + kh, 1);
    STAMP(11);
    f32x4 aq[2][3];
#pragma unroll
    for (int t = 0; t < 3; t++) { aq[0][t] = f32x4{0, 0, 0, 0}; aq[1][t] = f32x4{0, 0, 0, 0}; }
    {
      LAS char* wqp = sm + L_WQ + kh * 3072 + (4 * g + q4) * 96 + 8 * p4;
      u32x2 fa[6], fb[6];
#define RQ(F, P_, o, r) F[r] = trd(P_ + (o) + 32 * ((r) >> 1) + 1536 * ((r) & 1))
#define STEPQ(FA, FB, j) do { _Pragma("unroll") for (int k = 0; k < 6; k++) { aq[k & 1][k >> 1] = mfma32(FR(FA, k >> 1), xf[10 * (k & 1) + (j)], aq[k & 1][k >> 1]); \
          if ((j) < 9) RQ(FB, wqp, ((j) + 1) * 6144, k); SB(); } } while (0)
#pragma unroll
      for (int r = 0; r < 6; r++) RQ(fa, wqp, 0, r);
      SB();
#pragma unroll
      for (int j = 0; j < 10; j += 2) { STEPQ(fa, fb, j); STEPQ(fb, fa, j + 1);
      }
    }
#pragma unroll
    for (int t = 0; t < 3; t++) *(LAS f32x4*)(sm + L_EXQ + (tw ^ 1) * 3072 + t * 1024 + l * 16) = aq[1][t];
    SIG_SET(F_EX + (tw ^ 1), 2);
    SIG_ADD(F_P2);
    STAMP(12);
    SIG_WAIT2(F_EX + tw, 2, F_CSQ, 2);
    STAMP(13);
    const u32 slo0 = (rt * 128 + tw * 16 + l / 5) * 1280 + og * 80 + (l % 5) * 16;
    const u32 slo1 = (rt * 128 + tw * 16 + (64 + i16) / 5) * 1280 + og * 80 + ((64 + i16) % 5) * 16;
    SIG_WAIT(F_KR, 8);
    glds16(x, slo0, ldsb + L_V + tw * 1536);
    if (l < 16) glds16(x, slo1, ldsb + L_V + tw * 1536 + 1024);
    u32 pq[10];
    {
      f32x4 aqo[3], c2[3];
      u32 qn[3][2];
#pragma unroll
      for (int t = 0; t < 3; t++) {
        c2[t] = *(LAS f32x4*)(sm + L_CSQ + (16 * t + 4 * g) * 4) + *(LAS f32x4*)(sm + L_CSQ + (48 + 16 * t + 4 * g) * 4);
        aqo[t] = aq[0][t] + *(LAS f32x4*)(sm + L_EXQ + tw * 3072 + t * 1024 + l * 16);
        NRM(aqo[t], t, c2[t], qn);
      }
      u32x4 b32 = {qn[0][0], qn[0][1], qn[1][0], qn[1][1]};
      u32x2 b16 = {qn[2][0], qn[2][1]};
      f32x4 pp[3];
#pragma unroll
      for (int mt = 0; mt < 3; mt++) {
        pp[mt] = mfma32(wa32[mt], b32, f32x4{0, 0, 0, 0});
        pp[mt] = mfma16(wa16[mt], b16, pp[mt]);
      }
      softmax_pack(pp[0], pp[1], pp[2], pq);
#pragma unroll
      for (int k = 0; k < 10; k++) asm volatile("" : "+v"(pq[k]));
    }
    STAMP(14);
    u32 oad[3];
#pragma unroll
    for (int mt = 0; mt < 3; mt++) {
      const int col = mt < 2 ? 8 * q4 + 4 * mt + p4 : 32 + 2 * q4 + p4;
      const int hp = (mt == 2) ? (p4 == 2 ? 0 : 1) : (col >= 20 ? 1 : 0);
      const int row = (mt == 2 && p4 >= 2) ? hp * 21 + 20 : col + hp;
      oad[mt] = ((g >> 1) == hp) ? (u32)(L_KVT + row * 80 + 40 * (g & 1)) : L_ZERO;
      asm volatile("" : "+v"(oad[mt]));
    }
    STAMP(15);
    SIG_WAIT(F_KVT, 1);
    STAMP(16);
    {
      f32x4 oo[3];
#pragma unroll
      for (int mt = 0; mt < 3; mt++) {
        LAS char* ap = sm + oad[mt];
        u32x2 a0 = *(LAS u32x2*)(ap), a1 = *(LAS u32x2*)(ap + 8), a2 = *(LAS u32x2*)(ap + 16), a3 = *(LAS u32x2*)(ap + 24), a4 = *(LAS u32x2*)(ap + 32);
        oo[mt] = mfma32(u32x4{a0[0], a0[1], a1[0], a1[1]}, u32x4{pq[0], pq[1], pq[2], pq[3]}, f32x4{0, 0, 0, 0});
        oo[mt] = mfma32(u32x4{a2[0], a2[1], a3[0], a3[1]}, u32x4{pq[4], pq[5], pq[6], pq[7]}, oo[mt]);
        oo[mt] = mfma16(a4, u32x2{pq[8], pq[9]}, oo[mt]);
      }
      const float r0 = rcp(oo[2][2] + 1e-6f), r1 = rcp(oo[2][3] + 1e-6f);
      const float ra_ = g <= 2 ? r0 : r1, rb_ = g <= 1 ? r0 : r1;
      u32x4 o8 = {pk2(oo[0][0] * ra_, oo[0][1] * ra_), pk2(oo[0][2] * ra_, oo[0][3] * ra_), pk2(oo[1][0] * rb_, oo[1][1] * rb_), pk2(oo[1][2] * rb_, oo[1][3] * rb_)};
      u32 o2 = pk2(oo[2][0] * r1, oo[2][1] * r1);
      const u32 ob = reg + R_OB + tw * 1280 + l * 16;
      ST4(o8, ob);
      auto ra = __builtin_amdgcn_permlane32_swap(o2, o2, false, false);
      auto rb = __builtin_amdgcn_permlane16_swap(ra[0], ra[0], false, false);
      auto rc2 = __builtin_amdgcn_permlane16_swap(ra[1], ra[1], false, false);
      if (g == 0) ST4((u32x4{rb[0], rb[1], rc2[0], rc2[1]}), ob + 1024);
      asm volatile("s_waitcnt vmcnt(0)" ::: "memory");
      SIG_ADD(F_OS);
    }
    MEMFENCE();
    STAMP(17);
    u32x2 xv[3], xnp[3];
    {
      LAS char* xp = sm + L_V + tw * 1536 + i16 * 80 + 8 * g;
#pragma unroll
      for (int t = 0; t < 3; t++) {
        xv[t] = *(LAS u32x2*)(xp + 32 * t);
        u32 d0 = pk2(bflo(xv[t][0]) - m_b, bfhi(xv[t][0]) - m_b), d1 = pk2(bflo(xv[t][1]) - m_b, bfhi(xv[t][1]) - m_b);
        xnp[t] = u32x2{pk2(bflo(d0) * rs, bfhi(d0) * rs), pk2(bflo(d1) * rs, bfhi(d1) * rs)};
        asm volatile("" : "+v"(xnp[t]));
      }
    }
    const u32 obase = WS_REG + (rt * 16 + 8 * kh) * RS + R_OB + tg * 2560;
    u32 oa[5];
#pragma unroll
    for (int k = 0; k < 5; k++) { int C = 64 * k + l, pr = C / 80; oa[k] = obase + pr * (RS - 1280) + C * 16; asm volatile("" : "+v"(oa[k])); }
    f32x4 ao[2][3];
    int idx;
    {
      LAS char* wop = sm + (kh ? L_WO1 : L_WO0) + (4 * g + q4) * 96 + 8 * p4;
      SIG_WAIT2(F_WO, 2, F_IDX, 1);
      idx = __builtin_amdgcn_readfirstlane((int)*(volatile LAS u32*)(sm + L_SYN + F_IDX * 4) - 1);
      SIG_WAIT(kh ? F_O1 : F_O, 1);
      STAMP(19);
      {
        u32x4 of[2][10];
#pragma unroll
        for (int j = 0; j < 10; j++) {
          of[0][j] = __builtin_amdgcn_raw_buffer_load_b128(wsr, oa[j % 5], (j / 5) * 4 * RS + kh * 1280, 16);
          of[1][j] = __builtin_amdgcn_raw_buffer_load_b128(wsr, oa[j % 5], (j / 5) * 4 * RS + (1 - kh) * 1280, 16);
        }
#pragma unroll
        for (int t = 0; t < 3; t++) { ao[0][t] = f32x4{0, 0, 0, 0}; ao[1][t] = f32x4{0, 0, 0, 0}; }
        u32x2 fa[6], fb[6];
#define STEPO(FA, FB, j) do { _Pragma("unroll") for (int k = 0; k < 6; k++) { ao[k & 1][k >> 1] = mfma32(FR(FA, k >> 1), of[k & 1][j], ao[k & 1][k >> 1]); \
            if ((j) < 9) RQ(FB, wop, ((j) + 1) * 3072, k); SB(); } } while (0)
#pragma unroll
        for (int r = 0; r < 6; r++) RQ(fa, wop, 0, r);
        SB();
#pragma unroll
        for (int j = 0; j < 10; j += 2) { STEPO(fa, fb, j); STEPO(fb, fa, j + 1); }
      }
    }
#pragma unroll
    for (int t = 0; t < 3; t++) *(LAS f32x4*)(sm + L_EXQ + (tw ^ 1) * 3072 + t * 1024 + l * 16) = ao[1][t];
    SIG_SET(F_EX + (tw ^ 1), 3);
    STAMP(20);
    SIG_WAIT(F_EX + tw, 3);
    STAMP(21);
    {
      LAS char* xp = sm + L_V + tw * 1536 + i16 * 80 + 8 * g;
      if ((idx >> 4) == rt * 8 + tw) SIG_WAIT(F_Y, 1);
      if (idx == rt * 128 + tw * 16 + i16) {
#pragma unroll
        for (int t = 0; t < 3; t++) if (t < 2 || g < 2) {
          f32x4 yy = *(LAS f32x4*)(sm + L_Y + (16 * t + 4 * g) * 4);
          xnp[t] = u32x2{pk2(bflo(xnp[t][0]) + yy[0], bfhi(xnp[t][0]) + yy[1]), pk2(bflo(xnp[t][1]) + yy[2], bfhi(xnp[t][1]) + yy[3])};
        }
      }
#pragma unroll
      for (int t = 0; t < 3; t++) {
        f32x4 af = ao[0][t] + *(LAS f32x4*)(sm + L_EXQ + tw * 3072 + t * 1024 + l * 16);
        u32 p0 = pk2(af[0], af[1]), p1 = pk2(af[2], af[3]);
        u32 q0 = pk2(bflo(xv[t][0]) + bflo(p0), bfhi(xv[t][0]) + bfhi(p0)), q1 = pk2(bflo(xv[t][1]) + bflo(p1), bfhi(xv[t][1]) + bfhi(p1));
        u32x2 res = {pk2(bflo(q0) + bflo(xnp[t][0]), bfhi(q0) + bfhi(xnp[t][0])), pk2(bflo(q1) + bflo(xnp[t][1]), bfhi(q1) + bfhi(xnp[t][1]))};
        if (t < 2 || g < 2) *(LAS u32x2*)(xp + 32 * t) = res;
      }
      __builtin_amdgcn_wave_barrier();
      LAS char* op = sm + L_V + tw * 1536 + l * 16;
      *(u32x4*)((char*)out + slo0) = *(LAS u32x4*)(op);
      if (l < 16) *(u32x4*)((char*)out + slo1) = *(LAS u32x4*)(op + 1024);
    }
    STAMP(22);
    }
  }
}

extern "C" void kernel_launch(void* const* d_in, const int* in_sizes, int n_in, void* d_out, int out_size,
                              void* d_ws, size_t ws_size, hipStream_t stream) {
  fused_kernel<<<256, 768, 0, stream>>>((const u16*)d_in[0], (const u16*)d_in[1], (const u16*)d_in[2], (const u16*)d_in[3],
                                        (const u16*)d_in[5], (const u16*)d_in[6], (const u16*)d_in[7], (const u16*)d_in[8],
                                        (u16*)d_out, (char*)d_ws);
}
```

```cpp
#include <hip/hip_runtime.h>
#include <stdint.h>

typedef unsigned short u16;
typedef unsigned int u32;
typedef unsigned long long u64;
typedef __attribute__((ext_vector_type(8))) __bf16 bf16x8;
typedef __attribute__((ext_vector_type(4))) short s16x4;
typedef __attribute__((ext_vector_type(4))) float f32x4;
typedef __attribute__((ext_vector_type(2))) float f32x2;
typedef __attribute__((ext_vector_type(2))) __bf16 bf16x2;
typedef __attribute__((ext_vector_type(4))) u32 u32x4;
typedef __attribute__((ext_vector_type(2))) u32 u32x2;
#define LAS __attribute__((address_space(3)))
#define DI __device__ __forceinline__

#define FS 8
constexpr u32 WS_EPOCH = 0;
constexpr u32 WS_REC = 8192;
constexpr u32 WS_ST = 16384;
constexpr u32 WS_REG = 49152;
constexpr u32 RS = 23936;
constexpr u32 R_OB = 0;
constexpr u32 R_KVP = 10240;
constexpr u32 R_CS = 19584;
constexpr u32 R_KVR = 20608;
constexpr u32 R_I1 = 20864;
constexpr u32 R_I3 = 21760;
constexpr u32 WS_YB = WS_REG + 256 * RS;
constexpr u32 WS_MG = WS_YB + 327680;
constexpr u32 MGS = 1024 + 40 * 128;
constexpr u32 WS_IY = WS_MG + 128 * MGS;
constexpr u32 WS_END = WS_IY + 2048 * 128;
constexpr u32 WS_STAMP = 128u << 20;

constexpr u32 L_W1 = 0;
constexpr u32 L_WQ = 0;
constexpr u32 L_WO0 = 0;
constexpr u32 L_WO1 = 86016;
constexpr u32 L_Y2 = 137216;
constexpr u32 L_EXQ = 61440;
constexpr u32 L_CSP = 102400;
constexpr u32 L_EXA = 103424;
constexpr u32 L_PF = 162816;
constexpr u32 L_PHI = 122880;
constexpr u32 L_V = 143360;
constexpr u32 L_SM = 155648;
constexpr u32 L_WPHI = L_SM;
constexpr u32 L_ZERO = L_SM + 1920;
constexpr u32 L_KVT = L_SM + 1984;
constexpr u32 L_CSQ = L_KVT + 3360;
constexpr u32 L_KEY = L_CSQ + 768;
constexpr u32 L_IDX = L_KEY + 64;
constexpr u32 L_Y = L_IDX + 64;
constexpr u32 L_MS = L_Y + 160;
constexpr u32 L_SYN = L_MS + 512;
enum { F_W0 = 0, F_W1, F_ST, F_CS, F_P1, F_Q0, F_Q1, F_PHI, F_KV, F_CSQ, F_P2, F_KVT, F_WO, F_OS, F_O, F_Y, F_EX = 16, F_WP = 24, F_KR, F_IDX, F_YG, F_YP, F_O1 };
constexpr u32 L_TOTAL = 163840;

constexpr u32 SENT = 0xFFFFFFFFu;
constexpr u32 SPIN_CAP = 1u << 15;

DI u32 pk2(float a, float b) { f32x2 v = {a, b}; return __builtin_bit_cast(u32, __builtin_convertvector(v, bf16x2)); }
DI float bflo(u32 u) { return __uint_as_float(u << 16); }
DI float bfhi(u32 u) { return __uint_as_float(u & 0xFFFF0000u); }
DI float rbf(float a) { return bflo(pk2(a, 0.f)); }
DI float rcp(float a) { return __builtin_amdgcn_rcpf(a); }
DI float dot2(u32 a, u32 b, float c) { return __builtin_amdgcn_fdot2_f32_bf16(__builtin_bit_cast(bf16x2, a), __builtin_bit_cast(bf16x2, b), c, false); }
DI f32x4 mfma32(u32x4 a, u32x4 b, f32x4 c) {
  return __builtin_amdgcn_mfma_f32_16x16x32_bf16(__builtin_bit_cast(bf16x8, a), __builtin_bit_cast(bf16x8, b), c, 0, 0, 0);
}
DI f32x4 mfma16(u32x2 a, u32x2 b, f32x4 c) {
  return mfma32(u32x4{a[0], a[1], 0u, 0u}, u32x4{b[0], b[1], 0u, 0u}, c);
}
DI u32x2 trd(LAS char* p) { return __builtin_bit_cast(u32x2, __builtin_amdgcn_ds_read_tr16_b64_v4i16((LAS s16x4*)p)); }
DI u32x4 trd2(LAS char* p, u32 d) { u32x2 a = trd(p), b = trd(p + d); return u32x4{a[0], a[1], b[0], b[1]}; }
DI void glds16(const void* sbase, u32 voff, u32 lds_dst) {
  asm volatile("s_mov_b32 m0, %2\n\ts_nop 0\n\tglobal_load_lds_dwordx4 %0, %1" :: "v"(voff), "s"(sbase), "s"(lds_dst) : "memory");
}
DI u32 umin_(u32 a, u32 b) { return a < b ? a : b; }
DI u32 umax_(u32 a, u32 b) { return a > b ? a : b; }
DI u32 san(u32 v) { return umin_(v, 0xFFFFFFFEu); }
DI u32x4 san4(u32x4 v) { return u32x4{san(v[0]), san(v[1]), san(v[2]), san(v[3])}; }
DI u32 max4(u32x4 v) { return umax_(umax_(v[0], v[1]), umax_(v[2], v[3])); }
DI float xchg16_max(float v) { auto r = __builtin_amdgcn_permlane16_swap(__float_as_uint(v), __float_as_uint(v), false, false); return fmaxf(__uint_as_float(r[0]), __uint_as_float(r[1])); }
DI float xchg16_min(float v) { auto r = __builtin_amdgcn_permlane16_swap(__float_as_uint(v), __float_as_uint(v), false, false); return fminf(__uint_as_float(r[0]), __uint_as_float(r[1])); }
DI float xchg16_add(float v) { auto r = __builtin_amdgcn_permlane16_swap(__float_as_uint(v), __float_as_uint(v), false, false); return __uint_as_float(r[0]) + __uint_as_float(r[1]); }
DI float xchg32_add(float v) { auto r = __builtin_amdgcn_permlane32_swap(__float_as_uint(v), __float_as_uint(v), false, false); return __uint_as_float(r[0]) + __uint_as_float(r[1]); }
template <int CTRL> DI float dpp(float v) { return __int_as_float(__builtin_amdgcn_update_dpp(0, __float_as_int(v), CTRL, 0xf, 0xf, true)); }
template <int CTRL> DI u32 dppu(u32 v) { return (u32)__builtin_amdgcn_update_dpp(0, (int)v, CTRL, 0xf, 0xf, true); }
DI float wave_sum(float v) {
  v += dpp<0xB1>(v); v += dpp<0x4E>(v); v += dpp<0x141>(v); v += dpp<0x140>(v);
  return xchg32_add(xchg16_add(v));
}
DI float row_sum(float v) { v += dpp<0xB1>(v); v += dpp<0x4E>(v); v += dpp<0x141>(v); return v + dpp<0x140>(v); }
DI u32 row_max(u32 v) { v = umax_(v, dppu<0xB1>(v)); v = umax_(v, dppu<0x4E>(v)); v = umax_(v, dppu<0x141>(v)); return umax_(v, dppu<0x140>(v)); }
DI u32x4 ld4w(const char* base, u32 off) { u32x4 v; asm volatile("global_load_dwordx4 %0, %1, %2 sc1\n\ts_waitcnt vmcnt(0)" : "=&v"(v) : "v"(off), "s"(base) : "memory"); return v; }
DI u32x2 ld2w(const char* base, u32 off) { u32x2 v; asm volatile("global_load_dwordx2 %0, %1, %2 sc1\n\ts_waitcnt vmcnt(0)" : "=&v"(v) : "v"(off), "s"(base) : "memory"); return v; }
#define MEMFENCE() asm volatile("" ::: "memory")
#define BARRIER() asm volatile("s_waitcnt lgkmcnt(0)\n\ts_barrier" ::: "memory")
#define SB() __builtin_amdgcn_sched_barrier(0)
#define LD4(off) __builtin_amdgcn_raw_buffer_load_b128(wsr, (off), 0, 16)
#define LD2(off) __builtin_amdgcn_raw_buffer_load_b64(wsr, (off), 0, 16)
#define ST4(v, off) __builtin_amdgcn_raw_buffer_store_b128((v), wsr, (off), 0, 16)
#define ST2(v, off) __builtin_amdgcn_raw_buffer_store_b64((v), wsr, (off), 0, 16)

#define STAMP(k) do { } while (0)

DI void softmax_pack(const f32x4& p0, const f32x4& p1, const f32x4& p2, u32 (&o)[10]) {
  float p[10] = {p0[0], p0[1], p0[2], p0[3], p1[0], p1[1], p1[2], p1[3], p2[0], p2[1]};
  float mx = p[0], mn = p[0];
#pragma unroll
  for (int i = 1; i < 10; i++) { mx = fmaxf(mx, p[i]); mn = fminf(mn, p[i]); }
  mx = xchg16_max(mx); mn = xchg16_min(mn);
  const float L2E = 1.4426950408889634f;
  const float a = -mx * L2E, b = mn * L2E;
  float e1[10], e2[10], s1 = 0.f, s2 = 0.f;
#pragma unroll
  for (int i = 0; i < 10; i++) {
    e1[i] = __builtin_amdgcn_exp2f(fmaf(p[i], L2E, a));
    e2[i] = __builtin_amdgcn_exp2f(fmaf(p[i], -L2E, b));
    s1 += e1[i]; s2 += e2[i];
  }
  s1 = xchg16_add(s1); s2 = xchg16_add(s2);
  const float r1 = rcp(s1), r2 = rcp(s2);
#pragma unroll
  for (int i = 0; i < 5; i++) {
    o[i] = pk2(e1[2 * i] * r1, e1[2 * i + 1] * r1);
    o[5 + i] = pk2(e2[2 * i] * r2, e2[2 * i + 1] * r2);
  }
}

#define SIG_SET(f, v) do { MEMFENCE(); *(volatile LAS u32*)(sm + L_SYN + (f) * 4) = (v); MEMFENCE(); } while (0)
DI void sig_add(u32 addr) {
  u64 keep; u32 one = 1;
  asm volatile("s_mov_b64 %0, exec\n\ts_mov_b64 exec, 1\n\tds_add_u32 %1, %2\n\ts_mov_b64 exec, %0" : "=&s"(keep) : "v"(addr), "v"(one) : "memory");
}
#define SIG_ADD(f) sig_add(ldsb + L_SYN + (f) * 4)
DI void sig_wait(u32 addr, u32 val) {
  u32 tv, ts, cnt;
  asm volatile(
      "s_mov_b32 %2, 0x400000\n"
      "LW%=:\n\t"
      "ds_read_b32 %0, %3\n\t"
      "s_waitcnt lgkmcnt(0)\n\t"
      "v_readfirstlane_b32 %1, %0\n\t"
      "s_nop 1\n\t"
      "s_cmp_ge_u32 %1, %4\n\t"
      "s_cbranch_scc1 LD%=\n\t"
      "s_sleep 1\n\t"
      "s_sub_u32 %2, %2, 1\n\t"
      "s_cmp_lg_u32 %2, 0\n\t"
      "s_cbranch_scc1 LW%=\n"
      "LD%=:"
      : "=&v"(tv), "=&s"(ts), "=&s"(cnt) : "v"(addr), "s"(val) : "memory", "scc");
}
DI void sig_wait2(u32 a0, u32 v0, u32 a1, u32 v1) {
  u32 t0, t1, s0, s1, cnt;
  asm volatile(
      "s_mov_b32 %4, 0x400000\n"
      "LW%=:\n\t"
      "ds_read_b32 %0, %5\n\t"
      "ds_read_b32 %1, %7\n\t"
      "s_waitcnt lgkmcnt(0)\n\t"
      "v_readfirstlane_b32 %2, %0\n\t"
      "v_readfirstlane_b32 %3, %1\n\t"
      "s_nop 1\n\t"
      "s_cmp_ge_u32 %2, %6\n\t"
      "s_cbranch_scc0 LS%=\n\t"
      "s_cmp_ge_u32 %3, %8\n\t"
      "s_cbranch_scc1 LD%=\n"
      "LS%=:\n\t"
      "s_sleep 1\n\t"
      "s_sub_u32 %4, %4, 1\n\t"
      "s_cmp_lg_u32 %4, 0\n\t"
      "s_cbranch_scc1 LW%=\n"
      "LD%=:"
      : "=&v"(t0), "=&v"(t1), "=&s"(s0), "=&s"(s1), "=&s"(cnt) : "v"(a0), "s"(v0), "v"(a1), "s"(v1) : "memory", "scc");
}
#define SIG_WAIT2(f0, v0, f1, v1) sig_wait2(ldsb + L_SYN + (f0) * 4, (v0), ldsb + L_SYN + (f1) * 4, (v1))
#define SIG_WAIT(f, v) sig_wait(ldsb + L_SYN + (f) * 4, (v))
#define CLR_AT 1
#define GW_AT 4
#define REC_AT 3
#define ST_AT 2
#define GW_SIG F_W0
#define H1D 2
#define H1S 1
#define WORD 0
#define M3_SIG F_OS
#define M3_AT 4
#define H2S 2
#define MPRIO 3
#define M2_SIG F_P2
#define M2_AT 10
#define QW4 5
#define MPF 3
#define RAWBAR() asm volatile("s_barrier" ::: "memory")
__global__ __launch_bounds__(768, 3) void fused_kernel(
    const u16* __restrict__ x, const u16* __restrict__ wqkv, const u16* __restrict__ wphi, const u16* __restrict__ wo,
    const u16* __restrict__ wr, const u16* __restrict__ wg, const u16* __restrict__ wu, const u16* __restrict__ wd,
    u16* __restrict__ out, char* __restrict__ ws) {
  __shared__ __attribute__((aligned(1024))) char smem_[L_TOTAL];
  LAS char* sm = (LAS char*)smem_;
  const u32 ldsb = (u32)(size_t)smem_;
  STAMP(0);
  const int tid = threadIdx.x, l = tid & 63;
  const int w = __builtin_amdgcn_readfirstlane(tid >> 6);
  const int g = l >> 4, i16 = l & 15, q4 = i16 >> 2, p4 = l & 3;
  const int b = blockIdx.x, xcd = b & 7, slot = b >> 3;
  const int rt = (xcd >> 1) * 4 + (slot >> 3), hg = (xcd & 1) * 8 + (slot & 7);
  const int wgi = rt * 16 + hg;
  const bool fast = (0xA5 >> (hg & 7)) & 1;
  const int hh = hg & 1, cs2 = hg >> 1;
  const int rot = (hg + 2 * rt) % 5;
  const auto wsr = __builtin_amdgcn_make_buffer_rsrc((void*)ws, 0, WS_END, 0x00020000);
  const u32 reg = WS_REG + wgi * RS;
#define CROT(c) ((c) + rot >= 5 ? (c) + rot - 5 : (c) + rot)
#define FR(F, t) (u32x4{F[2 * (t)][0], F[2 * (t)][1], F[2 * (t) + 1][0], F[2 * (t) + 1][1]})
#define LOADEP() u32x2 ep; { const u32 z = 0; asm volatile("global_load_dwordx2 %0, %1, %2 sc1" : "=v"(ep) : "v"(z), "s"(ws) : "memory"); }
#define GETTAG() u32 tag_lo, tag_hi, ep_hi; { u64 e = (((u64)ep[1] << 32) | ep[0]) + 1; tag_lo = __builtin_amdgcn_readfirstlane((u32)e); ep_hi = __builtin_amdgcn_readfirstlane((u32)(e >> 32)); tag_hi = ep_hi ^ (tag_lo * 0x9E3779B1u + 0x7F4A7C15u); }

  if (tid < 32) *(LAS u32*)(sm + L_SYN + tid * 4) = 0u;
  if (w >= 8) {
    const int h = w - 8;
    __builtin_amdgcn_s_setprio(3);
    int l_ = __builtin_amdgcn_mbcnt_hi(~0u, __builtin_amdgcn_mbcnt_lo(~0u, 0u));
    asm volatile("" : "+v"(l_));
    const int l = l_, tid = w * 64 + l, g = l >> 4, i16 = l & 15, q4 = i16 >> 2, p4 = l & 3;
    const u32x4 ONES = {0x3F803F80u, 0x3F803F80u, 0x3F803F80u, 0x3F803F80u};
#define YSUM(half, sig) do { \
      const u32 ya = l < 60 ? WS_YB + (2 * cs2 + (half)) * 20480 + l * 16 : WS_END, yl = l < 20 ? ya : WS_END; \
      f32x4 s_, t; \
      _Pragma("unroll 1") for (u32 spin = 0; spin < SPIN_CAP; spin++) { \
        MEMFENCE(); \
        u32x4 yv[22]; \
        _Pragma("unroll") for (int k = 0; k < 22; k++) yv[k] = __builtin_amdgcn_raw_buffer_load_b128(wsr, k < 21 ? ya : yl, k * 960, 16); \
        u32 m = 0; s_ = f32x4{0, 0, 0, 0}; \
        _Pragma("unroll") for (int k = 0; k < 22; k++) { m = umax_(m, max4(yv[k])); s_ += __builtin_bit_cast(f32x4, yv[k]); } \
        if (__all(m != SENT)) break; \
        __builtin_amdgcn_s_sleep(6); } \
      _Pragma("unroll") for (int r = 0; r < 4; r++) { const float a_ = s_[r] + __shfl(s_[r], (l + 30) & 63); t[r] = a_ + __shfl(a_, (l + 10) & 63) + __shfl(a_, (l + 20) & 63); } \
      if (l < 10) *(LAS f32x4*)(sm + L_Y2 + (half) * 160 + l * 16) = f32x4{rbf(rbf(t[0]) * (sig)), rbf(rbf(t[1]) * (sig)), rbf(rbf(t[2]) * (sig)), rbf(rbf(t[3]) * (sig))}; \
      SIG_ADD(F_YP); } while (0)
    if (h < 2) {
      u32 woff[5];
#pragma unroll
      for (int k = 0; k < 5; k++) {
        int P = 64 * k + l, rho = P / 10, s = P - 10 * rho;
        woff[k] = (32 * ((rho >> 2) & 1) + 16 * ((rho >> 3) & 1) + 4 * ((rho >> 4) & 1) + (rho & 3)) * 3840 + 1280 + (s >= 5 ? 1200 : 0) + hg * 80 + s * 16;
      }
      const char* wbase = (const char*)wqkv + h * (320 * 3840);
#define WL(c) do { const char* b_ = wbase + CROT(c) * (64 * 3840); _Pragma("unroll") for (int k = 0; k < 10; k++) \
        glds16(b_ + (k / 5) * (8 * 3840), woff[k % 5], ldsb + L_W1 + (4 * (c) + 2 * (k / 5) + h) * 5120 + (k % 5) * 1024); } while (0)
#define WDONE2(N, c) do { asm volatile("s_waitcnt vmcnt(%0)" :: "i"(N) : "memory"); SIG_SET(F_W0 + h, (c) + 1); } while (0)
#define WDONE(N, c) do { asm volatile("s_waitcnt vmcnt(" #N ")" ::: "memory"); SIG_SET(F_W0 + h, (c) + 1); } while (0)
      WL(0);
      BARRIER();
      WDONE(0, 0);
      __builtin_amdgcn_s_setprio(0);
      WL(1);
      __builtin_amdgcn_s_setprio(3);
      STAMP(1);
      WL(2);
      STAMP(2);
      WDONE(10, 1); WL(3);
      STAMP(3);
      WDONE(10, 2); WL(4);
      STAMP(4);
      u32 qoff[3], po[7];
#pragma unroll
      for (int k = 0; k < 3; k++) {
        int P = 64 * k + l, rho = P / 6, s = P - 6 * rho; if (s > 4) s = 4;
        qoff[k] = (32 * ((rho >> 2) & 1) + 16 * ((rho >> 3) & 1) + 4 * ((rho >> 4) & 1) + (rho & 3)) * 3840 + hg * 80 + s * 16;
      }
#pragma unroll
      for (int k = 0; k < 7; k++) { const int qq = slot * 100 + h * 50 + (l >> 3) + (k < 6 ? 8 * k : 42), row = qq / 5, cc_ = qq - 5 * row; po[k] = row * 1280 + (xcd & 1) * 640 + cc_ * 128 + (l & 7) * 16; }
      WDONE(10, 3);
      u32x4 wr_[30];
#define OROT(k) (2 * CROT((k) / 6) + ((k) / 3 & 1))
#pragma unroll
      for (int k = 0; k < 30; k++) {
        asm volatile("global_load_dwordx4 %0, %1, %2" : "=v"(wr_[k]) : "v"(qoff[k % 3]), "s"(wbase + CROT(k / 6) * (64 * 3840) + ((k / 3) & 1) * (8 * 3840)));
        if (k == QW4) { WDONE2(QW4 + 1, 4); STAMP(5); }
      }
#pragma unroll
      for (int k = 0; k < 7; k++) glds16(wo, po[k], ldsb + L_PF);
      STAMP(6);
      SIG_WAIT(F_P1, 9);
      asm volatile("s_waitcnt vmcnt(7)" ::: "memory");
      STAMP(7);
#pragma unroll
      for (int k = 0; k < 30; k++) {
        asm volatile("" : "+v"(wr_[k]));
        *(LAS u32x4*)(sm + L_WQ + (2 * (k / 3) + h) * 3072 + (k % 3) * 1024 + l * 16) = wr_[k];
      }
      SIG_SET(F_Q0 + h, 1);
      STAMP(8);
      {
        f32x4 cq[3];
#pragma unroll
        for (int t = 0; t < 3; t++) cq[t] = f32x4{0, 0, 0, 0};
        LAS char* wqp = sm + L_WQ + h * 3072 + (4 * g + q4) * 96 + 8 * p4;
#pragma unroll
        for (int k = 0; k < 10; k++)
#pragma unroll
          for (int t = 0; t < 3; t++) cq[t] = mfma32(ONES, trd2(wqp + k * 6144 + 32 * t, 1536), cq[t]);
#pragma unroll
        for (int t = 0; t < 3; t++) *(LAS float*)(sm + L_CSQ + (48 * h + 16 * t + i16) * 4) = cq[t][0];
        SIG_ADD(F_CSQ);
        SIG_ADD(F_P2);
      }
      u32 ooff[5];
#pragma unroll
      for (int k = 0; k < 5; k++) { int P = 64 * k + l, rho = P / 10, s = P - 10 * rho; ooff[k] = (8 * ((rho >> 2) & 3) + 4 * ((rho >> 4) & 1) + (rho & 3)) * 1280 + cs2 * 160 + s * 16; }
#define OKS(n) (2 * CROT((n) >> 1) + ((n) & 1))
#pragma unroll
      for (int k = 0; k < 30; k++)
        asm volatile("global_load_dwordx4 %0, %1, %2" : "=v"(wr_[k]) : "v"(ooff[k % 5]), "s"((const char*)wo + (10 * h + OKS(k / 5)) * 40960));
      STAMP(9);
      SIG_WAIT(h ? F_KR : F_P2, h ? 8 : 10);
      STAMP(10);
      __builtin_amdgcn_s_setprio(0);
#pragma unroll
      for (int k = 30; k < 50; k++) glds16((const char*)wo + (10 * h + OKS(k / 5)) * 40960, ooff[k % 5], ldsb + (h ? L_WO1 : L_WO0) + OKS(k / 5) * 5120 + (k % 5) * 1024);
      asm volatile("s_waitcnt vmcnt(20)" ::: "memory");
#pragma unroll
      for (int k = 0; k < 30; k++) {
        asm volatile("" : "+v"(wr_[k]));
        *(LAS u32x4*)(sm + (h ? L_WO1 : L_WO0) + OKS(k / 5) * 5120 + (k % 5) * 1024 + l * 16) = wr_[k];
      }
      asm volatile("s_waitcnt vmcnt(0)" ::: "memory");
      SIG_ADD(F_WO);
      STAMP(12);
      if (h == 1) {
        __builtin_amdgcn_s_setprio(3);
        SIG_WAIT(F_IDX, 1);
        if ((((int)*(volatile LAS u32*)(sm + L_SYN + F_IDX * 4) - 1) >> 6) == rt * 2 + hh) {
          SIG_WAIT(F_YG, 1);
          const float sig = *(volatile LAS float*)(sm + L_IDX);
          YSUM(1, sig);
        }
      }
    } else {
      const int e = h - 2;
#define STATS_LD() u32x4 sx[5], sw[5]; { const u32 o = (4 * e + g) * 1280 + i16 * 16, o2 = i16 * 16; \
          _Pragma("unroll") for (int k = 0; k < 5; k++) { asm volatile("global_load_dwordx4 %0, %1, %2 offset:%3" : "=v"(sx[k]) : "v"(o), "s"(x + wgi * 8 * 640), "i"(k * 256)); \
            asm volatile("global_load_dwordx4 %0, %1, %2 offset:%3" : "=v"(sw[k]) : "v"(o2), "s"(wr), "i"(k * 256)); } }
#define STATS() do { _Pragma("unroll") for (int k = 0; k < 5; k++) asm volatile("" : "+v"(sx[k]), "+v"(sw[k])); \
          float s = 0.f, q = 0.f; \
          _Pragma("unroll") for (int k = 0; k < 20; k++) { s = dot2(sx[k >> 2][k & 3], 0x3F803F80u, s); q = dot2(sx[k >> 2][k & 3], sx[k >> 2][k & 3], q); } \
          const float mean = row_sum(s) * (1.f / 640.f); \
          const float var = fmaxf(row_sum(q) * (1.f / 640.f) - mean * mean, 0.f); \
          const float mb = rbf(mean), sb = rbf(__builtin_amdgcn_sqrtf(rbf(rbf(var) + 1e-5f))), rsb = rcp(sb); \
          const u32 ms = pk2(mb, sb); const int srow = wgi * 8 + 4 * e + g; \
          if (i16 == 0) ST4((u32x4{ms, tag_lo, ms, tag_hi}), WS_ST + srow * 16); \
          float r = 0.f; \
          _Pragma("unroll") for (int k = 0; k < 20; k++) { const u32 xd = sx[k >> 2][k & 3]; \
            u32 d = pk2(bflo(xd) - mb, bfhi(xd) - mb), n = pk2(bflo(d) * rsb, bfhi(d) * rsb); r = dot2(n, sw[k >> 2][k & 3], r); } \
          r = row_sum(r); \
          const u32 rb = pk2(r, 0.f) & 0xFFFFu; \
          const u32 ord = (rb & 0x8000u) ? (~rb & 0xFFFFu) : (rb | 0x8000u); \
          if (i16 == 0) *(LAS u32x2*)(sm + L_KEY + (4 * e + g) * 8) = u32x2{(ord << 16) | (u32)(0x7FFF - srow), ms}; \
          SIG_ADD(F_ST); } while (0)
      if (e == 1) {
        LOADEP();
        STATS_LD();
        BARRIER();
        if (l < 14) ST4((u32x4{SENT, SENT, SENT, SENT}), reg + R_KVR + l * 16);
        asm volatile("s_waitcnt vmcnt(1)" : "+v"(ep) :: "memory");
        GETTAG();
        STAMP(1);
        STATS();
        {
          SIG_WAIT(F_ST, 2);
          u32x2 kk = *(LAS u32x2*)(sm + L_KEY + (l & 7) * 8);
          u32 km = row_max(kk[0]);
          if (l < 8 && kk[0] == km) ST4((u32x4{kk[0], tag_lo, kk[1], tag_hi}), WS_REC + b * 16);
        }
        STAMP(2);
        u32x4 s0 = {0, 0, 0, 0}, s1 = {0, 0, 0, 0};
        const u32 sta = WS_ST + (rt * 128 + l) * 16;
#define LDST() asm volatile("global_load_dwordx4 %0, %2, %3 sc1\n\tglobal_load_dwordx4 %1, %2, %3 offset:1024 sc1\n\ts_waitcnt vmcnt(0)" : "=&v"(s0), "=&v"(s1) : "v"(sta), "s"(ws) : "memory")
        {
          f32x4 cs[5];
#pragma unroll
          for (int t = 0; t < 5; t++) cs[t] = f32x4{0, 0, 0, 0};
          LAS char* w1p = sm + L_W1 + (4 * g + q4) * 160 + 8 * p4;
          SIG_WAIT2(F_W0, 1, F_W1, 1);
#pragma unroll
          for (int k = 0; k < 4; k++)
#pragma unroll
            for (int t = 0; t < 5; t++) cs[t] = mfma32(ONES, trd2(w1p + k * 5120 + 32 * t, 2560), cs[t]);
          SIG_ADD(F_P1);
#pragma unroll
          for (int t = 0; t < 5; t++) *(LAS float*)(sm + L_CSP + (16 * t + i16) * 4) = cs[t][0];
          __builtin_amdgcn_wave_barrier();
          const u32x2 pv = *(LAS u32x2*)(sm + L_CSP + (l < 40 ? l : 0) * 8);
          if (l < 40) ST4((u32x4{pv[0], tag_lo, pv[1], tag_hi}), reg + R_CS + l * 16);
        }
        STAMP(3);
        SIG_WAIT2(F_W0, CLR_AT + 1, F_W1, CLR_AT + 1);
#pragma unroll
        for (int k = 0; k < 7; k++) ST4((u32x4{SENT, SENT, SENT, SENT}), (k * 64 + l < 420) ? reg + R_KVP + (k * 64 + l) * 16 : WS_END);
        SIG_WAIT2(F_W0, ST_AT + 1, F_W1, ST_AT + 1);
        {
          u32x4 cp[5];
          const u32 ca = WS_REG + ((rt < 15 ? rt / 5 : 2) * 80 + hg) * RS + R_CS + (l < 40 ? l : 0) * 16;
#pragma unroll 1
          for (u32 spin = 0; spin < SPIN_CAP; spin++) {
            MEMFENCE();
#pragma unroll
            for (int k = 0; k < 5; k++) cp[k] = __builtin_amdgcn_raw_buffer_load_b128(wsr, ca, k * 16 * RS, 16);
            LDST();
            bool ok = s0[1] == tag_lo && s0[3] == tag_hi && s0[0] == s0[2] && s1[1] == tag_lo && s1[3] == tag_hi && s1[0] == s1[2];
#pragma unroll
            for (int k = 0; k < 5; k++) ok = ok && cp[k][1] == tag_lo && cp[k][3] == tag_hi;
            if (__all(ok)) break;
            __builtin_amdgcn_s_sleep(6);
          }
          float c0 = 0.f, c1 = 0.f;
#pragma unroll
          for (int k = 0; k < 5; k++) { c0 += __uint_as_float(cp[k][0]); c1 += __uint_as_float(cp[k][2]); }
          if (l < 40) *(LAS f32x2*)(sm + L_CSP + l * 8) = f32x2{c0, c1};
        }
        *(LAS u32*)(sm + L_MS + l * 4) = s0[0];
        *(LAS u32*)(sm + L_MS + 256 + l * 4) = s1[0];
        SIG_SET(F_CS, 2);
        STAMP(4);
        asm volatile("s_waitcnt vmcnt(0)" ::: "memory");
        SIG_ADD(F_PHI);
        if (l < 16) ST2((u32x2{tag_lo, tag_hi}), WS_REG + (l * 16 + hg) * RS + R_I1 + rt * 8);
        STAMP(11);
        SIG_WAIT(F_PHI, 2);
        STAMP(5);
#define HINT(addr) do { _Pragma("unroll 1") for (u32 spin = 0; spin < SPIN_CAP; spin++) { u32x4 f_ = ld4w(ws, (addr) + (l & 7) * 16); \
          if (__all(f_[0] == tag_lo && f_[1] == tag_hi && f_[2] == tag_lo && f_[3] == tag_hi)) break; __builtin_amdgcn_s_sleep(4); } } while (0)
        HINT(reg + R_I1);
        SIG_WAIT(F_KR, 8);
        __builtin_amdgcn_s_sleep(H1D);
        STAMP(6);
        {
          const int he = l >= 28 ? l - 28 : l;
          const bool ok = (l < 56) && (rt < 15);
          const u32 h1a = ok ? WS_REG + ((l >= 28 ? 128 : 0) + hg) * RS + R_KVP + (28 * rt + he) * 16 : WS_END;
          u32x4 h1[8];
#pragma unroll 1
          for (u32 spin = 0; spin < SPIN_CAP; spin++) {
            MEMFENCE();
            u32 m = 0;
#pragma unroll
            for (int k = 0; k < 8; k++) h1[k] = __builtin_amdgcn_raw_buffer_load_b128(wsr, h1a, k * 16 * RS, 16);
#pragma unroll
            for (int k = 0; k < 8; k++) m = umax_(m, max4(h1[k]));
            if (__all(m != SENT)) break;
            __builtin_amdgcn_s_sleep(H1S);
          }
          f32x4 s = __builtin_bit_cast(f32x4, h1[0]);
#pragma unroll
          for (int k = 1; k < 8; k++) s += __builtin_bit_cast(f32x4, h1[k]);
#pragma unroll
          for (int r = 0; r < 4; r++) s[r] += __shfl(s[r], (l + 28) & 63);
          if (ok && l < 28) ST2((u32x2{san(pk2(s[0], s[1])), san(pk2(s[2], s[3]))}), reg + R_KVR + l * 8);
        }
        STAMP(7);
        {
          const int hpr = l / 14, e2 = l - 14 * hpr;
          const u32 h2a = WS_REG + (hpr * 16 + hg) * RS + R_KVR + e2 * 16;
          u32 ka[8];
#pragma unroll
          for (int k = 0; k < 8; k++) { int it = 112 * (k >> 1) + 2 * l + (k & 1), hf = it / 21, c = it - 21 * hf; ka[k] = L_KVT + c * 80 + hf * 8 + (hf >= 10 ? 1600 : 0); }
          u32x4 kv[4];
#pragma unroll 1
          for (u32 spin = 0; spin < SPIN_CAP; spin++) {
            MEMFENCE();
            u32 m = 0;
#pragma unroll
            for (int k = 0; k < 4; k++) kv[k] = LD4((l < 56 && 4 * k + hpr < 15) ? h2a + k * 64 * RS : WS_END);
#pragma unroll
            for (int k = 0; k < 4; k++) m = umax_(m, max4(kv[k]));
            if (__all(m != SENT)) break;
            __builtin_amdgcn_s_sleep(H2S);
          }
#pragma unroll
          for (int k = 0; k < 4; k++) if (l < 56 && 4 * k + hpr < 15) {
            *(LAS u32x2*)(sm + ka[2 * k]) = u32x2{kv[k][0], kv[k][1]};
            *(LAS u32x2*)(sm + ka[2 * k + 1]) = u32x2{kv[k][2], kv[k][3]};
          }
        }
        SIG_SET(F_KVT, 1);
        STAMP(8);
        SIG_WAIT(F_OS, 8);
        if (l < 16) ST2((u32x2{tag_lo, tag_hi}), WS_REG + (rt * 16 + l) * RS + R_I3 + hg * FS);
        STAMP(9);
#pragma unroll 1
        for (u32 spin = 0; spin < SPIN_CAP; spin++) {
          u32x2 f_ = ld2w(ws, reg + R_I3 + i16 * FS);
          const u64 okm = __ballot(f_[0] == tag_lo && f_[1] == tag_hi);
          if ((okm & 0xFF) == 0xFF) SIG_SET(F_O, 1);
          if ((okm & 0xFF00) == 0xFF00) SIG_SET(F_O1, 1);
          if ((okm & 0xFFFF) == 0xFFFF) break;
        }
        SIG_SET(F_O, 1); SIG_SET(F_O1, 1);
        STAMP(10);
      } else {
        LOADEP();
        STATS_LD();
        if (fast) {
          const u32 o = ((slot >> 1) * 40 + (l >> 3)) * 3840 + (xcd & 1) * 640 + 256 + (l & 7) * 16;
#pragma unroll
          for (int k = 0; k < 10; k++) glds16((const char*)wqkv + (k % 5) * (8 * 3840) + 1280 * (1 + k / 5), o, ldsb + L_PF);
        }
        u32 wp[7];
#pragma unroll
        for (int k = 0; k < 7; k++) { const u32 o = hg * 1600 + (l + 64 * k < 400 ? l + 64 * k : 0) * 4; asm volatile("global_load_dword %0, %1, %2" : "=v"(wp[k]) : "v"(o), "s"(wphi)); }
        BARRIER();
        if (l < 4) *(LAS u32x4*)(sm + L_ZERO + l * 16) = u32x4{0, 0, 0, 0};
        if (l < 40) { *(LAS u32*)(sm + L_WPHI + l * 48 + 20) = 0u; *(LAS u32*)(sm + L_WPHI + l * 48 + 44) = 0u; }
        asm volatile("s_waitcnt vmcnt(0)" : "+v"(ep), "+v"(wp[0]), "+v"(wp[1]), "+v"(wp[2]), "+v"(wp[3]), "+v"(wp[4]), "+v"(wp[5]), "+v"(wp[6]) :: "memory");
        GETTAG();
        STATS();
#pragma unroll
        for (int k = 0; k < 7; k++) { const int u = l + 64 * k, row = u / 10, z = u - 10 * row; if (u < 400) *(LAS u32*)(sm + L_WPHI + row * 48 + z * 4 + (z >= 5 ? 4 : 0)) = wp[k]; }
        SIG_SET(F_WP, 1);
        STAMP(1);
        if (fast) {
          const u32 o = ((slot >> 1) * 40 + (l >> 3)) * 3840 + (xcd & 1) * 640 + 256 + (l & 7) * 16;
#pragma unroll
          for (int k = 0; k < 5; k++) glds16((const char*)wqkv + k * (8 * 3840), o, ldsb + L_PF);
        }
        const int fi = wgi >> 1, cb = fi < 120 ? fi / 10 : 0, ksp = fi < 120 ? fi - 10 * cb : 0;
        if (fast) {
          SIG_WAIT(F_W0, REC_AT);
          const u32x4 S4 = {SENT, SENT, SENT, SENT};
          ST4(S4, WS_MG + fi * MGS + l * 16);
        }
        else {
          SIG_WAIT(F_W0, 5);
#pragma unroll
          for (int zz = 0; zz < 3; zz++) { const int u = 64 * zz + l, sl = u / 10; ST4((u32x4{SENT, SENT, SENT, SENT}), u < 160 ? WS_YB + (sl * 128 + fi) * 160 + (u - 10 * sl) * 16 : WS_END); }
        }
        u32x2 rec;
        {
          u32 km = 0, ms = 0;
          u32x4 r_[4];
#pragma unroll 1
          for (u32 spin = 0; spin < SPIN_CAP; spin++) {
            const u32 ra = WS_REC + l * 16;
            asm volatile("global_load_dwordx4 %0, %4, %5 sc1\n\tglobal_load_dwordx4 %1, %4, %5 offset:1024 sc1\n\tglobal_load_dwordx4 %2, %4, %5 offset:2048 sc1\n\t"
                         "global_load_dwordx4 %3, %4, %5 offset:3072 sc1\n\ts_waitcnt vmcnt(0)"
                         : "=&v"(r_[0]), "=&v"(r_[1]), "=&v"(r_[2]), "=&v"(r_[3]) : "v"(ra), "s"(ws) : "memory");
            bool ok = true;
#pragma unroll
            for (int k = 0; k < 4; k++) ok = ok && r_[k][1] == tag_lo && r_[k][3] == tag_hi;
            if (__all(ok)) break;
            __builtin_amdgcn_s_sleep(16);
          }
#pragma unroll
          for (int k = 0; k < 4; k++) if (r_[k][0] > km) { km = r_[k][0]; ms = r_[k][2]; }
          u32 kx = row_max(km);
          { auto r = __builtin_amdgcn_permlane16_swap(kx, kx, false, false); kx = umax_(r[0], r[1]); }
          { auto r = __builtin_amdgcn_permlane32_swap(kx, kx, false, false); kx = umax_(r[0], r[1]); }
          const int src = __builtin_ctzll(__ballot(km == kx));
          rec = u32x2{(u32)__builtin_amdgcn_readfirstlane(kx), (u32)__builtin_amdgcn_readlane(ms, src)};
        }
        const int idx = (0x7FFF - (rec[0] & 0xFFFFu)) & 2047;
        if (b == 0 && l == 0) ST2((u32x2{tag_lo, ep_hi}), WS_EPOCH);
        SIG_SET(F_IDX, idx + 1);
        STAMP(4);
        const u32 mreg = WS_MG + fi * MGS;
        if (fast) {
          {
            const int c0 = (cb % 6) * 256, d = c0 / 12 + l;
            if (fi < 120 && 12 * d < c0 + 256) ST2((u32x2{tag_lo, tag_hi}), WS_MG + d * MGS + 1024 + ((12 * d < c0 ? 20 : 0) + (cb / 6) * 10 + ksp) * FS);
          }
          u32x2 xr;
          { const u32 xo = (64 * ksp) * 2 + i16 * 8; asm volatile("global_load_dwordx2 %0, %1, %2" : "=v"(xr) : "v"(xo), "s"(x + idx * 640)); }
          u32x4 gw[32];
          __builtin_amdgcn_s_setprio(0);
          SIG_WAIT(GW_SIG, GW_AT);
          STAMP(2);
          {
            const u16* gm = cb < 6 ? wg + cb * 256 : wu + (cb - 6) * 256;
            const u32 go = (64 * ksp + 2 * (l >> 5)) * 3072 + (l & 31) * 16;
#pragma unroll
            for (int k = 0; k < 32; k++) asm volatile("global_load_dwordx4 %0, %1, %2 offset:%3" : "=v"(gw[k]) : "v"(go), "s"(gm + (k >> 1) * 4 * 1536), "i"((k & 1) * 3072));
          }
          __builtin_amdgcn_s_setprio(MPRIO);
          STAMP(3);
          {
            asm volatile("s_waitcnt vmcnt(16)" : "+v"(xr) :: "memory");
            const float sel_m = bflo(rec[1]), sel_r = rcp(bfhi(rec[1]));
            u32 xp = (l >> 5) ? xr[1] : xr[0];
            { u32 d = pk2(bflo(xp) - sel_m, bfhi(xp) - sel_m); xp = pk2(bflo(d) * sel_r, bfhi(d) * sel_r); }
            float v[8];
#pragma unroll
            for (int k = 0; k < 8; k++) v[k] = 0.f;
#define M1ROW(r) do { asm volatile("" : "+v"(gw[2 * r]), "+v"(gw[2 * r + 1])); const u32 a = dppu<0x150 + r>(xp); \
              _Pragma("unroll") for (int k = 0; k < 4; k++) { v[2 * k] = dot2(__builtin_amdgcn_perm(gw[2 * r + 1][k], gw[2 * r][k], 0x05040100u), a, v[2 * k]); \
                v[2 * k + 1] = dot2(__builtin_amdgcn_perm(gw[2 * r + 1][k], gw[2 * r][k], 0x07060302u), a, v[2 * k + 1]); } } while (0)
            M1ROW(0); M1ROW(1); M1ROW(2); M1ROW(3); M1ROW(4); M1ROW(5); M1ROW(6); M1ROW(7);
            asm volatile("s_waitcnt vmcnt(0)" ::: "memory");
            STAMP(5);
            M1ROW(8); M1ROW(9); M1ROW(10); M1ROW(11); M1ROW(12); M1ROW(13); M1ROW(14); M1ROW(15);
            u32x4 u4;
#pragma unroll
            for (int k = 0; k < 4; k++) {
              auto r = __builtin_amdgcn_permlane32_swap(__float_as_uint(v[k]), __float_as_uint(v[k + 4]), false, false);
              u4[k] = __float_as_uint(__uint_as_float(r[0]) + __uint_as_float(r[1]));
            }
            ST4(san4(u4), fi < 120 ? mreg + (l & 31) * 32 + (l >> 5) * 16 : WS_END);
          }
          __builtin_amdgcn_s_setprio(3);
          STAMP(6);
        } else {
          if (l < 16) ST2((u32x2{tag_lo, tag_hi}), WS_IY + (l * 128 + fi) * FS);
          u32x2 dw[36];
          {
            const u16* dp = wd + (12 * fi) * 640;
            const u32 dof = l * 8, dof2 = (l < 32 ? l : 0) * 8;
#pragma unroll
            for (int k = 0; k < 12; k++) {
              asm volatile("global_load_dwordx2 %0, %1, %2" : "=v"(dw[3 * k]) : "v"(dof), "s"(dp + k * 640));
              asm volatile("global_load_dwordx2 %0, %1, %2 offset:512" : "=v"(dw[3 * k + 1]) : "v"(dof), "s"(dp + k * 640));
              asm volatile("global_load_dwordx2 %0, %1, %2 offset:1024" : "=v"(dw[3 * k + 2]) : "v"(dof2), "s"(dp + k * 640));
            }
          }
          STAMP(7);
          {
            const int nsl = ((12 * fi) >> 8) == ((12 * fi + 11) >> 8) ? 20 : 40;
#pragma unroll 1
            for (u32 spin = 0; spin < SPIN_CAP; spin++) {
              u32x2 f0 = ld2w(ws, mreg + 1024 + (l < nsl ? l : 0) * FS);
              if (__all(f0[0] == tag_lo && f0[1] == tag_hi)) break;
              __builtin_amdgcn_s_sleep(6);
            }
          }
          SIG_WAIT(M2_SIG, M2_AT);
          STAMP(8);
          {
            const int kw = l / 6, pr = l - 6 * kw, col = 12 * fi + 2 * pr;
            const u32 ga = l < 60 ? WS_MG + ((col >> 8) * 10 + kw) * MGS + (col & 255) * 4 : WS_END;
            u32x2 g2, u2;
#pragma unroll 1
            for (u32 spin = 0; spin < SPIN_CAP; spin++) {
              MEMFENCE();
              g2 = LD2(ga); u2 = __builtin_amdgcn_raw_buffer_load_b64(wsr, ga, 60 * MGS, 16);
              if (__all(umax_(umax_(g2[0], g2[1]), umax_(u2[0], u2[1])) != SENT)) break;
              __builtin_amdgcn_s_sleep(8);
            }
            STAMP(15);
            f32x2 gg2 = __builtin_bit_cast(f32x2, g2), uu2 = __builtin_bit_cast(f32x2, u2);
            float z[4] = {gg2[0], gg2[1], uu2[0], uu2[1]};
#pragma unroll
            for (int k = 0; k < 4; k++) {
              const float z0 = z[k] + __shfl(z[k], (l + 30) & 63);
              z[k] = rbf(z0 + __shfl(z0, (l + 6) & 63) + __shfl(z0, (l + 12) & 63) + __shfl(z0, (l + 18) & 63) + __shfl(z0, (l + 24) & 63));
            }
            const u32 hp2 = pk2(rbf(z[0] * rcp(1.f + __expf(-z[0]))) * z[2], rbf(z[1] * rcp(1.f + __expf(-z[1]))) * z[3]);
#pragma unroll
            for (int k = 0; k < 36; k++) asm volatile("" : "+v"(dw[k]));
#pragma unroll
            for (int zz = 0; zz < 3; zz++) {
              f32x4 y = {0, 0, 0, 0};
#pragma unroll
              for (int i = 0; i < 6; i++) {
                const u32 hh = __builtin_amdgcn_readlane(hp2, i);
                const u32x2 d0 = dw[6 * i + zz], d1 = dw[6 * i + 3 + zz];
                y[0] = dot2(__builtin_amdgcn_perm(d1[0], d0[0], 0x05040100u), hh, y[0]); y[1] = dot2(__builtin_amdgcn_perm(d1[0], d0[0], 0x07060302u), hh, y[1]);
                y[2] = dot2(__builtin_amdgcn_perm(d1[1], d0[1], 0x05040100u), hh, y[2]); y[3] = dot2(__builtin_amdgcn_perm(d1[1], d0[1], 0x07060302u), hh, y[3]);
              }
              { const int u = 64 * zz + l, sl = u / 10; ST4(san4(__builtin_bit_cast(u32x4, y)), u < 160 ? WS_YB + (sl * 128 + fi) * 160 + (u - 10 * sl) * 16 : WS_END); }
            }
          }
        }
        STAMP(9);
        if ((idx >> 6) == rt * 2 + hh) {
          SIG_WAIT(F_PHI, 9);
          STAMP(11);
#pragma unroll 1
          for (u32 spin = 0; spin < SPIN_CAP; spin++) {
            __builtin_amdgcn_s_sleep(6);
            u32x2 f0, f1; const u32 fa_ = WS_IY + (hg * 128 + l) * FS, fb_ = fa_ + 64 * FS;
            asm volatile("global_load_dwordx2 %0, %2, %4 sc1\n\tglobal_load_dwordx2 %1, %3, %4 sc1\n\ts_waitcnt vmcnt(0)" : "=&v"(f0), "=&v"(f1) : "v"(fa_), "v"(fb_), "s"(ws) : "memory");
            if (__all(f0[0] == tag_lo && f0[1] == tag_hi && f1[0] == tag_lo && f1[1] == tag_hi)) break;
          }
          STAMP(12);
          u32 rb = rec[0] >> 16;
          rb = (rb & 0x8000u) ? (rb & 0x7FFFu) : (~rb & 0xFFFFu);
          const float sig = rbf(rcp(1.f + __expf(-bflo(rb))));
          *(LAS float*)(sm + L_IDX) = sig;
          SIG_WAIT(M3_SIG, M3_AT);
          SIG_SET(F_YG, 1);
          YSUM(0, sig);
        }
        SIG_SET(F_Y, idx + 1);
        STAMP(10);
      }
    }
  } else {
    const int tg = w >> 1, kh = w & 1, tw = w;
    u32x4 xf[20];
    const char* xb = (const char*)x + rt * (128 * 1280) + kh * 640;
    const u32 xoff = (u32)((((g >> 1) ? (tw ^ 1) : tw) * 16 + 4 * q4) * 1280 + (g & 1) * 64 + p4 * 16);
#define XI(c, r) ((r) < 2 ? 2 * (c) + (r) : 8 + 2 * (c) + (r))
#define XL1(c, r) asm volatile("global_load_dwordx4 %0, %1, %2 offset:%3" : "=v"(xf[XI(c, r)]) : "v"(xoff), "s"(xb + CROT(c) * 128), "i"((r) * 1280))
#define XG(c) do { XL1(c, 0); XL1(c, 1); XL1(c, 2); XL1(c, 3); } while (0)
    __builtin_amdgcn_s_setprio(2);
    XG(0);
    BARRIER();
    XG(1); XG(2); XG(3); XG(4);
    __builtin_amdgcn_s_setprio(0);
    STAMP(1);
    f32x4 acc[2][5];
#pragma unroll
    for (int t = 0; t < 5; t++) { acc[0][t] = f32x4{0, 0, 0, 0}; acc[1][t] = f32x4{0, 0, 0, 0}; }
    LAS char* w1p = sm + L_W1 + kh * 5120 + (4 * g + q4) * 160 + 8 * p4;
    const u64 mE = 0x5555555555555555ull, mO = ~mE, mL = 0x3333333333333333ull, mH = ~mL;
    u32 ta[4], ua[4];
#define DPX1 " quad_perm:[1,0,3,2] row_mask:0xf bank_mask:0xf\n\t"
#define DPX2 " quad_perm:[2,3,0,1] row_mask:0xf bank_mask:0xf\n\t"
#define CD2(M, X, d0, s0, k0, d1, s1, k1) asm volatile("s_mov_b64 vcc, %6\n\tv_cndmask_b32_dpp %0, %2, %3, vcc" X "v_cndmask_b32_dpp %1, %4, %5, vcc" X \
      : "=&v"(d0), "=&v"(d1) : "v"(s0), "v"(k0), "v"(s1), "v"(k1), "s"(M) : "vcc")
#define CD2I(M, X, d0, s0, d1, s1) asm volatile("s_mov_b64 vcc, %4\n\tv_cndmask_b32_dpp %0, %2, %0, vcc" X "v_cndmask_b32_dpp %1, %3, %1, vcc" X \
      : "+v"(d0), "+v"(d1) : "v"(s0), "v"(s1), "s"(M) : "vcc")
#define A_(c, r) xf[XI(c, r)]
#define TP(c, k) do { const int d_ = (k) & 3; u32 n0, n1; \
      if ((k) < 4) { CD2(mE, DPX1, n0, A_(c, 1)[d_], A_(c, 0)[d_], n1, A_(c, 3)[d_], A_(c, 2)[d_]); ua[d_] = n0; ta[d_] = n1; } \
      else if ((k) < 8) { n0 = A_(c, 1)[d_]; n1 = A_(c, 3)[d_]; CD2I(mO, DPX1, n0, A_(c, 0)[d_], n1, A_(c, 2)[d_]); A_(c, 1)[d_] = n0; A_(c, 3)[d_] = n1; } \
      else if ((k) < 12) { CD2(mL, DPX2, n0, ta[d_], ua[d_], n1, A_(c, 3)[d_], A_(c, 1)[d_]); A_(c, 0)[d_] = n0; A_(c, 2)[d_] = n1; } \
      else if ((k) < 16) { n1 = A_(c, 3)[d_]; CD2I(mH, DPX2, ta[d_], ua[d_], n1, A_(c, 1)[d_]); A_(c, 3)[d_] = n1; } \
      else { auto r0 = __builtin_amdgcn_permlane32_swap(A_(c, 0)[d_], ta[d_], false, false); auto r1 = __builtin_amdgcn_permlane32_swap(A_(c, 2)[d_], A_(c, 3)[d_], false, false); \
        A_(c, 0)[d_] = r0[0]; A_(c, 2)[d_] = r0[1]; A_(c, 1)[d_] = r1[0]; A_(c, 3)[d_] = r1[1]; } } while (0)
#define XW(N, c) asm volatile("s_waitcnt vmcnt(" #N ")" : "+v"(A_(c, 0)), "+v"(A_(c, 1)), "+v"(A_(c, 2)), "+v"(A_(c, 3)) :: "memory")
#define RD(F, j, r) F[r] = trd(w1p + (j) * 10240 + 32 * ((r) >> 1) + 2560 * ((r) & 1))
#define MF(F, j, k) acc[(k) & 1][(k) >> 1] = mfma32(FR(F, (k) >> 1), xf[10 * ((k) & 1) + (j)], acc[(k) & 1][(k) >> 1])
#define STEPA(FA, FB, c, V) do { _Pragma("unroll") for (int k = 0; k < 10; k++) { MF(FA, 2 * (c), k); RD(FB, 2 * (c) + 1, k); if ((c) < 4) TP((c) + 1, k); V(k); SB(); } } while (0)
#define STEPB(FA, FB, c) do { _Pragma("unroll") for (int k = 0; k < 4; k++) { MF(FB, 2 * (c) + 1, k); TP((c) + 1, 10 + k); SB(); } \
      if ((c) == 2) SIG_ADD(F_P1); \
      if ((c) == 3) SIG_WAIT2(F_W0 + kh, (c) + 2, F_WP, 1); else SIG_WAIT(F_W0 + kh, (c) + 2); \
      _Pragma("unroll") for (int k = 4; k < 10; k++) { if (k < 9) { RD(FA, 2 * (c) + 2, 2 * (k - 4)); RD(FA, 2 * (c) + 2, 2 * (k - 4) + 1); } MF(FB, 2 * (c) + 1, k); TP((c) + 1, 10 + k); SB(); } } while (0)
    {
      u32x2 fa[10], fb[10];
      XW(16, 0);
      u32 fil = l;
#pragma unroll
      for (int k = 0; k < 20; k++) { TP(0, k); asm volatile("v_add_u32 %0, 1, %0" : "+v"(fil)); SB(); }
      SIG_WAIT(F_W0 + kh, 1);
      STAMP(2);
#pragma unroll
      for (int r = 0; r < 10; r++) RD(fa, 0, r);
      SB();
#define V3(k) do { if ((k) == 1) XL1(3, 0); if ((k) == 3) XL1(3, 1); if ((k) == 5) XL1(3, 2); if ((k) == 7) XL1(3, 3); } while (0)
#define V4(k) do { if ((k) == 1) XL1(4, 0); if ((k) == 3) XL1(4, 1); if ((k) == 5) XL1(4, 2); if ((k) == 7) XL1(4, 3); } while (0)
#define VN(k) do { } while (0)
      XW(12, 1);
      STEPA(fa, fb, 0, VN); STEPB(fa, fb, 0);
      STAMP(3);
      XW(8, 2);
      STEPA(fa, fb, 1, VN); STEPB(fa, fb, 1);
      STAMP(4);
      XW(4, 3);
      STEPA(fa, fb, 2, VN); STEPB(fa, fb, 2);
      STAMP(5);
      XW(0, 4);
      STEPA(fa, fb, 3, VN); STEPB(fa, fb, 3);
      STAMP(6);
      STEPA(fa, fb, 4, VN);
#pragma unroll
      for (int k = 0; k < 10; k++) MF(fb, 9, (k + 1 - 2 * (k & 1)));
    }
    STAMP(7);
    {
    int l_ = __builtin_amdgcn_mbcnt_hi(~0u, __builtin_amdgcn_mbcnt_lo(~0u, 0u));
    asm volatile("" : "+v"(l_));
    const int l = l_, tid = w * 64 + l, g = l >> 4, i16 = l & 15, q4 = i16 >> 2, p4 = l & 3;
    {
      const int tp = tw ^ 1;
      *(LAS f32x4*)(sm + L_PHI + tp * 2560 + l * 16) = acc[1][0];
      *(LAS f32x4*)(sm + L_PHI + tp * 2560 + 1024 + l * 16) = acc[1][1];
      *(LAS f32x4*)(sm + L_V + tp * 1536 + l * 16) = acc[1][2];
      *(LAS f32x4*)(sm + L_EXA + tp * 2048 + l * 16) = acc[1][3];
      *(LAS f32x4*)(sm + L_EXA + tp * 2048 + 1024 + l * 16) = acc[1][4];
      SIG_SET(F_EX + tp, 1);
    }
    u32x4 wa32[3]; u32x2 wa16[3];
    {
      const int hp = p4 >> 1;
      const int d0 = 4 * g + q4, d1 = d0 + 16, d2 = d0 + 32;
      LAS char* a0 = sm + (hp == 0 ? L_WPHI + d0 * 48 + 24 * (p4 & 1) : L_ZERO);
      LAS char* a1 = sm + ((d1 >= 20) == (hp == 1) ? L_WPHI + d1 * 48 + 24 * (p4 & 1) : L_ZERO);
      LAS char* a2 = sm + ((hp == 1 && d2 < 40) ? L_WPHI + d2 * 48 + 24 * (p4 & 1) : L_ZERO);
#pragma unroll
      for (int mt = 0; mt < 3; mt++) {
        u32x2 e0 = trd(a0 + 8 * mt), e1 = trd(a1 + 8 * mt);
        wa32[mt] = u32x4{e0[0], e0[1], e1[0], e1[1]};
        wa16[mt] = trd(a2 + 8 * mt);
      }
    }
    SIG_WAIT2(F_EX + tw, 1, F_CS, 2);
    STAMP(8);
    const u32 msv = *(LAS u32*)(sm + L_MS + (tw * 16 + i16) * 4);
    const float m_b = bflo(msv), rs = rcp(bfhi(msv));
    f32x4 cc[5], a[5];
#pragma unroll
    for (int t = 0; t < 3; t++) cc[t] = *(LAS f32x4*)(sm + L_CSP + (16 * t + 4 * g) * 4);
    a[0] = acc[0][0] + *(LAS f32x4*)(sm + L_PHI + tw * 2560 + l * 16);
    a[1] = acc[0][1] + *(LAS f32x4*)(sm + L_PHI + tw * 2560 + 1024 + l * 16);
    a[2] = acc[0][2] + *(LAS f32x4*)(sm + L_V + tw * 1536 + l * 16);
    {
      u32 kn[5][2];
#define NRM(A, t, C, O) do { f32x4 v_ = (A - m_b * C) * rs; O[t][0] = pk2(v_[0], v_[1]); O[t][1] = pk2(v_[2], v_[3]); } while (0)
      NRM(a[0], 0, cc[0], kn); NRM(a[1], 1, cc[1], kn); NRM(a[2], 2, cc[2], kn);
      SB();
      cc[3] = *(LAS f32x4*)(sm + L_CSP + (48 + 4 * g) * 4); cc[4] = *(LAS f32x4*)(sm + L_CSP + (64 + 4 * g) * 4);
      a[3] = acc[0][3] + *(LAS f32x4*)(sm + L_EXA + tw * 2048 + l * 16);
      a[4] = acc[0][4] + *(LAS f32x4*)(sm + L_EXA + tw * 2048 + 1024 + l * 16);
      u32x4 b32 = {kn[0][0], kn[0][1], kn[1][0], kn[1][1]};
      u32x2 b16 = {kn[2][0], kn[2][1]};
      f32x4 pp[3];
#pragma unroll
      for (int mt = 0; mt < 3; mt++) {
        pp[mt] = mfma32(wa32[mt], b32, f32x4{0, 0, 0, 0});
        pp[mt] = mfma16(wa16[mt], b16, pp[mt]);
      }
      NRM(a[3], 3, cc[3], kn); NRM(a[4], 4, cc[4], kn);
      __builtin_amdgcn_wave_barrier();
      LAS char* vr = sm + L_V + (16 * tw + i16) * 96;
      u32x2 v2 = (g >= 2) ? u32x2{kn[2][0], kn[2][1]} : u32x2{g == 0 ? 0x3F80u : 0u, 0u};
      *(LAS u32x2*)(vr + (g >= 2 ? 8 * (g - 2) : 80 + 8 * g)) = v2;
      *(LAS u32x2*)(vr + 16 + 8 * g) = u32x2{kn[3][0], kn[3][1]};
      *(LAS u32x2*)(vr + 48 + 8 * g) = u32x2{kn[4][0], kn[4][1]};
      u32 ph[10];
      softmax_pack(pp[0], pp[1], pp[2], ph);
      LAS char* pr = sm + L_PHI + (16 * tw + i16) * 160 + 40 * g;
#pragma unroll
      for (int k = 0; k < 5; k++) *(LAS u32x2*)(pr + 8 * k) = u32x2{ph[2 * k], ph[2 * k + 1]};
    }
    STAMP(9);
    SIG_ADD(F_PHI);
    __builtin_amdgcn_s_setprio(2);
    {
      const int mt0 = (0x22111000 >> (4 * w)) & 3, nt0 = (0x10210210 >> (4 * w)) & 3;
      const int mt1 = (0x22144332 >> (4 * w)) & 7, nt1 = (0x10221212 >> (4 * w)) & 3;
      u32 ap0 = L_PHI + (4 * g + q4) * 160 + 8 * p4 + 32 * mt0, ap1 = ap0 + 32 * (mt1 - mt0);
      u32 bp0 = L_V + (4 * g + q4) * 96 + 8 * p4 + 32 * nt0, bp1 = bp0 + 32 * (nt1 - nt0);
#define KVA(mt, nt, en) ({ const int fq = 4 * (mt) + g, col = 16 * (nt) + i16; int c = col - (fq >= 10 ? 20 : 0); bool ok = (c >= 0 && c < 20); \
        if (col == 40) { c = 20; ok = true; } (ok && (en)) ? reg + R_KVP + (fq * 21 + c) * 16 : WS_END; })
      u32 ks0 = KVA(mt0, nt0, true), ks1 = KVA(mt1, nt1, w < 5);
      asm volatile("" : "+v"(ap0), "+v"(ap1), "+v"(bp0), "+v"(bp1), "+v"(ks0), "+v"(ks1));
      SIG_WAIT(F_PHI, 9);
      STAMP(10);
      f32x4 d0 = {0, 0, 0, 0}, d1 = {0, 0, 0, 0};
#pragma unroll
      for (int u = 0; u < 4; u++) {
        d0 = mfma32(trd2(sm + ap0 + u * 5120, 2560), trd2(sm + bp0 + u * 3072, 1536), d0);
        d1 = mfma32(trd2(sm + ap1 + u * 5120, 2560), trd2(sm + bp1 + u * 3072, 1536), d1);
      }
      ST4(san4(__builtin_bit_cast(u32x4, d0)), ks0); ST4(san4(__builtin_bit_cast(u32x4, d1)), ks1);
    }
    SIG_ADD(F_KR);
    SIG_WAIT(F_Q0 + kh, 1);
    STAMP(11);
    f32x4 aq[2][3];
#pragma unroll
    for (int t = 0; t < 3; t++) { aq[0][t] = f32x4{0, 0, 0, 0}; aq[1][t] = f32x4{0, 0, 0, 0}; }
    {
      LAS char* wqp = sm + L_WQ + kh * 3072 + (4 * g + q4) * 96 + 8 * p4;
      u32x2 fa[6], fb[6];
#define RQ(F, P_, o, r) F[r] = trd(P_ + (o) + 32 * ((r) >> 1) + 1536 * ((r) & 1))
#define STEPQ(FA, FB, j) do { _Pragma("unroll") for (int k = 0; k < 6; k++) { aq[k & 1][k >> 1] = mfma32(FR(FA, k >> 1), xf[10 * (k & 1) + (j)], aq[k & 1][k >> 1]); \
          if ((j) < 9) RQ(FB, wqp, ((j) + 1) * 6144, k); SB(); } } while (0)
#pragma unroll
      for (int r = 0; r < 6; r++) RQ(fa, wqp, 0, r);
      SB();
#pragma unroll
      for (int j = 0; j < 10; j += 2) { STEPQ(fa, fb, j); STEPQ(fb, fa, j + 1);
      }
    }
#pragma unroll
    for (int t = 0; t < 3; t++) *(LAS f32x4*)(sm + L_EXQ + (tw ^ 1) * 3072 + t * 1024 + l * 16) = aq[1][t];
    SIG_SET(F_EX + (tw ^ 1), 2);
    SIG_ADD(F_P2);
    STAMP(12);
    SIG_WAIT2(F_EX + tw, 2, F_CSQ, 2);
    STAMP(13);
    const int tt = 4 * hh + tg, og = 2 * cs2 + kh;
    const u32 slo0 = (rt * 128 + tt * 16 + l / 5) * 1280 + og * 80 + (l % 5) * 16;
    const u32 slo1 = (rt * 128 + tt * 16 + (64 + i16) / 5) * 1280 + og * 80 + ((64 + i16) % 5) * 16;
    SIG_WAIT(F_KR, 8);
    glds16(x, slo0, ldsb + L_V + tw * 1536);
    if (l < 16) glds16(x, slo1, ldsb + L_V + tw * 1536 + 1024);
    u32 pq[10];
    {
      f32x4 aqo[3], c2[3];
      u32 qn[3][2];
#pragma unroll
      for (int t = 0; t < 3; t++) {
        c2[t] = *(LAS f32x4*)(sm + L_CSQ + (16 * t + 4 * g) * 4) + *(LAS f32x4*)(sm + L_CSQ + (48 + 16 * t + 4 * g) * 4);
        aqo[t] = aq[0][t] + *(LAS f32x4*)(sm + L_EXQ + tw * 3072 + t * 1024 + l * 16);
        NRM(aqo[t], t, c2[t], qn);
      }
      u32x4 b32 = {qn[0][0], qn[0][1], qn[1][0], qn[1][1]};
      u32x2 b16 = {qn[2][0], qn[2][1]};
      f32x4 pp[3];
#pragma unroll
      for (int mt = 0; mt < 3; mt++) {
        pp[mt] = mfma32(wa32[mt], b32, f32x4{0, 0, 0, 0});
        pp[mt] = mfma16(wa16[mt], b16, pp[mt]);
      }
      softmax_pack(pp[0], pp[1], pp[2], pq);
#pragma unroll
      for (int k = 0; k < 10; k++) asm volatile("" : "+v"(pq[k]));
    }
    STAMP(14);
    u32 oad[3];
#pragma unroll
    for (int mt = 0; mt < 3; mt++) {
      const int col = mt < 2 ? 8 * q4 + 4 * mt + p4 : 32 + 2 * q4 + p4;
      const int hp = (mt == 2) ? (p4 == 2 ? 0 : 1) : (col >= 20 ? 1 : 0);
      const int row = (mt == 2 && p4 >= 2) ? hp * 21 + 20 : col + hp;
      oad[mt] = ((g >> 1) == hp) ? (u32)(L_KVT + row * 80 + 40 * (g & 1)) : L_ZERO;
      asm volatile("" : "+v"(oad[mt]));
    }
    STAMP(15);
    SIG_WAIT(F_KVT, 1);
    STAMP(16);
    {
      f32x4 oo[3];
#pragma unroll
      for (int mt = 0; mt < 3; mt++) {
        LAS char* ap = sm + oad[mt];
        u32x2 a0 = *(LAS u32x2*)(ap), a1 = *(LAS u32x2*)(ap + 8), a2 = *(LAS u32x2*)(ap + 16), a3 = *(LAS u32x2*)(ap + 24), a4 = *(LAS u32x2*)(ap + 32);
        oo[mt] = mfma32(u32x4{a0[0], a0[1], a1[0], a1[1]}, u32x4{pq[0], pq[1], pq[2], pq[3]}, f32x4{0, 0, 0, 0});
        oo[mt] = mfma32(u32x4{a2[0], a2[1], a3[0], a3[1]}, u32x4{pq[4], pq[5], pq[6], pq[7]}, oo[mt]);
        oo[mt] = mfma16(a4, u32x2{pq[8], pq[9]}, oo[mt]);
      }
      const float r0 = rcp(oo[2][2] + 1e-6f), r1 = rcp(oo[2][3] + 1e-6f);
      const float ra_ = g <= 2 ? r0 : r1, rb_ = g <= 1 ? r0 : r1;
      u32x4 o8 = {pk2(oo[0][0] * ra_, oo[0][1] * ra_), pk2(oo[0][2] * ra_, oo[0][3] * ra_), pk2(oo[1][0] * rb_, oo[1][1] * rb_), pk2(oo[1][2] * rb_, oo[1][3] * rb_)};
      u32 o2 = pk2(oo[2][0] * r1, oo[2][1] * r1);
      const u32 ob = reg + R_OB + tw * 1280 + l * 16;
      ST4(o8, ob);
      auto ra = __builtin_amdgcn_permlane32_swap(o2, o2, false, false);
      auto rb = __builtin_amdgcn_permlane16_swap(ra[0], ra[0], false, false);
      auto rc2 = __builtin_amdgcn_permlane16_swap(ra[1], ra[1], false, false);
      if (g == 0) ST4((u32x4{rb[0], rb[1], rc2[0], rc2[1]}), ob + 1024);
      asm volatile("s_waitcnt vmcnt(0)" ::: "memory");
      SIG_ADD(F_OS);
    }
    MEMFENCE();
    STAMP(17);
    u32x2 xv[3], xnp[3];
    const bool mid = (g >> 1) == kh;
    const u32 xo0 = 16 * kh + 8 * g, xo2 = mid ? (kh ? 8 * (g - 2) : 64 + 8 * g) : 0;
    LAS char* xp = sm + L_V + tw * 1536 + i16 * 80;
    {
      const u32 ms2 = *(LAS u32*)(sm + L_MS + (tt * 16 + i16) * 4);
      const float m2 = bflo(ms2), rs2 = rcp(bfhi(ms2));
#pragma unroll
      for (int t = 0; t < 3; t++) {
        xv[t] = *(LAS u32x2*)(xp + (t < 2 ? xo0 + 32 * t : xo2));
        u32 d0 = pk2(bflo(xv[t][0]) - m2, bfhi(xv[t][0]) - m2), d1 = pk2(bflo(xv[t][1]) - m2, bfhi(xv[t][1]) - m2);
        xnp[t] = u32x2{pk2(bflo(d0) * rs2, bfhi(d0) * rs2), pk2(bflo(d1) * rs2, bfhi(d1) * rs2)};
        asm volatile("" : "+v"(xnp[t]));
      }
    }
    const u32 obase = WS_REG + (rt * 16 + 8 * kh) * RS + R_OB + tt * 1280;
    u32 oa[5];
#pragma unroll
    for (int k = 0; k < 5; k++) { int C = 64 * k + l, pr = C / 80; oa[k] = obase + pr * (RS - 1280) + C * 16; asm volatile("" : "+v"(oa[k])); }
    f32x4 ao[5];
    int idx;
    {
      LAS char* wb = sm + (kh ? L_WO1 : L_WO0) + (4 * g + q4) * 160 + 8 * p4;
      LAS char* wA = wb + (kh ? 96 : 0); LAS char* wB = wb + (kh ? 0 : 96);
#define RO(F, j, r) F[r] = trd(((r) < 4 ? wA + 32 * ((r) >> 1) : (r) < 6 ? wb + 64 : wB + 32 * (((r) - 6) >> 1)) + (j) * 5120 + 2560 * ((r) & 1))
      SIG_WAIT2(F_WO, 2, F_IDX, 1);
      idx = __builtin_amdgcn_readfirstlane((int)*(volatile LAS u32*)(sm + L_SYN + F_IDX * 4) - 1);
      u32x2 wf[4][10];
#pragma unroll
      for (int j = 0; j < 3; j++)
#pragma unroll
        for (int r = 0; r < 10; r++) RO(wf[j], j, r);
      SB();
      SIG_WAIT(kh ? F_O1 : F_O, 1);
      STAMP(19);
      u32x4 of[10];
#pragma unroll
      for (int j = 0; j < 10; j++) of[j] = __builtin_amdgcn_raw_buffer_load_b128(wsr, oa[j % 5], (j / 5) * 4 * RS, 16);
#pragma unroll
      for (int t = 0; t < 5; t++) ao[t] = f32x4{0, 0, 0, 0};
      SB();
#pragma unroll
      for (int j = 0; j < 10; j++) {
#pragma unroll
        for (int k = 0; k < 5; k++) {
          if (j + 3 < 10) { RO(wf[(j + 3) & 3], j + 3, 2 * k); RO(wf[(j + 3) & 3], j + 3, 2 * k + 1); }
          const int t = j == 9 ? (k + 2) % 5 : k;
          ao[t] = mfma32(FR(wf[j & 3], t), of[j], ao[t]);
          SB();
        }
      }
    }
#pragma unroll
    for (int t = 0; t < 3; t++) *(LAS f32x4*)(sm + L_EXQ + (tw ^ 1) * 3072 + t * 1024 + l * 16) = ao[t < 2 ? 3 + t : 2];
    SIG_SET(F_EX + (tw ^ 1), 3);
    STAMP(20);
    SIG_WAIT(F_EX + tw, 3);
    STAMP(21);
    {
      if ((idx >> 4) == rt * 8 + tt) SIG_WAIT(F_YP, 2);
      if (idx == rt * 128 + tt * 16 + i16) {
#pragma unroll
        for (int t = 0; t < 3; t++) {
          f32x4 yy = *(LAS f32x4*)(sm + L_Y2 + kh * 160 + 2 * (t < 2 ? xo0 + 32 * t : xo2));
          xnp[t] = u32x2{pk2(bflo(xnp[t][0]) + yy[0], bfhi(xnp[t][0]) + yy[1]), pk2(bflo(xnp[t][1]) + yy[2], bfhi(xnp[t][1]) + yy[3])};
        }
      }
#pragma unroll
      for (int t = 0; t < 3; t++) {
        f32x4 af = ao[t] + *(LAS f32x4*)(sm + L_EXQ + tw * 3072 + t * 1024 + l * 16);
        u32 p0 = pk2(af[0], af[1]), p1 = pk2(af[2], af[3]);
        u32 q0 = pk2(bflo(xv[t][0]) + bflo(p0), bfhi(xv[t][0]) + bfhi(p0)), q1 = pk2(bflo(xv[t][1]) + bflo(p1), bfhi(xv[t][1]) + bfhi(p1));
        u32x2 res = {pk2(bflo(q0) + bflo(xnp[t][0]), bfhi(q0) + bfhi(xnp[t][0])), pk2(bflo(q1) + bflo(xnp[t][1]), bfhi(q1) + bfhi(xnp[t][1]))};
        if (t < 2 || mid) *(LAS u32x2*)(xp + (t < 2 ? xo0 + 32 * t : xo2)) = res;
      }
      __builtin_amdgcn_wave_barrier();
      LAS char* op = sm + L_V + tw * 1536 + l * 16;
      *(u32x4*)((char*)out + slo0) = *(LAS u32x4*)(op);
      if (l < 16) *(u32x4*)((char*)out + slo1) = *(LAS u32x4*)(op + 1024);
    }
    STAMP(22);
    }
  }
}

extern "C" void kernel_launch(void* const* d_in, const int* in_sizes, int n_in, void* d_out, int out_size,
                              void* d_ws, size_t ws_size, hipStream_t stream) {
  fused_kernel<<<256, 768, 0, stream>>>((const u16*)d_in[0], (const u16*)d_in[1], (const u16*)d_in[2], (const u16*)d_in[3],
                                        (const u16*)d_in[5], (const u16*)d_in[6], (const u16*)d_in[7], (const u16*)d_in[8],
                                        (u16*)d_out, (char*)d_ws);
}
```
